```python
import torch

def _softmax(x, axis=-1):
    return torch.softmax(x, dim=axis)
def _gelu(x):
    return torch.nn.functional.gelu(x, approximate="tanh")
def _silu(x):
    return torch.nn.functional.silu(x)
def _sigmoid(x):
    return torch.sigmoid(x)
def _layernorm(x, axis=-1, eps=1e-5):
    m = x.mean(dim=axis, keepdim=True)
    v = x.var(dim=axis, keepdim=True, unbiased=False)
    return (x - m) / torch.sqrt(v + eps)
def _scatter_add(tgt, idx, src, axis=0):
    return tgt.clone().index_add_(axis, idx, src)
def _segment_sum(src, seg, n):
    out = torch.zeros((n,) + src.shape[1:], dtype=src.dtype)
    return out.index_add_(0, seg, src)
def _pad(x, pw):
    flat = tuple(v for pair in reversed(pw) for v in pair)
    return torch.nn.functional.pad(x, flat)

kernel_name = 'concat_heads__compressed_kv_cla__sliding_local_dilated__prompt'

def setup_inputs():

    _w = (lambda a: (lambda t: t.to(torch.bfloat16) if t.is_floating_point() else t)(torch.as_tensor(a)))
    def _A_setup():
        B, H, Sq, Sk, D, M = 1, 32, 2048, 2048, 128, 640
        Dc = 4 * D
        return {
            "x": _w((lambda *s: torch.randn(*s, dtype=torch.bfloat16) * 0.2)(B, Sq, M)),
            "c_kv": _w((lambda *s: torch.randn(*s, dtype=torch.bfloat16) * 1.6)(B, Sk, Dc)),
            "w_q": _w((lambda *s: torch.randn(*s, dtype=torch.bfloat16) * 3.2)(M, H, D) * (M ** -0.5)),
            "w_uk": _w((lambda *s: torch.randn(*s, dtype=torch.bfloat16) * 3.2)(Dc, H, D) * (Dc ** -0.5)),
            "w_uv": _w((lambda *s: torch.randn(*s, dtype=torch.bfloat16) * 1.6)(Dc, H, D) * (Dc ** -0.5)),
            "w_o": _w((lambda *s: torch.randn(*s, dtype=torch.bfloat16) * 1.6)(H, D, M) * ((H * D) ** -0.5)),
        }
    _A_in = _A_setup()

    def _B_setup():
        B, S, D = 1, 2048, 640
        return {
            "x": _w((lambda *s: torch.randn(*s, dtype=torch.bfloat16) * 0.4)(B, S, D)),
            "wqkv": _w((lambda *s: torch.randn(*s, dtype=torch.bfloat16) * 0.4)(D, 3 * D)),
            "wo": _w((lambda *s: torch.randn(*s, dtype=torch.bfloat16) * 0.05)(D, D)),
        }
    _B_in = _B_setup()

    return {
        "A_x": _A_in["x"],
        "A_c_kv": _A_in["c_kv"],
        "A_w_q": _A_in["w_q"],
        "A_w_uk": _A_in["w_uk"],
        "A_w_uv": _A_in["w_uv"],
        "A_w_o": _A_in["w_o"],
        "B_x": _B_in["x"],
        "B_wqkv": _B_in["wqkv"],
        "B_wo": _B_in["wo"],
    }

def reference(A_x, A_c_kv, A_w_q, A_w_uk, A_w_uv, A_w_o, B_x, B_wqkv, B_wo):
    def _A_ref(x, c_kv, w_q, w_uk, w_uv, w_o):
        B, Sq, M = x.shape[0], x.shape[1], x.shape[2]
        Sk = c_kv.shape[1]
        H, D = w_q.shape[1], w_q.shape[2]
        q = torch.einsum("bqm,mhd->bhqd", x, w_q)
        k = torch.einsum("bkc,chd->bhkd", c_kv, w_uk)
        v = torch.einsum("bkc,chd->bhkd", c_kv, w_uv)
        scores = torch.einsum("bhqd,bhkd->bhqk", q, k) * (D ** -0.5)
        off = Sk - Sq
        qi = torch.arange(Sq)[:, None]
        kj = torch.arange(Sk)[None, :]
        cmask = kj <= (qi + off)
        scores = torch.where(cmask[None, None], scores, -1e9)
        p = _softmax(scores, axis=-1)
        ctx = torch.einsum("bhqk,bhkd->bhqd", p, v)
        return torch.einsum("bhqd,hdm->bqm", ctx, w_o)

    def _B_ref(x, wqkv, wo):
        B, S, D = x.shape
        H = 32; Dh = D // H
        W, dil = 8, 2
        qkv = (lambda x, *d: x.permute(*d))((x @ wqkv).reshape(B, S, 3, H, Dh), 2, 0, 3, 1, 4)
        q, k, v = qkv[0], qkv[1], qkv[2]
        scale = Dh ** -0.5
        pos = torch.arange(S)
        off = torch.arange(W) * dil
        idx = pos[:, None] - off[None, :]
        mask = idx >= 0
        idx_c = torch.where(mask, idx, 0).reshape(-1)
        kg = k[:, :, idx_c, :].reshape(B, H, S, W, Dh)
        vg = v[:, :, idx_c, :].reshape(B, H, S, W, Dh)
        logits = torch.einsum("bhsd,bhswd->bhsw", q, kg) * scale
        logits = torch.where(mask[None, None, :, :], logits, -1e9)
        attn = _softmax(logits, axis=-1)
        o = torch.einsum("bhsw,bhswd->bhsd", attn, vg)
        return (lambda x, *d: x.permute(*d))(o, 0, 2, 1, 3).reshape(B, S, D) @ wo

    a = _A_ref(A_x, A_c_kv, A_w_q, A_w_uk, A_w_uv, A_w_o)
    b = _B_ref(B_x, B_wqkv, B_wo)
    return torch.concatenate([a, b], axis=-1)
```

```cpp
#include <hip/hip_runtime.h>
typedef unsigned u32x4 __attribute__((ext_vector_type(4)));
typedef float f32x32 __attribute__((ext_vector_type(32)));
#define DI __device__ __forceinline__
constexpr unsigned OFF_K = 0, OFF_V = 16u << 20, OFF_CTX = 32u << 20, OFF_O = 48u << 20, OFF_P = 52u << 20, OFF_W = 72u << 20, OFF_F = 80u << 20;
constexpr unsigned F_KV = 4096, F_CTX = 8192, F_O = 12288, F_W = 20480, F_END = 24576, NOWHERE = 0x40000000u;
DI unsigned rfl(unsigned x) { return __builtin_amdgcn_readfirstlane(x); }
DI unsigned long long rfl64(unsigned long long x) { return rfl((unsigned)x) | ((unsigned long long)rfl((unsigned)(x >> 32)) << 32); }
DI unsigned lane_id() { unsigned l; asm volatile("v_mbcnt_lo_u32_b32 %0, -1, 0\n\tv_mbcnt_hi_u32_b32 %0, -1, %0" : "=v"(l)); return l; }
DI u32x4 srd(const void* base, unsigned off, unsigned n) {
  unsigned long long a = (unsigned long long)base + off;
  u32x4 d = {rfl((unsigned)a), rfl((unsigned)(a >> 32) & 0xffffu), rfl(n), 0x00020000u};
  return d;
}

typedef char* const __attribute__((address_space(4)))* KArg;
#define IDS const unsigned xcd = b & 7, li = b >> 3, h = xcd * 4 + (li >> 3), j = li & 7, rh = 128 * (15 - j) + 32 * w, rl = 128 * j + 32 * w, \
  rb = (xcd >> 1) * 4 + (li & 3), g = (xcd & 1) * 8 + (li >> 2), s = xcd & 3, rt = (xcd & 4) ? 15 - j : j, ct = li >> 3, tile = rt * 4 + ct;
__global__ __launch_bounds__(256, 1) void fused(const char* x, const char* ckv, const char* wq, const char* wuk, const char* wuv, const char* wo,
                                                const char* bx, const char* wqkv, const char* wob, char* ws, char* out) {
  asm volatile("s_getpc_b64 s[80:81]\n.Lkc0:\n\ts_mov_b32 s82, .Lkc1-.Lkc0\n\ts_mov_b32 s83, 0x20000\n\tbuffer_load_dword a127, %0, s[80:83], %1 offen"
               :: "v"(threadIdx.x * 128), "s"((blockIdx.x >> 3) * 32768) : "s80", "s81", "s82", "s83", "a127", "memory");
  unsigned w = rfl(threadIdx.x >> 6), b = blockIdx.x;
  KArg ka = (KArg)__builtin_amdgcn_kernarg_segment_ptr();
  unsigned long long tag;
  asm volatile("s_load_dwordx2 %0, %6, 0x8\n\ts_load_dwordx2 %1, %6, 0x18\n\ts_load_dwordx2 %2, %6, 0x20\n\ts_load_dwordx2 %3, %6, 0x48\n\ts_load_dwordx2 %4, %6, 0x0\n\ts_load_dwordx2 %5, %6, 0x10\n\ts_waitcnt lgkmcnt(0)"
               : "=&s"(ckv), "=&s"(wuk), "=&s"(wuv), "=&s"(ws), "=&s"(x), "=&s"(wq) : "s"(ka));
  f32x32 O0, O1, O2, O3;
  unsigned v[32];
  unsigned l = threadIdx.x & 63, s52 = w * 8192, s53 = w * 4096;
  u32x4 d24, d28, d32;
  {
  IDS
  d24 = srd(ckv, j * 262144, 262144), d28 = srd(wuk, h * 256, 512 * 8192 - h * 256), d32 = srd(wuv, h * 256, 512 * 8192 - h * 256);
  for (int i = 0; i < 4; i++) v[14 + i] = (16 * w + 4 * i + (l >> 4)) * 8192 + (((l & 15) ^ ((l >> 4) << 2) ^ (i >> 1)) << 4) - 1024 * i;
  for (int i = 0; i < 8; i++) { const unsigned row = 8 * i + (l >> 3); v[6 + i] = (64 * w + row) * 1024 + (((l & 7) ^ (((row >> 1) & 3) | ((row >> 2) & 4))) << 4) - 1024 * (i & 3); }
  asm volatile(
  "s_nop 4\n"
  "s_mov_b32 s55, 0x0\n"
  "s_mov_b32 s56, 0x0\n"
  "s_add_u32 m0, s53, 0x18000\n"
  "s_nop 0\n"
  "buffer_load_dwordx4 v14, s[28:31], s56 offen offset:0 lds\n"
  "buffer_load_dwordx4 v15, s[28:31], s56 offen offset:1024 lds\n"
  "buffer_load_dwordx4 v16, s[28:31], s56 offen offset:2048 lds\n"
  "buffer_load_dwordx4 v17, s[28:31], s56 offen offset:3072 lds\n"
  "s_add_u32 m0, s52, 0x10000\n"
  "s_nop 0\n"
  "buffer_load_dwordx4 v6, s[24:27], s55 offen offset:0 lds\n"
  "buffer_load_dwordx4 v7, s[24:27], s55 offen offset:1024 lds\n"
  "buffer_load_dwordx4 v8, s[24:27], s55 offen offset:2048 lds\n"
  "buffer_load_dwordx4 v9, s[24:27], s55 offen offset:3072 lds\n"
  "s_add_u32 m0, s53, 0x1c000\n"
  "s_nop 0\n"
  "buffer_load_dwordx4 v14, s[32:35], s56 offen offset:0 lds\n"
  "buffer_load_dwordx4 v15, s[32:35], s56 offen offset:1024 lds\n"
  "buffer_load_dwordx4 v16, s[32:35], s56 offen offset:2048 lds\n"
  "buffer_load_dwordx4 v17, s[32:35], s56 offen offset:3072 lds\n"
  "s_add_u32 m0, s52, 0x11000\n"
  "s_nop 0\n"
  "buffer_load_dwordx4 v10, s[24:27], s55 offen offset:0 lds\n"
  "buffer_load_dwordx4 v11, s[24:27], s55 offen offset:1024 lds\n"
  "buffer_load_dwordx4 v12, s[24:27], s55 offen offset:2048 lds\n"
  "buffer_load_dwordx4 v13, s[24:27], s55 offen offset:3072 lds\n"
  "s_mov_b32 s55, 0x80\n"
  "s_mov_b32 s56, 0x80000\n"
  "s_add_u32 m0, s53, 0x8000\n"
  "s_nop 0\n"
  "buffer_load_dwordx4 v14, s[28:31], s56 offen offset:0 lds\n"
  "buffer_load_dwordx4 v15, s[28:31], s56 offen offset:1024 lds\n"
  "buffer_load_dwordx4 v16, s[28:31], s56 offen offset:2048 lds\n"
  "buffer_load_dwordx4 v17, s[28:31], s56 offen offset:3072 lds\n"
  "s_add_u32 m0, s52, 0x0\n"
  "s_nop 0\n"
  "buffer_load_dwordx4 v6, s[24:27], s55 offen offset:0 lds\n"
  "buffer_load_dwordx4 v7, s[24:27], s55 offen offset:1024 lds\n"
  "buffer_load_dwordx4 v8, s[24:27], s55 offen offset:2048 lds\n"
  "buffer_load_dwordx4 v9, s[24:27], s55 offen offset:3072 lds\n"
  "s_add_u32 m0, s53, 0xc000\n"
  "s_nop 0\n"
  "buffer_load_dwordx4 v14, s[32:35], s56 offen offset:0 lds\n"
  "buffer_load_dwordx4 v15, s[32:35], s56 offen offset:1024 lds\n"
  "buffer_load_dwordx4 v16, s[32:35], s56 offen offset:2048 lds\n"
  "buffer_load_dwordx4 v17, s[32:35], s56 offen offset:3072 lds\n"
  "s_add_u32 m0, s52, 0x1000\n"
  "s_nop 0\n"
  "buffer_load_dwordx4 v10, s[24:27], s55 offen offset:0 lds\n"
  "buffer_load_dwordx4 v11, s[24:27], s55 offen offset:1024 lds\n"
  "buffer_load_dwordx4 v12, s[24:27], s55 offen offset:2048 lds\n"
  "buffer_load_dwordx4 v13, s[24:27], s55 offen offset:3072 lds\n"
  : "+{v6}"(v[6]), "+{v7}"(v[7]), "+{v8}"(v[8]), "+{v9}"(v[9]), "+{v10}"(v[10]), "+{v11}"(v[11]), "+{v12}"(v[12]), "+{v13}"(v[13]), "+{v14}"(v[14]), "+{v15}"(v[15]), "+{v16}"(v[16]), "+{v17}"(v[17]), "+v"(l)
  : "{s52}"(s52), "{s53}"(s53), "{s[24:27]}"(d24), "{s[28:31]}"(d28), "{s[32:35]}"(d32)
  : "s55", "s56", "scc", "memory");
  }
  asm volatile("" : "+s"(w), "+s"(b));
  {
  IDS
  l &= 63;
  const unsigned g4 = l >> 4, mr = 2 * (l & 8) + (l & 7), kh = w >> 1, dh = w & 1, nh = 32 - 2 * j, nl = 2 * j + 2;
  const unsigned r = l & 31, hf = l >> 5, q = (l >> 2) & 3, p = l & 3, g1 = (l >> 4) & 1;
  const unsigned a0 = r * 128 + ((hf ^ ((r >> 1) & 7)) << 4), w0 = (8 * hf + q) * 256 + g1 * 32 + p * 8;
  for (int i = 0; i < 4; i++) { v[2 + i] = 98304 + (8 * g4 + q) * 256 + (((2 * dh + (i >> 1)) ^ q) << 6) + 32 * (p >> 1) + 8 * (p & 1) + 16 * ((i ^ g4) & 1);
    O3[i] = __uint_as_float(114688 + w * 8192 + (a0 ^ (i * 32))); O3[4 + i] = __uint_as_float(114688 + 32768 + w0 + ((i ^ q) << 6)); }
  O3[8] = __uint_as_float(rh + r - 4 * hf); O3[9] = __uint_as_float(rl + r - 4 * hf);
  for (int i = 0; i < 2; i++) { v[i] = 65536 + kh * 16384 + mr * 128 + (((4 * i + g4) ^ (((l >> 1) & 3) | ((l >> 1) & 4))) << 4);
    O3[10 + i] = __uint_as_float(hf * (i ? 4096 : 1024) + g1 * 512 + mr * 16); }
  for (int i = 0; i < 8; i++) { const unsigned row = 8 * i + (l >> 3), sw = (((l & 7) ^ ((row >> 1) & 7)) << 4) - 1024 * (i & 3);
    v[18 + i] = ((i < 4 ? rh : rl) + (row & 31)) * 1280 + sw; }
  for (int i = 0; i < 4; i++) v[26 + i] = v[14 + i] ^ ((i >> 1) << 4);
  v[30] = l * 16;
  for (int i = 0; i < 3; i++) { const unsigned k = 40 * rb + 10 * w + (i == 2 ? 6 : 4 * i) + (l >> 4), c = min((l & 15) ^ ((k & 3) << 2), 14u), sg = (c * 52) >> 8;
    O3[24 + i] = __uint_as_float(k * 3840 + sg * 1200 + 80 * g + c * 16); }
  O3[27] = __uint_as_float((l >> 1) * 8192 + (l & 1) * 128); O3[29] = __uint_as_float((l >> 2) * 1024 + (l & 3) * 128); O3[28] = __uint_as_float(min(l >> 1, 19u) * 8192 + (l & 1) * 128);
  unsigned s90 = OFF_W + g * 163840 + ((li & 3) * 4 + w) * 8192, s91 = max(int(128 * rb - 32) * 1280 + int((li >> 2) * 4 + w) * 6400, 0);
  unsigned s95 = OFF_P + tile * 147456 + s * 36864 + w * 9216;
  unsigned s21 = OFF_W + g * 163840 + (40 * rb + 10 * w) * 256, s22 = w == 0 ? F_W + (g * 16 + rb) * 8 : NOWHERE, s23 = F_W + g * 128;
  u32x4 d36 = srd(ws, 0, OFF_F), d40 = srd(x, 0, 2048 * 1280), d44 = srd(wq, h * 256, 640 * 8192 - h * 256), d48 = srd(ws, OFF_F, F_END);
  unsigned s58 = OFF_K + h * 524288 + (4 * j + 2 * kh) * 16384 + dh * 4096;
  unsigned s62 = (nh + 2) * 16384 + s53, s63 = (nl + 4) * 16384, s64 = (nh + 4) * 16384;
  unsigned s72 = w == 0 ? F_KV + (h * 8 + j) * 8 : NOWHERE, s73 = F_KV + h * 64, s74 = OFF_K + h * 524288;
  unsigned s75 = ((h & 3) * 4 + w) * 16384, s76 = (64 * j + 32 * kh) * 8192, s77 = (80 * j + 20 * w) * 8192;
  unsigned s78 = ((h & 2) ? j : 15 - j) * 163840 + ((h & 1) * 4 + w) * 20480, s79 = (b * 4 + w) * 4096;
  unsigned long long p70, p14; unsigned long long p88 = (unsigned long long)ka; unsigned s20 = OFF_CTX + (rl >> 5) * 262144 + h * 8192;
  asm volatile(
  "s_load_dwordx2 s[70:71], s[48:49], 0x0\n"
  "s_load_dwordx8 s[12:19], s[88:89], 0x28\n"
  "s_mov_b32 s86, 2457600\n"
  "s_mov_b32 s87, 0x20000\n"
  "v_lshlrev_b32 v31, 3, v30\n"
  "s_mov_b32 s83, 0x20000\n"
  "s_add_u32 s54, s53, 0x10000\n"
  "s_mov_b32 s60, s54\n"
  "s_mov_b32 s61, 0x4000\n"
  "s_mov_b32 s67, 0x53800000\n"
  "s_mov_b32 s68, 0x3e0293ee\n"
  "s_mov_b32 s69, s68\n"
  "s_sub_u32 s65, s63, 0x8000\n"
  "s_sub_u32 s66, s62, 0x4000\n"
  "s_add_u32 s59, s58, 0x1000000\n"
  "s_and_b32 s57, s53, 0x1000\n"
  "s_lshr_b32 s57, s57, 1\n"
  "s_sub_u32 s59, s59, s57\n"
  "v_accvgpr_read_b32 v31, a125\n"
  "buffer_load_dword a127, v31, s[24:27], s75 offen offset:256\n"
  "v_accvgpr_read_b32 v31, a123\n"
  "s_bitcmp1_b32 s53, 12\n"
  "s_cselect_b64 s[80:81], s[32:33], s[28:29]\n"
  "s_mov_b32 s82, s30\n"
  "s_sub_u32 s57, s76, 0x100000\n"
  "s_cmp_lt_u32 s57, 0x180000\n"
  "s_cselect_b32 s57, s76, 0x40000000\n"
  "buffer_load_dword a127, v31, s[80:83], s57 offen\n"
  "s_waitcnt vmcnt(18) lgkmcnt(0)\n"
  "s_mov_b64 s[84:85], s[16:17]\n"
  "s_add_u32 s70, s70, 1\n"
  "s_addc_u32 s71, s71, 1\n"
  "s_barrier\n"
  "ds_read_b128 a[128:131], v0 offset:0\n"
  "ds_read_b64_tr_b16 a[160:161], v2 offset:0\n"
  "ds_read_b64_tr_b16 a[162:163], v2 offset:1024\n"
  "ds_read_b64_tr_b16 a[164:165], v3 offset:0\n"
  "ds_read_b64_tr_b16 a[166:167], v3 offset:1024\n"
  "ds_read_b64_tr_b16 a[168:169], v4 offset:0\n"
  "ds_read_b64_tr_b16 a[170:171], v4 offset:1024\n"
  "ds_read_b64_tr_b16 a[172:173], v5 offset:0\n"
  "ds_read_b64_tr_b16 a[174:175], v5 offset:1024\n"
  "ds_read_b64_tr_b16 a[176:177], v2 offset:16384\n"
  "ds_read_b64_tr_b16 a[178:179], v2 offset:17408\n"
  "ds_read_b64_tr_b16 a[180:181], v3 offset:16384\n"
  "ds_read_b64_tr_b16 a[182:183], v3 offset:17408\n"
  "ds_read_b64_tr_b16 a[184:185], v4 offset:16384\n"
  "ds_read_b64_tr_b16 a[186:187], v4 offset:17408\n"
  "ds_read_b64_tr_b16 a[188:189], v5 offset:16384\n"
  "ds_read_b64_tr_b16 a[190:191], v5 offset:17408\n"
  "ds_read_b128 a[132:135], v0 offset:1024\n"
  "ds_read_b128 a[136:139], v0 offset:4096\n"
  "ds_read_b128 a[140:143], v0 offset:5120\n"
  "ds_read_b128 a[144:147], v0 offset:8192\n"
  "ds_read_b128 a[148:151], v0 offset:9216\n"
  "ds_read_b128 a[152:155], v0 offset:12288\n"
  "ds_read_b128 a[156:159], v0 offset:13312\n"
  "s_waitcnt lgkmcnt(15)\n"
  "v_mfma_f32_16x16x32_bf16 v[32:35], a[160:163], a[128:131], 0\n"
  "ds_read_b128 a[192:195], v1 offset:0\n"
  "v_mfma_f32_16x16x32_bf16 v[36:39], a[164:167], a[128:131], 0\n"
  "v_mfma_f32_16x16x32_bf16 v[48:51], a[168:171], a[128:131], 0\n"
  "ds_read_b64_tr_b16 a[224:225], v2 offset:8192\n"
  "v_mfma_f32_16x16x32_bf16 v[52:55], a[172:175], a[128:131], 0\n"
  "s_waitcnt lgkmcnt(15)\n"
  "v_mfma_f32_16x16x32_bf16 v[64:67], a[128:131], a[176:179], 0\n"
  "ds_read_b64_tr_b16 a[226:227], v2 offset:9216\n"
  "s_waitcnt lgkmcnt(14)\n"
  "v_mfma_f32_16x16x32_bf16 v[72:75], a[128:131], a[180:183], 0\n"
  "s_waitcnt lgkmcnt(12)\n"
  "v_mfma_f32_16x16x32_bf16 v[80:83], a[128:131], a[184:187], 0\n"
  "ds_read_b64_tr_b16 a[228:229], v3 offset:8192\n"
  "s_waitcnt lgkmcnt(11)\n"
  "v_mfma_f32_16x16x32_bf16 v[88:91], a[128:131], a[188:191], 0\n"
  "s_waitcnt lgkmcnt(10)\n"
  "v_mfma_f32_16x16x32_bf16 v[40:43], a[160:163], a[132:135], 0\n"
  "ds_read_b64_tr_b16 a[230:231], v3 offset:9216\n"
  "v_mfma_f32_16x16x32_bf16 v[44:47], a[164:167], a[132:135], 0\n"
  "v_mfma_f32_16x16x32_bf16 v[56:59], a[168:171], a[132:135], 0\n"
  "ds_read_b64_tr_b16 a[232:233], v4 offset:8192\n"
  "v_mfma_f32_16x16x32_bf16 v[60:63], a[172:175], a[132:135], 0\n"
  "v_mfma_f32_16x16x32_bf16 v[68:71], a[132:135], a[176:179], 0\n"
  "ds_read_b64_tr_b16 a[234:235], v4 offset:9216\n"
  "v_mfma_f32_16x16x32_bf16 v[76:79], a[132:135], a[180:183], 0\n"
  "v_mfma_f32_16x16x32_bf16 v[84:87], a[132:135], a[184:187], 0\n"
  "ds_read_b64_tr_b16 a[236:237], v5 offset:8192\n"
  "v_mfma_f32_16x16x32_bf16 v[92:95], a[132:135], a[188:191], 0\n"
  "s_waitcnt lgkmcnt(13)\n"
  "v_mfma_f32_16x16x32_bf16 v[96:99], a[160:163], a[136:139], 0\n"
  "ds_read_b64_tr_b16 a[238:239], v5 offset:9216\n"
  "v_mfma_f32_16x16x32_bf16 v[100:103], a[164:167], a[136:139], 0\n"
  "v_mfma_f32_16x16x32_bf16 v[112:115], a[168:171], a[136:139], 0\n"
  "ds_read_b64_tr_b16 a[240:241], v2 offset:24576\n"
  "v_mfma_f32_16x16x32_bf16 v[116:119], a[172:175], a[136:139], 0\n"
  "v_mfma_f32_16x16x32_bf16 v[128:131], a[136:139], a[176:179], 0\n"
  "ds_read_b64_tr_b16 a[242:243], v2 offset:25600\n"
  "v_mfma_f32_16x16x32_bf16 v[136:139], a[136:139], a[180:183], 0\n"
  "v_mfma_f32_16x16x32_bf16 v[144:147], a[136:139], a[184:187], 0\n"
  "ds_read_b64_tr_b16 a[244:245], v3 offset:24576\n"
  "v_mfma_f32_16x16x32_bf16 v[152:155], a[136:139], a[188:191], 0\n"
  "s_waitcnt lgkmcnt(15)\n"
  "v_mfma_f32_16x16x32_bf16 v[104:107], a[160:163], a[140:143], 0\n"
  "ds_read_b64_tr_b16 a[246:247], v3 offset:25600\n"
  "v_mfma_f32_16x16x32_bf16 v[108:111], a[164:167], a[140:143], 0\n"
  "v_mfma_f32_16x16x32_bf16 v[120:123], a[168:171], a[140:143], 0\n"
  "ds_read_b64_tr_b16 a[248:249], v4 offset:24576\n"
  "v_mfma_f32_16x16x32_bf16 v[124:127], a[172:175], a[140:143], 0\n"
  "v_mfma_f32_16x16x32_bf16 v[132:135], a[140:143], a[176:179], 0\n"
  "ds_read_b64_tr_b16 a[250:251], v4 offset:25600\n"
  "v_mfma_f32_16x16x32_bf16 v[140:143], a[140:143], a[180:183], 0\n"
  "v_mfma_f32_16x16x32_bf16 v[148:151], a[140:143], a[184:187], 0\n"
  "ds_read_b64_tr_b16 a[252:253], v5 offset:24576\n"
  "v_mfma_f32_16x16x32_bf16 v[156:159], a[140:143], a[188:191], 0\n"
  "v_mfma_f32_16x16x32_bf16 v[160:163], a[160:163], a[144:147], 0\n"
  "ds_read_b64_tr_b16 a[254:255], v5 offset:25600\n"
  "v_mfma_f32_16x16x32_bf16 v[164:167], a[164:167], a[144:147], 0\n"
  "v_mfma_f32_16x16x32_bf16 v[176:179], a[168:171], a[144:147], 0\n"
  "ds_read_b128 a[196:199], v1 offset:1024\n"
  "v_mfma_f32_16x16x32_bf16 v[180:183], a[172:175], a[144:147], 0\n"
  "v_mfma_f32_16x16x32_bf16 v[192:195], a[144:147], a[176:179], 0\n"
  "ds_read_b128 a[200:203], v1 offset:4096\n"
  "v_mfma_f32_16x16x32_bf16 v[200:203], a[144:147], a[180:183], 0\n"
  "v_mfma_f32_16x16x32_bf16 v[208:211], a[144:147], a[184:187], 0\n"
  "ds_read_b128 a[204:207], v1 offset:5120\n"
  "v_mfma_f32_16x16x32_bf16 v[216:219], a[144:147], a[188:191], 0\n"
  "s_waitcnt lgkmcnt(15)\n"
  "v_mfma_f32_16x16x32_bf16 v[168:171], a[160:163], a[148:151], 0\n"
  "ds_read_b128 a[208:211], v1 offset:8192\n"
  "v_mfma_f32_16x16x32_bf16 v[172:175], a[164:167], a[148:151], 0\n"
  "s_cmp_ge_u32 s76, 0x280000\n"
  "s_cselect_b32 s57, s76, 0x40000000\n"
  "buffer_load_dword a127, v31, s[80:83], s57 offen\n"
  "v_mfma_f32_16x16x32_bf16 v[184:187], a[168:171], a[148:151], 0\n"
  "ds_read_b128 a[212:215], v1 offset:9216\n"
  "v_mfma_f32_16x16x32_bf16 v[188:191], a[172:175], a[148:151], 0\n"
  "v_mfma_f32_16x16x32_bf16 v[196:199], a[148:151], a[176:179], 0\n"
  "ds_read_b128 a[216:219], v1 offset:12288\n"
  "v_mfma_f32_16x16x32_bf16 v[204:207], a[148:151], a[180:183], 0\n"
  "v_accvgpr_read_b32 v31, a125\n"
  "v_mfma_f32_16x16x32_bf16 v[212:215], a[148:151], a[184:187], 0\n"
  "ds_read_b128 a[220:223], v1 offset:13312\n"
  "v_mfma_f32_16x16x32_bf16 v[220:223], a[148:151], a[188:191], 0\n"
  "v_mfma_f32_16x16x32_bf16 v[224:227], a[160:163], a[152:155], 0\n"
  "v_mfma_f32_16x16x32_bf16 v[228:231], a[164:167], a[152:155], 0\n"
  "v_or_b32 v31, 0x100, v31\n"
  "v_mfma_f32_16x16x32_bf16 v[240:243], a[168:171], a[152:155], 0\n"
  "v_mfma_f32_16x16x32_bf16 v[244:247], a[172:175], a[152:155], 0\n"
  "v_mfma_f32_16x16x32_bf16 a[0:3], a[152:155], a[176:179], 0\n"
  "v_mfma_f32_16x16x32_bf16 a[8:11], a[152:155], a[180:183], 0\n"
  "buffer_load_dword a127, v31, s[24:27], s75 offen offset:512\n"
  "v_mfma_f32_16x16x32_bf16 a[16:19], a[152:155], a[184:187], 0\n"
  "v_mfma_f32_16x16x32_bf16 a[24:27], a[152:155], a[188:191], 0\n"
  "v_mfma_f32_16x16x32_bf16 v[232:235], a[160:163], a[156:159], 0\n"
  "v_mfma_f32_16x16x32_bf16 v[236:239], a[164:167], a[156:159], 0\n"
  "v_mfma_f32_16x16x32_bf16 v[248:251], a[168:171], a[156:159], 0\n"
  "v_mfma_f32_16x16x32_bf16 v[252:255], a[172:175], a[156:159], 0\n"
  "v_mfma_f32_16x16x32_bf16 a[4:7], a[156:159], a[176:179], 0\n"
  "v_mfma_f32_16x16x32_bf16 a[12:15], a[156:159], a[180:183], 0\n"
  "v_mfma_f32_16x16x32_bf16 a[20:23], a[156:159], a[184:187], 0\n"
  "v_mfma_f32_16x16x32_bf16 a[28:31], a[156:159], a[188:191], 0\n"
  "s_waitcnt lgkmcnt(0)\n"
  "v_mfma_f32_16x16x32_bf16 v[32:35], a[224:227], a[192:195], v[32:35]\n"
  "v_xor_b32 v0, 0x10000, v0\n"
  "v_xor_b32 v2, 0x10000, v2\n"
  "v_mfma_f32_16x16x32_bf16 v[36:39], a[228:231], a[192:195], v[36:39]\n"
  "v_xor_b32 v3, 0x10000, v3\n"
  "v_xor_b32 v4, 0x10000, v4\n"
  "v_mfma_f32_16x16x32_bf16 v[48:51], a[232:235], a[192:195], v[48:51]\n"
  "v_xor_b32 v5, 0x10000, v5\n"
  "v_xor_b32 v1, 0x10000, v1\n"
  "v_mfma_f32_16x16x32_bf16 v[52:55], a[236:239], a[192:195], v[52:55]\n"
  "s_waitcnt vmcnt(4) lgkmcnt(0)\n"
  "s_barrier\n"
  "v_mfma_f32_16x16x32_bf16 v[64:67], a[192:195], a[240:243], v[64:67]\n"
  "s_mov_b32 s55, 0x100\n"
  "s_mov_b32 s56, 0x100000\n"
  "ds_read_b128 a[128:131], v0 offset:0\n"
  "v_mfma_f32_16x16x32_bf16 v[72:75], a[192:195], a[244:247], v[72:75]\n"
  "s_add_u32 m0, s53, 0x18000\n"
  "v_mfma_f32_16x16x32_bf16 v[80:83], a[192:195], a[248:251], v[80:83]\n"
  "ds_read_b64_tr_b16 a[160:161], v2 offset:0\n"
  "v_mfma_f32_16x16x32_bf16 v[88:91], a[192:195], a[252:255], v[88:91]\n"
  "buffer_load_dwordx4 v14, s[28:31], s56 offen offset:0 lds\n"
  "v_mfma_f32_16x16x32_bf16 v[40:43], a[224:227], a[196:199], v[40:43]\n"
  "ds_read_b64_tr_b16 a[162:163], v2 offset:1024\n"
  "v_mfma_f32_16x16x32_bf16 v[44:47], a[228:231], a[196:199], v[44:47]\n"
  "v_mfma_f32_16x16x32_bf16 v[56:59], a[232:235], a[196:199], v[56:59]\n"
  "ds_read_b64_tr_b16 a[164:165], v3 offset:0\n"
  "v_mfma_f32_16x16x32_bf16 v[60:63], a[236:239], a[196:199], v[60:63]\n"
  "v_mfma_f32_16x16x32_bf16 v[68:71], a[196:199], a[240:243], v[68:71]\n"
  "buffer_load_dwordx4 v15, s[28:31], s56 offen offset:1024 lds\n"
  "ds_read_b64_tr_b16 a[166:167], v3 offset:1024\n"
  "v_mfma_f32_16x16x32_bf16 v[76:79], a[196:199], a[244:247], v[76:79]\n"
  "v_mfma_f32_16x16x32_bf16 v[84:87], a[196:199], a[248:251], v[84:87]\n"
  "ds_read_b64_tr_b16 a[168:169], v4 offset:0\n"
  "v_mfma_f32_16x16x32_bf16 v[92:95], a[196:199], a[252:255], v[92:95]\n"
  "v_mfma_f32_16x16x32_bf16 v[96:99], a[224:227], a[200:203], v[96:99]\n"
  "ds_read_b64_tr_b16 a[170:171], v4 offset:1024\n"
  "v_mfma_f32_16x16x32_bf16 v[100:103], a[228:231], a[200:203], v[100:103]\n"
  "buffer_load_dwordx4 v16, s[28:31], s56 offen offset:2048 lds\n"
  "v_mfma_f32_16x16x32_bf16 v[112:115], a[232:235], a[200:203], v[112:115]\n"
  "ds_read_b64_tr_b16 a[172:173], v5 offset:0\n"
  "v_mfma_f32_16x16x32_bf16 v[116:119], a[236:239], a[200:203], v[116:119]\n"
  "v_mfma_f32_16x16x32_bf16 v[128:131], a[200:203], a[240:243], v[128:131]\n"
  "ds_read_b64_tr_b16 a[174:175], v5 offset:1024\n"
  "v_mfma_f32_16x16x32_bf16 v[136:139], a[200:203], a[244:247], v[136:139]\n"
  "v_mfma_f32_16x16x32_bf16 v[144:147], a[200:203], a[248:251], v[144:147]\n"
  "buffer_load_dwordx4 v17, s[28:31], s56 offen offset:3072 lds\n"
  "ds_read_b64_tr_b16 a[176:177], v2 offset:16384\n"
  "v_mfma_f32_16x16x32_bf16 v[152:155], a[200:203], a[252:255], v[152:155]\n"
  "v_mfma_f32_16x16x32_bf16 v[104:107], a[224:227], a[204:207], v[104:107]\n"
  "ds_read_b64_tr_b16 a[178:179], v2 offset:17408\n"
  "v_mfma_f32_16x16x32_bf16 v[108:111], a[228:231], a[204:207], v[108:111]\n"
  "s_add_u32 m0, s52, 0x10000\n"
  "v_mfma_f32_16x16x32_bf16 v[120:123], a[232:235], a[204:207], v[120:123]\n"
  "ds_read_b64_tr_b16 a[180:181], v3 offset:16384\n"
  "v_mfma_f32_16x16x32_bf16 v[124:127], a[236:239], a[204:207], v[124:127]\n"
  "buffer_load_dwordx4 v6, s[24:27], s55 offen offset:0 lds\n"
  "v_mfma_f32_16x16x32_bf16 v[132:135], a[204:207], a[240:243], v[132:135]\n"
  "ds_read_b64_tr_b16 a[182:183], v3 offset:17408\n"
  "v_mfma_f32_16x16x32_bf16 v[140:143], a[204:207], a[244:247], v[140:143]\n"
  "v_mfma_f32_16x16x32_bf16 v[148:151], a[204:207], a[248:251], v[148:151]\n"
  "ds_read_b64_tr_b16 a[184:185], v4 offset:16384\n"
  "v_mfma_f32_16x16x32_bf16 v[156:159], a[204:207], a[252:255], v[156:159]\n"
  "v_mfma_f32_16x16x32_bf16 v[160:163], a[224:227], a[208:211], v[160:163]\n"
  "buffer_load_dwordx4 v7, s[24:27], s55 offen offset:1024 lds\n"
  "ds_read_b64_tr_b16 a[186:187], v4 offset:17408\n"
  "v_mfma_f32_16x16x32_bf16 v[164:167], a[228:231], a[208:211], v[164:167]\n"
  "v_mfma_f32_16x16x32_bf16 v[176:179], a[232:235], a[208:211], v[176:179]\n"
  "ds_read_b64_tr_b16 a[188:189], v5 offset:16384\n"
  "v_mfma_f32_16x16x32_bf16 v[180:183], a[236:239], a[208:211], v[180:183]\n"
  "v_mfma_f32_16x16x32_bf16 v[192:195], a[208:211], a[240:243], v[192:195]\n"
  "ds_read_b64_tr_b16 a[190:191], v5 offset:17408\n"
  "v_mfma_f32_16x16x32_bf16 v[200:203], a[208:211], a[244:247], v[200:203]\n"
  "buffer_load_dwordx4 v8, s[24:27], s55 offen offset:2048 lds\n"
  "v_mfma_f32_16x16x32_bf16 v[208:211], a[208:211], a[248:251], v[208:211]\n"
  "ds_read_b128 a[132:135], v0 offset:1024\n"
  "v_mfma_f32_16x16x32_bf16 v[216:219], a[208:211], a[252:255], v[216:219]\n"
  "v_mfma_f32_16x16x32_bf16 v[168:171], a[224:227], a[212:215], v[168:171]\n"
  "ds_read_b128 a[136:139], v0 offset:4096\n"
  "v_mfma_f32_16x16x32_bf16 v[172:175], a[228:231], a[212:215], v[172:175]\n"
  "v_mfma_f32_16x16x32_bf16 v[184:187], a[232:235], a[212:215], v[184:187]\n"
  "buffer_load_dwordx4 v9, s[24:27], s55 offen offset:3072 lds\n"
  "ds_read_b128 a[140:143], v0 offset:5120\n"
  "v_mfma_f32_16x16x32_bf16 v[188:191], a[236:239], a[212:215], v[188:191]\n"
  "v_mfma_f32_16x16x32_bf16 v[196:199], a[212:215], a[240:243], v[196:199]\n"
  "ds_read_b128 a[144:147], v0 offset:8192\n"
  "v_mfma_f32_16x16x32_bf16 v[204:207], a[212:215], a[244:247], v[204:207]\n"
  "s_add_u32 m0, s53, 0x1c000\n"
  "v_mfma_f32_16x16x32_bf16 v[212:215], a[212:215], a[248:251], v[212:215]\n"
  "ds_read_b128 a[148:151], v0 offset:9216\n"
  "v_mfma_f32_16x16x32_bf16 v[220:223], a[212:215], a[252:255], v[220:223]\n"
  "buffer_load_dwordx4 v14, s[32:35], s56 offen offset:0 lds\n"
  "v_mfma_f32_16x16x32_bf16 v[224:227], a[224:227], a[216:219], v[224:227]\n"
  "ds_read_b128 a[152:155], v0 offset:12288\n"
  "v_mfma_f32_16x16x32_bf16 v[228:231], a[228:231], a[216:219], v[228:231]\n"
  "v_mfma_f32_16x16x32_bf16 v[240:243], a[232:235], a[216:219], v[240:243]\n"
  "ds_read_b128 a[156:159], v0 offset:13312\n"
  "v_mfma_f32_16x16x32_bf16 v[244:247], a[236:239], a[216:219], v[244:247]\n"
  "v_mfma_f32_16x16x32_bf16 a[0:3], a[216:219], a[240:243], a[0:3]\n"
  "buffer_load_dwordx4 v15, s[32:35], s56 offen offset:1024 lds\n"
  "v_mfma_f32_16x16x32_bf16 a[8:11], a[216:219], a[244:247], a[8:11]\n"
  "v_mfma_f32_16x16x32_bf16 a[16:19], a[216:219], a[248:251], a[16:19]\n"
  "v_mfma_f32_16x16x32_bf16 a[24:27], a[216:219], a[252:255], a[24:27]\n"
  "v_mfma_f32_16x16x32_bf16 v[232:235], a[224:227], a[220:223], v[232:235]\n"
  "v_mfma_f32_16x16x32_bf16 v[236:239], a[228:231], a[220:223], v[236:239]\n"
  "buffer_load_dwordx4 v16, s[32:35], s56 offen offset:2048 lds\n"
  "v_mfma_f32_16x16x32_bf16 v[248:251], a[232:235], a[220:223], v[248:251]\n"
  "v_mfma_f32_16x16x32_bf16 v[252:255], a[236:239], a[220:223], v[252:255]\n"
  "v_mfma_f32_16x16x32_bf16 a[4:7], a[220:223], a[240:243], a[4:7]\n"
  "v_mfma_f32_16x16x32_bf16 a[12:15], a[220:223], a[244:247], a[12:15]\n"
  "v_mfma_f32_16x16x32_bf16 a[20:23], a[220:223], a[248:251], a[20:23]\n"
  "buffer_load_dwordx4 v17, s[32:35], s56 offen offset:3072 lds\n"
  "v_mfma_f32_16x16x32_bf16 a[28:31], a[220:223], a[252:255], a[28:31]\n"
  "s_waitcnt lgkmcnt(0)\n"
  "v_mfma_f32_16x16x32_bf16 v[32:35], a[160:163], a[128:131], v[32:35]\n"
  "ds_read_b128 a[192:195], v1 offset:0\n"
  "v_mfma_f32_16x16x32_bf16 v[36:39], a[164:167], a[128:131], v[36:39]\n"
  "s_add_u32 m0, s52, 0x11000\n"
  "v_mfma_f32_16x16x32_bf16 v[48:51], a[168:171], a[128:131], v[48:51]\n"
  "ds_read_b64_tr_b16 a[224:225], v2 offset:8192\n"
  "v_mfma_f32_16x16x32_bf16 v[52:55], a[172:175], a[128:131], v[52:55]\n"
  "buffer_load_dwordx4 v10, s[24:27], s55 offen offset:0 lds\n"
  "v_mfma_f32_16x16x32_bf16 v[64:67], a[128:131], a[176:179], v[64:67]\n"
  "ds_read_b64_tr_b16 a[226:227], v2 offset:9216\n"
  "v_mfma_f32_16x16x32_bf16 v[72:75], a[128:131], a[180:183], v[72:75]\n"
  "v_mfma_f32_16x16x32_bf16 v[80:83], a[128:131], a[184:187], v[80:83]\n"
  "ds_read_b64_tr_b16 a[228:229], v3 offset:8192\n"
  "v_mfma_f32_16x16x32_bf16 v[88:91], a[128:131], a[188:191], v[88:91]\n"
  "v_mfma_f32_16x16x32_bf16 v[40:43], a[160:163], a[132:135], v[40:43]\n"
  "buffer_load_dwordx4 v11, s[24:27], s55 offen offset:1024 lds\n"
  "ds_read_b64_tr_b16 a[230:231], v3 offset:9216\n"
  "v_mfma_f32_16x16x32_bf16 v[44:47], a[164:167], a[132:135], v[44:47]\n"
  "v_mfma_f32_16x16x32_bf16 v[56:59], a[168:171], a[132:135], v[56:59]\n"
  "ds_read_b64_tr_b16 a[232:233], v4 offset:8192\n"
  "v_mfma_f32_16x16x32_bf16 v[60:63], a[172:175], a[132:135], v[60:63]\n"
  "v_mfma_f32_16x16x32_bf16 v[68:71], a[132:135], a[176:179], v[68:71]\n"
  "ds_read_b64_tr_b16 a[234:235], v4 offset:9216\n"
  "v_mfma_f32_16x16x32_bf16 v[76:79], a[132:135], a[180:183], v[76:79]\n"
  "buffer_load_dwordx4 v12, s[24:27], s55 offen offset:2048 lds\n"
  "v_mfma_f32_16x16x32_bf16 v[84:87], a[132:135], a[184:187], v[84:87]\n"
  "ds_read_b64_tr_b16 a[236:237], v5 offset:8192\n"
  "v_mfma_f32_16x16x32_bf16 v[92:95], a[132:135], a[188:191], v[92:95]\n"
  "v_mfma_f32_16x16x32_bf16 v[96:99], a[160:163], a[136:139], v[96:99]\n"
  "ds_read_b64_tr_b16 a[238:239], v5 offset:9216\n"
  "v_mfma_f32_16x16x32_bf16 v[100:103], a[164:167], a[136:139], v[100:103]\n"
  "v_mfma_f32_16x16x32_bf16 v[112:115], a[168:171], a[136:139], v[112:115]\n"
  "buffer_load_dwordx4 v13, s[24:27], s55 offen offset:3072 lds\n"
  "ds_read_b64_tr_b16 a[240:241], v2 offset:24576\n"
  "v_mfma_f32_16x16x32_bf16 v[116:119], a[172:175], a[136:139], v[116:119]\n"
  "v_mfma_f32_16x16x32_bf16 v[128:131], a[136:139], a[176:179], v[128:131]\n"
  "ds_read_b64_tr_b16 a[242:243], v2 offset:25600\n"
  "v_mfma_f32_16x16x32_bf16 v[136:139], a[136:139], a[180:183], v[136:139]\n"
  "v_mfma_f32_16x16x32_bf16 v[144:147], a[136:139], a[184:187], v[144:147]\n"
  "ds_read_b64_tr_b16 a[244:245], v3 offset:24576\n"
  "v_mfma_f32_16x16x32_bf16 v[152:155], a[136:139], a[188:191], v[152:155]\n"
  "v_mfma_f32_16x16x32_bf16 v[104:107], a[160:163], a[140:143], v[104:107]\n"
  "ds_read_b64_tr_b16 a[246:247], v3 offset:25600\n"
  "v_mfma_f32_16x16x32_bf16 v[108:111], a[164:167], a[140:143], v[108:111]\n"
  "v_mfma_f32_16x16x32_bf16 v[120:123], a[168:171], a[140:143], v[120:123]\n"
  "ds_read_b64_tr_b16 a[248:249], v4 offset:24576\n"
  "v_mfma_f32_16x16x32_bf16 v[124:127], a[172:175], a[140:143], v[124:127]\n"
  "v_mfma_f32_16x16x32_bf16 v[132:135], a[140:143], a[176:179], v[132:135]\n"
  "ds_read_b64_tr_b16 a[250:251], v4 offset:25600\n"
  "v_mfma_f32_16x16x32_bf16 v[140:143], a[140:143], a[180:183], v[140:143]\n"
  "v_mfma_f32_16x16x32_bf16 v[148:151], a[140:143], a[184:187], v[148:151]\n"
  "ds_read_b64_tr_b16 a[252:253], v5 offset:24576\n"
  "v_mfma_f32_16x16x32_bf16 v[156:159], a[140:143], a[188:191], v[156:159]\n"
  "v_mfma_f32_16x16x32_bf16 v[160:163], a[160:163], a[144:147], v[160:163]\n"
  "ds_read_b64_tr_b16 a[254:255], v5 offset:25600\n"
  "v_mfma_f32_16x16x32_bf16 v[164:167], a[164:167], a[144:147], v[164:167]\n"
  "v_mfma_f32_16x16x32_bf16 v[176:179], a[168:171], a[144:147], v[176:179]\n"
  "ds_read_b128 a[196:199], v1 offset:1024\n"
  "v_mfma_f32_16x16x32_bf16 v[180:183], a[172:175], a[144:147], v[180:183]\n"
  "v_mfma_f32_16x16x32_bf16 v[192:195], a[144:147], a[176:179], v[192:195]\n"
  "ds_read_b128 a[200:203], v1 offset:4096\n"
  "v_mfma_f32_16x16x32_bf16 v[200:203], a[144:147], a[180:183], v[200:203]\n"
  "v_mfma_f32_16x16x32_bf16 v[208:211], a[144:147], a[184:187], v[208:211]\n"
  "ds_read_b128 a[204:207], v1 offset:5120\n"
  "v_mfma_f32_16x16x32_bf16 v[216:219], a[144:147], a[188:191], v[216:219]\n"
  "v_mfma_f32_16x16x32_bf16 v[168:171], a[160:163], a[148:151], v[168:171]\n"
  "ds_read_b128 a[208:211], v1 offset:8192\n"
  "v_mfma_f32_16x16x32_bf16 v[172:175], a[164:167], a[148:151], v[172:175]\n"
  "v_accvgpr_read_b32 v31, a124\n"
  "v_mfma_f32_16x16x32_bf16 v[184:187], a[168:171], a[148:151], v[184:187]\n"
  "ds_read_b128 a[212:215], v1 offset:9216\n"
  "v_mfma_f32_16x16x32_bf16 v[188:191], a[172:175], a[148:151], v[188:191]\n"
  "v_mfma_f32_16x16x32_bf16 v[196:199], a[148:151], a[176:179], v[196:199]\n"
  "ds_read_b128 a[216:219], v1 offset:12288\n"
  "v_mfma_f32_16x16x32_bf16 v[204:207], a[148:151], a[180:183], v[204:207]\n"
  "buffer_load_dword a127, v31, s[44:47], s77 offen\n"
  "v_mfma_f32_16x16x32_bf16 v[212:215], a[148:151], a[184:187], v[212:215]\n"
  "ds_read_b128 a[220:223], v1 offset:13312\n"
  "v_mfma_f32_16x16x32_bf16 v[220:223], a[148:151], a[188:191], v[220:223]\n"
  "v_mfma_f32_16x16x32_bf16 v[224:227], a[160:163], a[152:155], v[224:227]\n"
  "v_mfma_f32_16x16x32_bf16 v[228:231], a[164:167], a[152:155], v[228:231]\n"
  "v_mfma_f32_16x16x32_bf16 v[240:243], a[168:171], a[152:155], v[240:243]\n"
  "v_mfma_f32_16x16x32_bf16 v[244:247], a[172:175], a[152:155], v[244:247]\n"
  "v_mfma_f32_16x16x32_bf16 a[0:3], a[152:155], a[176:179], a[0:3]\n"
  "v_mfma_f32_16x16x32_bf16 a[8:11], a[152:155], a[180:183], a[8:11]\n"
  "v_mfma_f32_16x16x32_bf16 a[16:19], a[152:155], a[184:187], a[16:19]\n"
  "v_mfma_f32_16x16x32_bf16 a[24:27], a[152:155], a[188:191], a[24:27]\n"
  "v_mfma_f32_16x16x32_bf16 v[232:235], a[160:163], a[156:159], v[232:235]\n"
  "v_mfma_f32_16x16x32_bf16 v[236:239], a[164:167], a[156:159], v[236:239]\n"
  "v_mfma_f32_16x16x32_bf16 v[248:251], a[168:171], a[156:159], v[248:251]\n"
  "v_mfma_f32_16x16x32_bf16 v[252:255], a[172:175], a[156:159], v[252:255]\n"
  "v_mfma_f32_16x16x32_bf16 a[4:7], a[156:159], a[176:179], a[4:7]\n"
  "v_mfma_f32_16x16x32_bf16 a[12:15], a[156:159], a[180:183], a[12:15]\n"
  "v_mfma_f32_16x16x32_bf16 a[20:23], a[156:159], a[184:187], a[20:23]\n"
  "v_mfma_f32_16x16x32_bf16 a[28:31], a[156:159], a[188:191], a[28:31]\n"
  "s_waitcnt lgkmcnt(0)\n"
  "v_mfma_f32_16x16x32_bf16 v[32:35], a[224:227], a[192:195], v[32:35]\n"
  "v_xor_b32 v0, 0x10000, v0\n"
  "v_xor_b32 v2, 0x10000, v2\n"
  "v_mfma_f32_16x16x32_bf16 v[36:39], a[228:231], a[192:195], v[36:39]\n"
  "v_xor_b32 v3, 0x10000, v3\n"
  "v_xor_b32 v4, 0x10000, v4\n"
  "v_mfma_f32_16x16x32_bf16 v[48:51], a[232:235], a[192:195], v[48:51]\n"
  "v_xor_b32 v5, 0x10000, v5\n"
  "v_xor_b32 v1, 0x10000, v1\n"
  "v_mfma_f32_16x16x32_bf16 v[52:55], a[236:239], a[192:195], v[52:55]\n"
  "s_waitcnt vmcnt(1) lgkmcnt(0)\n"
  "s_barrier\n"
  "v_mfma_f32_16x16x32_bf16 v[64:67], a[192:195], a[240:243], v[64:67]\n"
  "s_mov_b32 s55, 0x180\n"
  "s_mov_b32 s56, 0x180000\n"
  "ds_read_b128 a[128:131], v0 offset:0\n"
  "v_mfma_f32_16x16x32_bf16 v[72:75], a[192:195], a[244:247], v[72:75]\n"
  "s_add_u32 m0, s53, 0x8000\n"
  "v_mfma_f32_16x16x32_bf16 v[80:83], a[192:195], a[248:251], v[80:83]\n"
  "ds_read_b64_tr_b16 a[160:161], v2 offset:0\n"
  "v_mfma_f32_16x16x32_bf16 v[88:91], a[192:195], a[252:255], v[88:91]\n"
  "buffer_load_dwordx4 v14, s[28:31], s56 offen offset:0 lds\n"
  "v_mfma_f32_16x16x32_bf16 v[40:43], a[224:227], a[196:199], v[40:43]\n"
  "ds_read_b64_tr_b16 a[162:163], v2 offset:1024\n"
  "v_mfma_f32_16x16x32_bf16 v[44:47], a[228:231], a[196:199], v[44:47]\n"
  "v_mfma_f32_16x16x32_bf16 v[56:59], a[232:235], a[196:199], v[56:59]\n"
  "ds_read_b64_tr_b16 a[164:165], v3 offset:0\n"
  "v_mfma_f32_16x16x32_bf16 v[60:63], a[236:239], a[196:199], v[60:63]\n"
  "v_mfma_f32_16x16x32_bf16 v[68:71], a[196:199], a[240:243], v[68:71]\n"
  "buffer_load_dwordx4 v15, s[28:31], s56 offen offset:1024 lds\n"
  "ds_read_b64_tr_b16 a[166:167], v3 offset:1024\n"
  "v_mfma_f32_16x16x32_bf16 v[76:79], a[196:199], a[244:247], v[76:79]\n"
  "v_mfma_f32_16x16x32_bf16 v[84:87], a[196:199], a[248:251], v[84:87]\n"
  "ds_read_b64_tr_b16 a[168:169], v4 offset:0\n"
  "v_mfma_f32_16x16x32_bf16 v[92:95], a[196:199], a[252:255], v[92:95]\n"
  "v_mfma_f32_16x16x32_bf16 v[96:99], a[224:227], a[200:203], v[96:99]\n"
  "ds_read_b64_tr_b16 a[170:171], v4 offset:1024\n"
  "v_mfma_f32_16x16x32_bf16 v[100:103], a[228:231], a[200:203], v[100:103]\n"
  "buffer_load_dwordx4 v16, s[28:31], s56 offen offset:2048 lds\n"
  "v_mfma_f32_16x16x32_bf16 v[112:115], a[232:235], a[200:203], v[112:115]\n"
  "ds_read_b64_tr_b16 a[172:173], v5 offset:0\n"
  "v_mfma_f32_16x16x32_bf16 v[116:119], a[236:239], a[200:203], v[116:119]\n"
  "v_mfma_f32_16x16x32_bf16 v[128:131], a[200:203], a[240:243], v[128:131]\n"
  "ds_read_b64_tr_b16 a[174:175], v5 offset:1024\n"
  "v_mfma_f32_16x16x32_bf16 v[136:139], a[200:203], a[244:247], v[136:139]\n"
  "v_mfma_f32_16x16x32_bf16 v[144:147], a[200:203], a[248:251], v[144:147]\n"
  "buffer_load_dwordx4 v17, s[28:31], s56 offen offset:3072 lds\n"
  "ds_read_b64_tr_b16 a[176:177], v2 offset:16384\n"
  "v_mfma_f32_16x16x32_bf16 v[152:155], a[200:203], a[252:255], v[152:155]\n"
  "v_mfma_f32_16x16x32_bf16 v[104:107], a[224:227], a[204:207], v[104:107]\n"
  "ds_read_b64_tr_b16 a[178:179], v2 offset:17408\n"
  "v_mfma_f32_16x16x32_bf16 v[108:111], a[228:231], a[204:207], v[108:111]\n"
  "s_add_u32 m0, s52, 0x0\n"
  "v_mfma_f32_16x16x32_bf16 v[120:123], a[232:235], a[204:207], v[120:123]\n"
  "ds_read_b64_tr_b16 a[180:181], v3 offset:16384\n"
  "v_mfma_f32_16x16x32_bf16 v[124:127], a[236:239], a[204:207], v[124:127]\n"
  "buffer_load_dwordx4 v6, s[24:27], s55 offen offset:0 lds\n"
  "v_mfma_f32_16x16x32_bf16 v[132:135], a[204:207], a[240:243], v[132:135]\n"
  "ds_read_b64_tr_b16 a[182:183], v3 offset:17408\n"
  "v_mfma_f32_16x16x32_bf16 v[140:143], a[204:207], a[244:247], v[140:143]\n"
  "v_mfma_f32_16x16x32_bf16 v[148:151], a[204:207], a[248:251], v[148:151]\n"
  "ds_read_b64_tr_b16 a[184:185], v4 offset:16384\n"
  "v_mfma_f32_16x16x32_bf16 v[156:159], a[204:207], a[252:255], v[156:159]\n"
  "v_mfma_f32_16x16x32_bf16 v[160:163], a[224:227], a[208:211], v[160:163]\n"
  "buffer_load_dwordx4 v7, s[24:27], s55 offen offset:1024 lds\n"
  "ds_read_b64_tr_b16 a[186:187], v4 offset:17408\n"
  "v_mfma_f32_16x16x32_bf16 v[164:167], a[228:231], a[208:211], v[164:167]\n"
  "v_mfma_f32_16x16x32_bf16 v[176:179], a[232:235], a[208:211], v[176:179]\n"
  "ds_read_b64_tr_b16 a[188:189], v5 offset:16384\n"
  "v_mfma_f32_16x16x32_bf16 v[180:183], a[236:239], a[208:211], v[180:183]\n"
  "v_mfma_f32_16x16x32_bf16 v[192:195], a[208:211], a[240:243], v[192:195]\n"
  "ds_read_b64_tr_b16 a[190:191], v5 offset:17408\n"
  "v_mfma_f32_16x16x32_bf16 v[200:203], a[208:211], a[244:247], v[200:203]\n"
  "buffer_load_dwordx4 v8, s[24:27], s55 offen offset:2048 lds\n"
  "v_mfma_f32_16x16x32_bf16 v[208:211], a[208:211], a[248:251], v[208:211]\n"
  "ds_read_b128 a[132:135], v0 offset:1024\n"
  "v_mfma_f32_16x16x32_bf16 v[216:219], a[208:211], a[252:255], v[216:219]\n"
  "v_mfma_f32_16x16x32_bf16 v[168:171], a[224:227], a[212:215], v[168:171]\n"
  "ds_read_b128 a[136:139], v0 offset:4096\n"
  "v_mfma_f32_16x16x32_bf16 v[172:175], a[228:231], a[212:215], v[172:175]\n"
  "v_mfma_f32_16x16x32_bf16 v[184:187], a[232:235], a[212:215], v[184:187]\n"
  "buffer_load_dwordx4 v9, s[24:27], s55 offen offset:3072 lds\n"
  "ds_read_b128 a[140:143], v0 offset:5120\n"
  "v_mfma_f32_16x16x32_bf16 v[188:191], a[236:239], a[212:215], v[188:191]\n"
  "v_mfma_f32_16x16x32_bf16 v[196:199], a[212:215], a[240:243], v[196:199]\n"
  "ds_read_b128 a[144:147], v0 offset:8192\n"
  "v_mfma_f32_16x16x32_bf16 v[204:207], a[212:215], a[244:247], v[204:207]\n"
  "s_add_u32 m0, s53, 0xc000\n"
  "v_mfma_f32_16x16x32_bf16 v[212:215], a[212:215], a[248:251], v[212:215]\n"
  "ds_read_b128 a[148:151], v0 offset:9216\n"
  "v_mfma_f32_16x16x32_bf16 v[220:223], a[212:215], a[252:255], v[220:223]\n"
  "buffer_load_dwordx4 v14, s[32:35], s56 offen offset:0 lds\n"
  "v_mfma_f32_16x16x32_bf16 v[224:227], a[224:227], a[216:219], v[224:227]\n"
  "ds_read_b128 a[152:155], v0 offset:12288\n"
  "v_mfma_f32_16x16x32_bf16 v[228:231], a[228:231], a[216:219], v[228:231]\n"
  "v_mfma_f32_16x16x32_bf16 v[240:243], a[232:235], a[216:219], v[240:243]\n"
  "ds_read_b128 a[156:159], v0 offset:13312\n"
  "v_mfma_f32_16x16x32_bf16 v[244:247], a[236:239], a[216:219], v[244:247]\n"
  "v_mfma_f32_16x16x32_bf16 a[0:3], a[216:219], a[240:243], a[0:3]\n"
  "buffer_load_dwordx4 v15, s[32:35], s56 offen offset:1024 lds\n"
  "v_mfma_f32_16x16x32_bf16 a[8:11], a[216:219], a[244:247], a[8:11]\n"
  "v_mfma_f32_16x16x32_bf16 a[16:19], a[216:219], a[248:251], a[16:19]\n"
  "v_mfma_f32_16x16x32_bf16 a[24:27], a[216:219], a[252:255], a[24:27]\n"
  "v_mfma_f32_16x16x32_bf16 v[232:235], a[224:227], a[220:223], v[232:235]\n"
  "v_mfma_f32_16x16x32_bf16 v[236:239], a[228:231], a[220:223], v[236:239]\n"
  "buffer_load_dwordx4 v16, s[32:35], s56 offen offset:2048 lds\n"
  "v_mfma_f32_16x16x32_bf16 v[248:251], a[232:235], a[220:223], v[248:251]\n"
  "v_mfma_f32_16x16x32_bf16 v[252:255], a[236:239], a[220:223], v[252:255]\n"
  "v_mfma_f32_16x16x32_bf16 a[4:7], a[220:223], a[240:243], a[4:7]\n"
  "v_mfma_f32_16x16x32_bf16 a[12:15], a[220:223], a[244:247], a[12:15]\n"
  "v_mfma_f32_16x16x32_bf16 a[20:23], a[220:223], a[248:251], a[20:23]\n"
  "buffer_load_dwordx4 v17, s[32:35], s56 offen offset:3072 lds\n"
  "v_mfma_f32_16x16x32_bf16 a[28:31], a[220:223], a[252:255], a[28:31]\n"
  "s_waitcnt lgkmcnt(0)\n"
  "v_mfma_f32_16x16x32_bf16 v[32:35], a[160:163], a[128:131], v[32:35]\n"
  "ds_read_b128 a[192:195], v1 offset:0\n"
  "v_mfma_f32_16x16x32_bf16 v[36:39], a[164:167], a[128:131], v[36:39]\n"
  "s_add_u32 m0, s52, 0x1000\n"
  "v_mfma_f32_16x16x32_bf16 v[48:51], a[168:171], a[128:131], v[48:51]\n"
  "ds_read_b64_tr_b16 a[224:225], v2 offset:8192\n"
  "v_mfma_f32_16x16x32_bf16 v[52:55], a[172:175], a[128:131], v[52:55]\n"
  "buffer_load_dwordx4 v10, s[24:27], s55 offen offset:0 lds\n"
  "v_mfma_f32_16x16x32_bf16 v[64:67], a[128:131], a[176:179], v[64:67]\n"
  "ds_read_b64_tr_b16 a[226:227], v2 offset:9216\n"
  "v_mfma_f32_16x16x32_bf16 v[72:75], a[128:131], a[180:183], v[72:75]\n"
  "v_mfma_f32_16x16x32_bf16 v[80:83], a[128:131], a[184:187], v[80:83]\n"
  "ds_read_b64_tr_b16 a[228:229], v3 offset:8192\n"
  "v_mfma_f32_16x16x32_bf16 v[88:91], a[128:131], a[188:191], v[88:91]\n"
  "v_mfma_f32_16x16x32_bf16 v[40:43], a[160:163], a[132:135], v[40:43]\n"
  "buffer_load_dwordx4 v11, s[24:27], s55 offen offset:1024 lds\n"
  "ds_read_b64_tr_b16 a[230:231], v3 offset:9216\n"
  "v_mfma_f32_16x16x32_bf16 v[44:47], a[164:167], a[132:135], v[44:47]\n"
  "v_mfma_f32_16x16x32_bf16 v[56:59], a[168:171], a[132:135], v[56:59]\n"
  "ds_read_b64_tr_b16 a[232:233], v4 offset:8192\n"
  "v_mfma_f32_16x16x32_bf16 v[60:63], a[172:175], a[132:135], v[60:63]\n"
  "v_mfma_f32_16x16x32_bf16 v[68:71], a[132:135], a[176:179], v[68:71]\n"
  "ds_read_b64_tr_b16 a[234:235], v4 offset:9216\n"
  "v_mfma_f32_16x16x32_bf16 v[76:79], a[132:135], a[180:183], v[76:79]\n"
  "buffer_load_dwordx4 v12, s[24:27], s55 offen offset:2048 lds\n"
  "v_mfma_f32_16x16x32_bf16 v[84:87], a[132:135], a[184:187], v[84:87]\n"
  "ds_read_b64_tr_b16 a[236:237], v5 offset:8192\n"
  "v_mfma_f32_16x16x32_bf16 v[92:95], a[132:135], a[188:191], v[92:95]\n"
  "v_mfma_f32_16x16x32_bf16 v[96:99], a[160:163], a[136:139], v[96:99]\n"
  "ds_read_b64_tr_b16 a[238:239], v5 offset:9216\n"
  "v_mfma_f32_16x16x32_bf16 v[100:103], a[164:167], a[136:139], v[100:103]\n"
  "v_mfma_f32_16x16x32_bf16 v[112:115], a[168:171], a[136:139], v[112:115]\n"
  "buffer_load_dwordx4 v13, s[24:27], s55 offen offset:3072 lds\n"
  "ds_read_b64_tr_b16 a[240:241], v2 offset:24576\n"
  "v_mfma_f32_16x16x32_bf16 v[116:119], a[172:175], a[136:139], v[116:119]\n"
  "v_mfma_f32_16x16x32_bf16 v[128:131], a[136:139], a[176:179], v[128:131]\n"
  "ds_read_b64_tr_b16 a[242:243], v2 offset:25600\n"
  "v_mfma_f32_16x16x32_bf16 v[136:139], a[136:139], a[180:183], v[136:139]\n"
  "v_mfma_f32_16x16x32_bf16 v[144:147], a[136:139], a[184:187], v[144:147]\n"
  "ds_read_b64_tr_b16 a[244:245], v3 offset:24576\n"
  "v_mfma_f32_16x16x32_bf16 v[152:155], a[136:139], a[188:191], v[152:155]\n"
  "v_mfma_f32_16x16x32_bf16 v[104:107], a[160:163], a[140:143], v[104:107]\n"
  "ds_read_b64_tr_b16 a[246:247], v3 offset:25600\n"
  "v_mfma_f32_16x16x32_bf16 v[108:111], a[164:167], a[140:143], v[108:111]\n"
  "v_mfma_f32_16x16x32_bf16 v[120:123], a[168:171], a[140:143], v[120:123]\n"
  "ds_read_b64_tr_b16 a[248:249], v4 offset:24576\n"
  "v_mfma_f32_16x16x32_bf16 v[124:127], a[172:175], a[140:143], v[124:127]\n"
  "v_mfma_f32_16x16x32_bf16 v[132:135], a[140:143], a[176:179], v[132:135]\n"
  "ds_read_b64_tr_b16 a[250:251], v4 offset:25600\n"
  "v_mfma_f32_16x16x32_bf16 v[140:143], a[140:143], a[180:183], v[140:143]\n"
  "v_mfma_f32_16x16x32_bf16 v[148:151], a[140:143], a[184:187], v[148:151]\n"
  "ds_read_b64_tr_b16 a[252:253], v5 offset:24576\n"
  "v_mfma_f32_16x16x32_bf16 v[156:159], a[140:143], a[188:191], v[156:159]\n"
  "v_mfma_f32_16x16x32_bf16 v[160:163], a[160:163], a[144:147], v[160:163]\n"
  "ds_read_b64_tr_b16 a[254:255], v5 offset:25600\n"
  "v_mfma_f32_16x16x32_bf16 v[164:167], a[164:167], a[144:147], v[164:167]\n"
  "v_mfma_f32_16x16x32_bf16 v[176:179], a[168:171], a[144:147], v[176:179]\n"
  "ds_read_b128 a[196:199], v1 offset:1024\n"
  "v_mfma_f32_16x16x32_bf16 v[180:183], a[172:175], a[144:147], v[180:183]\n"
  "v_mfma_f32_16x16x32_bf16 v[192:195], a[144:147], a[176:179], v[192:195]\n"
  "ds_read_b128 a[200:203], v1 offset:4096\n"
  "v_mfma_f32_16x16x32_bf16 v[200:203], a[144:147], a[180:183], v[200:203]\n"
  "v_mfma_f32_16x16x32_bf16 v[208:211], a[144:147], a[184:187], v[208:211]\n"
  "ds_read_b128 a[204:207], v1 offset:5120\n"
  "v_mfma_f32_16x16x32_bf16 v[216:219], a[144:147], a[188:191], v[216:219]\n"
  "v_mfma_f32_16x16x32_bf16 v[168:171], a[160:163], a[148:151], v[168:171]\n"
  "ds_read_b128 a[208:211], v1 offset:8192\n"
  "v_mfma_f32_16x16x32_bf16 v[172:175], a[164:167], a[148:151], v[172:175]\n"
  "v_lshlrev_b32 v31, 3, v30\n"
  "v_mfma_f32_16x16x32_bf16 v[184:187], a[168:171], a[148:151], v[184:187]\n"
  "ds_read_b128 a[212:215], v1 offset:9216\n"
  "v_mfma_f32_16x16x32_bf16 v[188:191], a[172:175], a[148:151], v[188:191]\n"
  "v_mfma_f32_16x16x32_bf16 v[196:199], a[148:151], a[176:179], v[196:199]\n"
  "ds_read_b128 a[216:219], v1 offset:12288\n"
  "v_mfma_f32_16x16x32_bf16 v[204:207], a[148:151], a[180:183], v[204:207]\n"
  "buffer_load_dword a127, v31, s[40:43], s78 offen\n"
  "s_add_u32 s78, s78, 0x2000\n"
  "v_mfma_f32_16x16x32_bf16 v[212:215], a[148:151], a[184:187], v[212:215]\n"
  "ds_read_b128 a[220:223], v1 offset:13312\n"
  "v_mfma_f32_16x16x32_bf16 v[220:223], a[148:151], a[188:191], v[220:223]\n"
  "v_mfma_f32_16x16x32_bf16 v[224:227], a[160:163], a[152:155], v[224:227]\n"
  "v_mfma_f32_16x16x32_bf16 v[228:231], a[164:167], a[152:155], v[228:231]\n"
  "v_mfma_f32_16x16x32_bf16 v[240:243], a[168:171], a[152:155], v[240:243]\n"
  "v_mfma_f32_16x16x32_bf16 v[244:247], a[172:175], a[152:155], v[244:247]\n"
  "v_mfma_f32_16x16x32_bf16 a[0:3], a[152:155], a[176:179], a[0:3]\n"
  "v_mfma_f32_16x16x32_bf16 a[8:11], a[152:155], a[180:183], a[8:11]\n"
  "v_mfma_f32_16x16x32_bf16 a[16:19], a[152:155], a[184:187], a[16:19]\n"
  "v_mfma_f32_16x16x32_bf16 a[24:27], a[152:155], a[188:191], a[24:27]\n"
  "v_mfma_f32_16x16x32_bf16 v[232:235], a[160:163], a[156:159], v[232:235]\n"
  "v_mfma_f32_16x16x32_bf16 v[236:239], a[164:167], a[156:159], v[236:239]\n"
  "v_mfma_f32_16x16x32_bf16 v[248:251], a[168:171], a[156:159], v[248:251]\n"
  "v_mfma_f32_16x16x32_bf16 v[252:255], a[172:175], a[156:159], v[252:255]\n"
  "v_mfma_f32_16x16x32_bf16 a[4:7], a[156:159], a[176:179], a[4:7]\n"
  "v_mfma_f32_16x16x32_bf16 a[12:15], a[156:159], a[180:183], a[12:15]\n"
  "v_mfma_f32_16x16x32_bf16 a[20:23], a[156:159], a[184:187], a[20:23]\n"
  "v_mfma_f32_16x16x32_bf16 a[28:31], a[156:159], a[188:191], a[28:31]\n"
  "s_waitcnt lgkmcnt(0)\n"
  "v_mfma_f32_16x16x32_bf16 v[32:35], a[224:227], a[192:195], v[32:35]\n"
  "v_xor_b32 v0, 0x10000, v0\n"
  "v_xor_b32 v2, 0x10000, v2\n"
  "v_mfma_f32_16x16x32_bf16 v[36:39], a[228:231], a[192:195], v[36:39]\n"
  "v_xor_b32 v3, 0x10000, v3\n"
  "v_xor_b32 v4, 0x10000, v4\n"
  "v_mfma_f32_16x16x32_bf16 v[48:51], a[232:235], a[192:195], v[48:51]\n"
  "v_xor_b32 v5, 0x10000, v5\n"
  "v_xor_b32 v1, 0x10000, v1\n"
  "v_mfma_f32_16x16x32_bf16 v[52:55], a[236:239], a[192:195], v[52:55]\n"
  "s_waitcnt vmcnt(1) lgkmcnt(0)\n"
  "s_barrier\n"
  "v_mfma_f32_16x16x32_bf16 v[64:67], a[192:195], a[240:243], v[64:67]\n"
  "s_mov_b32 s55, 0x200\n"
  "s_mov_b32 s56, 0x200000\n"
  "ds_read_b128 a[128:131], v0 offset:0\n"
  "v_mfma_f32_16x16x32_bf16 v[72:75], a[192:195], a[244:247], v[72:75]\n"
  "s_add_u32 m0, s53, 0x18000\n"
  "v_mfma_f32_16x16x32_bf16 v[80:83], a[192:195], a[248:251], v[80:83]\n"
  "ds_read_b64_tr_b16 a[160:161], v2 offset:0\n"
  "v_mfma_f32_16x16x32_bf16 v[88:91], a[192:195], a[252:255], v[88:91]\n"
  "buffer_load_dwordx4 v14, s[28:31], s56 offen offset:0 lds\n"
  "v_mfma_f32_16x16x32_bf16 v[40:43], a[224:227], a[196:199], v[40:43]\n"
  "ds_read_b64_tr_b16 a[162:163], v2 offset:1024\n"
  "v_mfma_f32_16x16x32_bf16 v[44:47], a[228:231], a[196:199], v[44:47]\n"
  "v_mfma_f32_16x16x32_bf16 v[56:59], a[232:235], a[196:199], v[56:59]\n"
  "ds_read_b64_tr_b16 a[164:165], v3 offset:0\n"
  "v_mfma_f32_16x16x32_bf16 v[60:63], a[236:239], a[196:199], v[60:63]\n"
  "v_mfma_f32_16x16x32_bf16 v[68:71], a[196:199], a[240:243], v[68:71]\n"
  "buffer_load_dwordx4 v15, s[28:31], s56 offen offset:1024 lds\n"
  "ds_read_b64_tr_b16 a[166:167], v3 offset:1024\n"
  "v_mfma_f32_16x16x32_bf16 v[76:79], a[196:199], a[244:247], v[76:79]\n"
  "v_mfma_f32_16x16x32_bf16 v[84:87], a[196:199], a[248:251], v[84:87]\n"
  "ds_read_b64_tr_b16 a[168:169], v4 offset:0\n"
  "v_mfma_f32_16x16x32_bf16 v[92:95], a[196:199], a[252:255], v[92:95]\n"
  "v_mfma_f32_16x16x32_bf16 v[96:99], a[224:227], a[200:203], v[96:99]\n"
  "ds_read_b64_tr_b16 a[170:171], v4 offset:1024\n"
  "v_mfma_f32_16x16x32_bf16 v[100:103], a[228:231], a[200:203], v[100:103]\n"
  "buffer_load_dwordx4 v16, s[28:31], s56 offen offset:2048 lds\n"
  "v_mfma_f32_16x16x32_bf16 v[112:115], a[232:235], a[200:203], v[112:115]\n"
  "ds_read_b64_tr_b16 a[172:173], v5 offset:0\n"
  "v_mfma_f32_16x16x32_bf16 v[116:119], a[236:239], a[200:203], v[116:119]\n"
  "v_mfma_f32_16x16x32_bf16 v[128:131], a[200:203], a[240:243], v[128:131]\n"
  "ds_read_b64_tr_b16 a[174:175], v5 offset:1024\n"
  "v_mfma_f32_16x16x32_bf16 v[136:139], a[200:203], a[244:247], v[136:139]\n"
  "v_mfma_f32_16x16x32_bf16 v[144:147], a[200:203], a[248:251], v[144:147]\n"
  "buffer_load_dwordx4 v17, s[28:31], s56 offen offset:3072 lds\n"
  "ds_read_b64_tr_b16 a[176:177], v2 offset:16384\n"
  "v_mfma_f32_16x16x32_bf16 v[152:155], a[200:203], a[252:255], v[152:155]\n"
  "v_mfma_f32_16x16x32_bf16 v[104:107], a[224:227], a[204:207], v[104:107]\n"
  "ds_read_b64_tr_b16 a[178:179], v2 offset:17408\n"
  "v_mfma_f32_16x16x32_bf16 v[108:111], a[228:231], a[204:207], v[108:111]\n"
  "s_add_u32 m0, s52, 0x10000\n"
  "v_mfma_f32_16x16x32_bf16 v[120:123], a[232:235], a[204:207], v[120:123]\n"
  "ds_read_b64_tr_b16 a[180:181], v3 offset:16384\n"
  "v_mfma_f32_16x16x32_bf16 v[124:127], a[236:239], a[204:207], v[124:127]\n"
  "buffer_load_dwordx4 v6, s[24:27], s55 offen offset:0 lds\n"
  "v_mfma_f32_16x16x32_bf16 v[132:135], a[204:207], a[240:243], v[132:135]\n"
  "ds_read_b64_tr_b16 a[182:183], v3 offset:17408\n"
  "v_mfma_f32_16x16x32_bf16 v[140:143], a[204:207], a[244:247], v[140:143]\n"
  "v_mfma_f32_16x16x32_bf16 v[148:151], a[204:207], a[248:251], v[148:151]\n"
  "ds_read_b64_tr_b16 a[184:185], v4 offset:16384\n"
  "v_mfma_f32_16x16x32_bf16 v[156:159], a[204:207], a[252:255], v[156:159]\n"
  "v_mfma_f32_16x16x32_bf16 v[160:163], a[224:227], a[208:211], v[160:163]\n"
  "buffer_load_dwordx4 v7, s[24:27], s55 offen offset:1024 lds\n"
  "ds_read_b64_tr_b16 a[186:187], v4 offset:17408\n"
  "v_mfma_f32_16x16x32_bf16 v[164:167], a[228:231], a[208:211], v[164:167]\n"
  "v_mfma_f32_16x16x32_bf16 v[176:179], a[232:235], a[208:211], v[176:179]\n"
  "ds_read_b64_tr_b16 a[188:189], v5 offset:16384\n"
  "v_mfma_f32_16x16x32_bf16 v[180:183], a[236:239], a[208:211], v[180:183]\n"
  "v_mfma_f32_16x16x32_bf16 v[192:195], a[208:211], a[240:243], v[192:195]\n"
  "ds_read_b64_tr_b16 a[190:191], v5 offset:17408\n"
  "v_mfma_f32_16x16x32_bf16 v[200:203], a[208:211], a[244:247], v[200:203]\n"
  "buffer_load_dwordx4 v8, s[24:27], s55 offen offset:2048 lds\n"
  "v_mfma_f32_16x16x32_bf16 v[208:211], a[208:211], a[248:251], v[208:211]\n"
  "ds_read_b128 a[132:135], v0 offset:1024\n"
  "v_mfma_f32_16x16x32_bf16 v[216:219], a[208:211], a[252:255], v[216:219]\n"
  "v_mfma_f32_16x16x32_bf16 v[168:171], a[224:227], a[212:215], v[168:171]\n"
  "ds_read_b128 a[136:139], v0 offset:4096\n"
  "v_mfma_f32_16x16x32_bf16 v[172:175], a[228:231], a[212:215], v[172:175]\n"
  "v_mfma_f32_16x16x32_bf16 v[184:187], a[232:235], a[212:215], v[184:187]\n"
  "buffer_load_dwordx4 v9, s[24:27], s55 offen offset:3072 lds\n"
  "ds_read_b128 a[140:143], v0 offset:5120\n"
  "v_mfma_f32_16x16x32_bf16 v[188:191], a[236:239], a[212:215], v[188:191]\n"
  "v_mfma_f32_16x16x32_bf16 v[196:199], a[212:215], a[240:243], v[196:199]\n"
  "ds_read_b128 a[144:147], v0 offset:8192\n"
  "v_mfma_f32_16x16x32_bf16 v[204:207], a[212:215], a[244:247], v[204:207]\n"
  "s_add_u32 m0, s53, 0x1c000\n"
  "v_mfma_f32_16x16x32_bf16 v[212:215], a[212:215], a[248:251], v[212:215]\n"
  "ds_read_b128 a[148:151], v0 offset:9216\n"
  "v_mfma_f32_16x16x32_bf16 v[220:223], a[212:215], a[252:255], v[220:223]\n"
  "buffer_load_dwordx4 v14, s[32:35], s56 offen offset:0 lds\n"
  "v_mfma_f32_16x16x32_bf16 v[224:227], a[224:227], a[216:219], v[224:227]\n"
  "ds_read_b128 a[152:155], v0 offset:12288\n"
  "v_mfma_f32_16x16x32_bf16 v[228:231], a[228:231], a[216:219], v[228:231]\n"
  "v_mfma_f32_16x16x32_bf16 v[240:243], a[232:235], a[216:219], v[240:243]\n"
  "ds_read_b128 a[156:159], v0 offset:13312\n"
  "v_mfma_f32_16x16x32_bf16 v[244:247], a[236:239], a[216:219], v[244:247]\n"
  "v_mfma_f32_16x16x32_bf16 a[0:3], a[216:219], a[240:243], a[0:3]\n"
  "buffer_load_dwordx4 v15, s[32:35], s56 offen offset:1024 lds\n"
  "v_mfma_f32_16x16x32_bf16 a[8:11], a[216:219], a[244:247], a[8:11]\n"
  "v_mfma_f32_16x16x32_bf16 a[16:19], a[216:219], a[248:251], a[16:19]\n"
  "v_mfma_f32_16x16x32_bf16 a[24:27], a[216:219], a[252:255], a[24:27]\n"
  "v_mfma_f32_16x16x32_bf16 v[232:235], a[224:227], a[220:223], v[232:235]\n"
  "v_mfma_f32_16x16x32_bf16 v[236:239], a[228:231], a[220:223], v[236:239]\n"
  "buffer_load_dwordx4 v16, s[32:35], s56 offen offset:2048 lds\n"
  "v_mfma_f32_16x16x32_bf16 v[248:251], a[232:235], a[220:223], v[248:251]\n"
  "v_mfma_f32_16x16x32_bf16 v[252:255], a[236:239], a[220:223], v[252:255]\n"
  "v_mfma_f32_16x16x32_bf16 a[4:7], a[220:223], a[240:243], a[4:7]\n"
  "v_mfma_f32_16x16x32_bf16 a[12:15], a[220:223], a[244:247], a[12:15]\n"
  "v_mfma_f32_16x16x32_bf16 a[20:23], a[220:223], a[248:251], a[20:23]\n"
  "buffer_load_dwordx4 v17, s[32:35], s56 offen offset:3072 lds\n"
  "v_mfma_f32_16x16x32_bf16 a[28:31], a[220:223], a[252:255], a[28:31]\n"
  "s_waitcnt lgkmcnt(0)\n"
  "v_mfma_f32_16x16x32_bf16 v[32:35], a[160:163], a[128:131], v[32:35]\n"
  "ds_read_b128 a[192:195], v1 offset:0\n"
  "v_mfma_f32_16x16x32_bf16 v[36:39], a[164:167], a[128:131], v[36:39]\n"
  "s_add_u32 m0, s52, 0x11000\n"
  "v_mfma_f32_16x16x32_bf16 v[48:51], a[168:171], a[128:131], v[48:51]\n"
  "ds_read_b64_tr_b16 a[224:225], v2 offset:8192\n"
  "v_mfma_f32_16x16x32_bf16 v[52:55], a[172:175], a[128:131], v[52:55]\n"
  "buffer_load_dwordx4 v10, s[24:27], s55 offen offset:0 lds\n"
  "v_mfma_f32_16x16x32_bf16 v[64:67], a[128:131], a[176:179], v[64:67]\n"
  "ds_read_b64_tr_b16 a[226:227], v2 offset:9216\n"
  "v_mfma_f32_16x16x32_bf16 v[72:75], a[128:131], a[180:183], v[72:75]\n"
  "v_mfma_f32_16x16x32_bf16 v[80:83], a[128:131], a[184:187], v[80:83]\n"
  "ds_read_b64_tr_b16 a[228:229], v3 offset:8192\n"
  "v_mfma_f32_16x16x32_bf16 v[88:91], a[128:131], a[188:191], v[88:91]\n"
  "v_mfma_f32_16x16x32_bf16 v[40:43], a[160:163], a[132:135], v[40:43]\n"
  "buffer_load_dwordx4 v11, s[24:27], s55 offen offset:1024 lds\n"
  "ds_read_b64_tr_b16 a[230:231], v3 offset:9216\n"
  "v_mfma_f32_16x16x32_bf16 v[44:47], a[164:167], a[132:135], v[44:47]\n"
  "v_mfma_f32_16x16x32_bf16 v[56:59], a[168:171], a[132:135], v[56:59]\n"
  "ds_read_b64_tr_b16 a[232:233], v4 offset:8192\n"
  "v_mfma_f32_16x16x32_bf16 v[60:63], a[172:175], a[132:135], v[60:63]\n"
  "v_mfma_f32_16x16x32_bf16 v[68:71], a[132:135], a[176:179], v[68:71]\n"
  "ds_read_b64_tr_b16 a[234:235], v4 offset:9216\n"
  "v_mfma_f32_16x16x32_bf16 v[76:79], a[132:135], a[180:183], v[76:79]\n"
  "buffer_load_dwordx4 v12, s[24:27], s55 offen offset:2048 lds\n"
  "v_mfma_f32_16x16x32_bf16 v[84:87], a[132:135], a[184:187], v[84:87]\n"
  "ds_read_b64_tr_b16 a[236:237], v5 offset:8192\n"
  "v_mfma_f32_16x16x32_bf16 v[92:95], a[132:135], a[188:191], v[92:95]\n"
  "v_mfma_f32_16x16x32_bf16 v[96:99], a[160:163], a[136:139], v[96:99]\n"
  "ds_read_b64_tr_b16 a[238:239], v5 offset:9216\n"
  "v_mfma_f32_16x16x32_bf16 v[100:103], a[164:167], a[136:139], v[100:103]\n"
  "v_mfma_f32_16x16x32_bf16 v[112:115], a[168:171], a[136:139], v[112:115]\n"
  "buffer_load_dwordx4 v13, s[24:27], s55 offen offset:3072 lds\n"
  "ds_read_b64_tr_b16 a[240:241], v2 offset:24576\n"
  "v_mfma_f32_16x16x32_bf16 v[116:119], a[172:175], a[136:139], v[116:119]\n"
  "v_mfma_f32_16x16x32_bf16 v[128:131], a[136:139], a[176:179], v[128:131]\n"
  "ds_read_b64_tr_b16 a[242:243], v2 offset:25600\n"
  "v_mfma_f32_16x16x32_bf16 v[136:139], a[136:139], a[180:183], v[136:139]\n"
  "v_mfma_f32_16x16x32_bf16 v[144:147], a[136:139], a[184:187], v[144:147]\n"
  "ds_read_b64_tr_b16 a[244:245], v3 offset:24576\n"
  "v_mfma_f32_16x16x32_bf16 v[152:155], a[136:139], a[188:191], v[152:155]\n"
  "v_mfma_f32_16x16x32_bf16 v[104:107], a[160:163], a[140:143], v[104:107]\n"
  "ds_read_b64_tr_b16 a[246:247], v3 offset:25600\n"
  "v_mfma_f32_16x16x32_bf16 v[108:111], a[164:167], a[140:143], v[108:111]\n"
  "v_mfma_f32_16x16x32_bf16 v[120:123], a[168:171], a[140:143], v[120:123]\n"
  "ds_read_b64_tr_b16 a[248:249], v4 offset:24576\n"
  "v_mfma_f32_16x16x32_bf16 v[124:127], a[172:175], a[140:143], v[124:127]\n"
  "v_mfma_f32_16x16x32_bf16 v[132:135], a[140:143], a[176:179], v[132:135]\n"
  "ds_read_b64_tr_b16 a[250:251], v4 offset:25600\n"
  "v_mfma_f32_16x16x32_bf16 v[140:143], a[140:143], a[180:183], v[140:143]\n"
  "v_mfma_f32_16x16x32_bf16 v[148:151], a[140:143], a[184:187], v[148:151]\n"
  "ds_read_b64_tr_b16 a[252:253], v5 offset:24576\n"
  "v_mfma_f32_16x16x32_bf16 v[156:159], a[140:143], a[188:191], v[156:159]\n"
  "v_mfma_f32_16x16x32_bf16 v[160:163], a[160:163], a[144:147], v[160:163]\n"
  "ds_read_b64_tr_b16 a[254:255], v5 offset:25600\n"
  "v_mfma_f32_16x16x32_bf16 v[164:167], a[164:167], a[144:147], v[164:167]\n"
  "v_mfma_f32_16x16x32_bf16 v[176:179], a[168:171], a[144:147], v[176:179]\n"
  "ds_read_b128 a[196:199], v1 offset:1024\n"
  "v_mfma_f32_16x16x32_bf16 v[180:183], a[172:175], a[144:147], v[180:183]\n"
  "v_mfma_f32_16x16x32_bf16 v[192:195], a[144:147], a[176:179], v[192:195]\n"
  "ds_read_b128 a[200:203], v1 offset:4096\n"
  "v_mfma_f32_16x16x32_bf16 v[200:203], a[144:147], a[180:183], v[200:203]\n"
  "v_mfma_f32_16x16x32_bf16 v[208:211], a[144:147], a[184:187], v[208:211]\n"
  "ds_read_b128 a[204:207], v1 offset:5120\n"
  "v_mfma_f32_16x16x32_bf16 v[216:219], a[144:147], a[188:191], v[216:219]\n"
  "v_mfma_f32_16x16x32_bf16 v[168:171], a[160:163], a[148:151], v[168:171]\n"
  "ds_read_b128 a[208:211], v1 offset:8192\n"
  "v_mfma_f32_16x16x32_bf16 v[172:175], a[164:167], a[148:151], v[172:175]\n"
  "v_lshlrev_b32 v31, 3, v30\n"
  "v_mfma_f32_16x16x32_bf16 v[184:187], a[168:171], a[148:151], v[184:187]\n"
  "ds_read_b128 a[212:215], v1 offset:9216\n"
  "v_mfma_f32_16x16x32_bf16 v[188:191], a[172:175], a[148:151], v[188:191]\n"
  "v_mfma_f32_16x16x32_bf16 v[196:199], a[148:151], a[176:179], v[196:199]\n"
  "ds_read_b128 a[216:219], v1 offset:12288\n"
  "v_mfma_f32_16x16x32_bf16 v[204:207], a[148:151], a[180:183], v[204:207]\n"
  "buffer_load_dword a127, v31, s[40:43], s78 offen\n"
  "s_add_u32 s78, s78, 0x2000\n"
  "v_mfma_f32_16x16x32_bf16 v[212:215], a[148:151], a[184:187], v[212:215]\n"
  "ds_read_b128 a[220:223], v1 offset:13312\n"
  "v_mfma_f32_16x16x32_bf16 v[220:223], a[148:151], a[188:191], v[220:223]\n"
  "v_mfma_f32_16x16x32_bf16 v[224:227], a[160:163], a[152:155], v[224:227]\n"
  "v_mfma_f32_16x16x32_bf16 v[228:231], a[164:167], a[152:155], v[228:231]\n"
  "v_mfma_f32_16x16x32_bf16 v[240:243], a[168:171], a[152:155], v[240:243]\n"
  "v_mfma_f32_16x16x32_bf16 v[244:247], a[172:175], a[152:155], v[244:247]\n"
  "v_mfma_f32_16x16x32_bf16 a[0:3], a[152:155], a[176:179], a[0:3]\n"
  "v_mfma_f32_16x16x32_bf16 a[8:11], a[152:155], a[180:183], a[8:11]\n"
  "v_mfma_f32_16x16x32_bf16 a[16:19], a[152:155], a[184:187], a[16:19]\n"
  "v_mfma_f32_16x16x32_bf16 a[24:27], a[152:155], a[188:191], a[24:27]\n"
  "v_mfma_f32_16x16x32_bf16 v[232:235], a[160:163], a[156:159], v[232:235]\n"
  "v_mfma_f32_16x16x32_bf16 v[236:239], a[164:167], a[156:159], v[236:239]\n"
  "v_mfma_f32_16x16x32_bf16 v[248:251], a[168:171], a[156:159], v[248:251]\n"
  "v_mfma_f32_16x16x32_bf16 v[252:255], a[172:175], a[156:159], v[252:255]\n"
  "v_mfma_f32_16x16x32_bf16 a[4:7], a[156:159], a[176:179], a[4:7]\n"
  "v_mfma_f32_16x16x32_bf16 a[12:15], a[156:159], a[180:183], a[12:15]\n"
  "v_mfma_f32_16x16x32_bf16 a[20:23], a[156:159], a[184:187], a[20:23]\n"
  "v_mfma_f32_16x16x32_bf16 a[28:31], a[156:159], a[188:191], a[28:31]\n"
  "s_waitcnt lgkmcnt(0)\n"
  "v_mfma_f32_16x16x32_bf16 v[32:35], a[224:227], a[192:195], v[32:35]\n"
  "v_xor_b32 v0, 0x10000, v0\n"
  "v_xor_b32 v2, 0x10000, v2\n"
  "v_mfma_f32_16x16x32_bf16 v[36:39], a[228:231], a[192:195], v[36:39]\n"
  "v_xor_b32 v3, 0x10000, v3\n"
  "v_xor_b32 v4, 0x10000, v4\n"
  "v_mfma_f32_16x16x32_bf16 v[48:51], a[232:235], a[192:195], v[48:51]\n"
  "v_xor_b32 v5, 0x10000, v5\n"
  "v_xor_b32 v1, 0x10000, v1\n"
  "v_mfma_f32_16x16x32_bf16 v[52:55], a[236:239], a[192:195], v[52:55]\n"
  "s_waitcnt vmcnt(1) lgkmcnt(0)\n"
  "s_barrier\n"
  "v_mfma_f32_16x16x32_bf16 v[64:67], a[192:195], a[240:243], v[64:67]\n"
  "s_mov_b32 s55, 0x280\n"
  "s_mov_b32 s56, 0x280000\n"
  "ds_read_b128 a[128:131], v0 offset:0\n"
  "v_mfma_f32_16x16x32_bf16 v[72:75], a[192:195], a[244:247], v[72:75]\n"
  "s_add_u32 m0, s53, 0x8000\n"
  "v_mfma_f32_16x16x32_bf16 v[80:83], a[192:195], a[248:251], v[80:83]\n"
  "ds_read_b64_tr_b16 a[160:161], v2 offset:0\n"
  "v_mfma_f32_16x16x32_bf16 v[88:91], a[192:195], a[252:255], v[88:91]\n"
  "buffer_load_dwordx4 v14, s[28:31], s56 offen offset:0 lds\n"
  "v_mfma_f32_16x16x32_bf16 v[40:43], a[224:227], a[196:199], v[40:43]\n"
  "ds_read_b64_tr_b16 a[162:163], v2 offset:1024\n"
  "v_mfma_f32_16x16x32_bf16 v[44:47], a[228:231], a[196:199], v[44:47]\n"
  "v_mfma_f32_16x16x32_bf16 v[56:59], a[232:235], a[196:199], v[56:59]\n"
  "ds_read_b64_tr_b16 a[164:165], v3 offset:0\n"
  "v_mfma_f32_16x16x32_bf16 v[60:63], a[236:239], a[196:199], v[60:63]\n"
  "v_mfma_f32_16x16x32_bf16 v[68:71], a[196:199], a[240:243], v[68:71]\n"
  "buffer_load_dwordx4 v15, s[28:31], s56 offen offset:1024 lds\n"
  "ds_read_b64_tr_b16 a[166:167], v3 offset:1024\n"
  "v_mfma_f32_16x16x32_bf16 v[76:79], a[196:199], a[244:247], v[76:79]\n"
  "v_mfma_f32_16x16x32_bf16 v[84:87], a[196:199], a[248:251], v[84:87]\n"
  "ds_read_b64_tr_b16 a[168:169], v4 offset:0\n"
  "v_mfma_f32_16x16x32_bf16 v[92:95], a[196:199], a[252:255], v[92:95]\n"
  "v_mfma_f32_16x16x32_bf16 v[96:99], a[224:227], a[200:203], v[96:99]\n"
  "ds_read_b64_tr_b16 a[170:171], v4 offset:1024\n"
  "v_mfma_f32_16x16x32_bf16 v[100:103], a[228:231], a[200:203], v[100:103]\n"
  "buffer_load_dwordx4 v16, s[28:31], s56 offen offset:2048 lds\n"
  "v_mfma_f32_16x16x32_bf16 v[112:115], a[232:235], a[200:203], v[112:115]\n"
  "ds_read_b64_tr_b16 a[172:173], v5 offset:0\n"
  "v_mfma_f32_16x16x32_bf16 v[116:119], a[236:239], a[200:203], v[116:119]\n"
  "v_mfma_f32_16x16x32_bf16 v[128:131], a[200:203], a[240:243], v[128:131]\n"
  "ds_read_b64_tr_b16 a[174:175], v5 offset:1024\n"
  "v_mfma_f32_16x16x32_bf16 v[136:139], a[200:203], a[244:247], v[136:139]\n"
  "v_mfma_f32_16x16x32_bf16 v[144:147], a[200:203], a[248:251], v[144:147]\n"
  "buffer_load_dwordx4 v17, s[28:31], s56 offen offset:3072 lds\n"
  "ds_read_b64_tr_b16 a[176:177], v2 offset:16384\n"
  "v_mfma_f32_16x16x32_bf16 v[152:155], a[200:203], a[252:255], v[152:155]\n"
  "v_mfma_f32_16x16x32_bf16 v[104:107], a[224:227], a[204:207], v[104:107]\n"
  "ds_read_b64_tr_b16 a[178:179], v2 offset:17408\n"
  "v_mfma_f32_16x16x32_bf16 v[108:111], a[228:231], a[204:207], v[108:111]\n"
  "s_add_u32 m0, s52, 0x0\n"
  "v_mfma_f32_16x16x32_bf16 v[120:123], a[232:235], a[204:207], v[120:123]\n"
  "ds_read_b64_tr_b16 a[180:181], v3 offset:16384\n"
  "v_mfma_f32_16x16x32_bf16 v[124:127], a[236:239], a[204:207], v[124:127]\n"
  "buffer_load_dwordx4 v6, s[24:27], s55 offen offset:0 lds\n"
  "v_mfma_f32_16x16x32_bf16 v[132:135], a[204:207], a[240:243], v[132:135]\n"
  "ds_read_b64_tr_b16 a[182:183], v3 offset:17408\n"
  "v_mfma_f32_16x16x32_bf16 v[140:143], a[204:207], a[244:247], v[140:143]\n"
  "v_mfma_f32_16x16x32_bf16 v[148:151], a[204:207], a[248:251], v[148:151]\n"
  "ds_read_b64_tr_b16 a[184:185], v4 offset:16384\n"
  "v_mfma_f32_16x16x32_bf16 v[156:159], a[204:207], a[252:255], v[156:159]\n"
  "v_mfma_f32_16x16x32_bf16 v[160:163], a[224:227], a[208:211], v[160:163]\n"
  "buffer_load_dwordx4 v7, s[24:27], s55 offen offset:1024 lds\n"
  "ds_read_b64_tr_b16 a[186:187], v4 offset:17408\n"
  "v_mfma_f32_16x16x32_bf16 v[164:167], a[228:231], a[208:211], v[164:167]\n"
  "v_mfma_f32_16x16x32_bf16 v[176:179], a[232:235], a[208:211], v[176:179]\n"
  "ds_read_b64_tr_b16 a[188:189], v5 offset:16384\n"
  "v_mfma_f32_16x16x32_bf16 v[180:183], a[236:239], a[208:211], v[180:183]\n"
  "v_mfma_f32_16x16x32_bf16 v[192:195], a[208:211], a[240:243], v[192:195]\n"
  "ds_read_b64_tr_b16 a[190:191], v5 offset:17408\n"
  "v_mfma_f32_16x16x32_bf16 v[200:203], a[208:211], a[244:247], v[200:203]\n"
  "buffer_load_dwordx4 v8, s[24:27], s55 offen offset:2048 lds\n"
  "v_mfma_f32_16x16x32_bf16 v[208:211], a[208:211], a[248:251], v[208:211]\n"
  "ds_read_b128 a[132:135], v0 offset:1024\n"
  "v_mfma_f32_16x16x32_bf16 v[216:219], a[208:211], a[252:255], v[216:219]\n"
  "v_mfma_f32_16x16x32_bf16 v[168:171], a[224:227], a[212:215], v[168:171]\n"
  "ds_read_b128 a[136:139], v0 offset:4096\n"
  "v_mfma_f32_16x16x32_bf16 v[172:175], a[228:231], a[212:215], v[172:175]\n"
  "v_mfma_f32_16x16x32_bf16 v[184:187], a[232:235], a[212:215], v[184:187]\n"
  "buffer_load_dwordx4 v9, s[24:27], s55 offen offset:3072 lds\n"
  "ds_read_b128 a[140:143], v0 offset:5120\n"
  "v_mfma_f32_16x16x32_bf16 v[188:191], a[236:239], a[212:215], v[188:191]\n"
  "v_mfma_f32_16x16x32_bf16 v[196:199], a[212:215], a[240:243], v[196:199]\n"
  "ds_read_b128 a[144:147], v0 offset:8192\n"
  "v_mfma_f32_16x16x32_bf16 v[204:207], a[212:215], a[244:247], v[204:207]\n"
  "s_add_u32 m0, s53, 0xc000\n"
  "v_mfma_f32_16x16x32_bf16 v[212:215], a[212:215], a[248:251], v[212:215]\n"
  "ds_read_b128 a[148:151], v0 offset:9216\n"
  "v_mfma_f32_16x16x32_bf16 v[220:223], a[212:215], a[252:255], v[220:223]\n"
  "buffer_load_dwordx4 v14, s[32:35], s56 offen offset:0 lds\n"
  "v_mfma_f32_16x16x32_bf16 v[224:227], a[224:227], a[216:219], v[224:227]\n"
  "ds_read_b128 a[152:155], v0 offset:12288\n"
  "v_mfma_f32_16x16x32_bf16 v[228:231], a[228:231], a[216:219], v[228:231]\n"
  "v_mfma_f32_16x16x32_bf16 v[240:243], a[232:235], a[216:219], v[240:243]\n"
  "ds_read_b128 a[156:159], v0 offset:13312\n"
  "v_mfma_f32_16x16x32_bf16 v[244:247], a[236:239], a[216:219], v[244:247]\n"
  "v_mfma_f32_16x16x32_bf16 a[0:3], a[216:219], a[240:243], a[0:3]\n"
  "buffer_load_dwordx4 v15, s[32:35], s56 offen offset:1024 lds\n"
  "v_mfma_f32_16x16x32_bf16 a[8:11], a[216:219], a[244:247], a[8:11]\n"
  "v_mfma_f32_16x16x32_bf16 a[16:19], a[216:219], a[248:251], a[16:19]\n"
  "v_mfma_f32_16x16x32_bf16 a[24:27], a[216:219], a[252:255], a[24:27]\n"
  "v_mfma_f32_16x16x32_bf16 v[232:235], a[224:227], a[220:223], v[232:235]\n"
  "v_mfma_f32_16x16x32_bf16 v[236:239], a[228:231], a[220:223], v[236:239]\n"
  "buffer_load_dwordx4 v16, s[32:35], s56 offen offset:2048 lds\n"
  "v_mfma_f32_16x16x32_bf16 v[248:251], a[232:235], a[220:223], v[248:251]\n"
  "v_mfma_f32_16x16x32_bf16 v[252:255], a[236:239], a[220:223], v[252:255]\n"
  "v_mfma_f32_16x16x32_bf16 a[4:7], a[220:223], a[240:243], a[4:7]\n"
  "v_mfma_f32_16x16x32_bf16 a[12:15], a[220:223], a[244:247], a[12:15]\n"
  "v_mfma_f32_16x16x32_bf16 a[20:23], a[220:223], a[248:251], a[20:23]\n"
  "buffer_load_dwordx4 v17, s[32:35], s56 offen offset:3072 lds\n"
  "v_mfma_f32_16x16x32_bf16 a[28:31], a[220:223], a[252:255], a[28:31]\n"
  "s_waitcnt lgkmcnt(0)\n"
  "v_mfma_f32_16x16x32_bf16 v[32:35], a[160:163], a[128:131], v[32:35]\n"
  "ds_read_b128 a[192:195], v1 offset:0\n"
  "v_mfma_f32_16x16x32_bf16 v[36:39], a[164:167], a[128:131], v[36:39]\n"
  "s_add_u32 m0, s52, 0x1000\n"
  "v_mfma_f32_16x16x32_bf16 v[48:51], a[168:171], a[128:131], v[48:51]\n"
  "ds_read_b64_tr_b16 a[224:225], v2 offset:8192\n"
  "v_mfma_f32_16x16x32_bf16 v[52:55], a[172:175], a[128:131], v[52:55]\n"
  "buffer_load_dwordx4 v10, s[24:27], s55 offen offset:0 lds\n"
  "v_mfma_f32_16x16x32_bf16 v[64:67], a[128:131], a[176:179], v[64:67]\n"
  "ds_read_b64_tr_b16 a[226:227], v2 offset:9216\n"
  "v_mfma_f32_16x16x32_bf16 v[72:75], a[128:131], a[180:183], v[72:75]\n"
  "v_mfma_f32_16x16x32_bf16 v[80:83], a[128:131], a[184:187], v[80:83]\n"
  "ds_read_b64_tr_b16 a[228:229], v3 offset:8192\n"
  "v_mfma_f32_16x16x32_bf16 v[88:91], a[128:131], a[188:191], v[88:91]\n"
  "v_mfma_f32_16x16x32_bf16 v[40:43], a[160:163], a[132:135], v[40:43]\n"
  "buffer_load_dwordx4 v11, s[24:27], s55 offen offset:1024 lds\n"
  "ds_read_b64_tr_b16 a[230:231], v3 offset:9216\n"
  "v_mfma_f32_16x16x32_bf16 v[44:47], a[164:167], a[132:135], v[44:47]\n"
  "v_mfma_f32_16x16x32_bf16 v[56:59], a[168:171], a[132:135], v[56:59]\n"
  "ds_read_b64_tr_b16 a[232:233], v4 offset:8192\n"
  "v_mfma_f32_16x16x32_bf16 v[60:63], a[172:175], a[132:135], v[60:63]\n"
  "v_mfma_f32_16x16x32_bf16 v[68:71], a[132:135], a[176:179], v[68:71]\n"
  "ds_read_b64_tr_b16 a[234:235], v4 offset:9216\n"
  "v_mfma_f32_16x16x32_bf16 v[76:79], a[132:135], a[180:183], v[76:79]\n"
  "buffer_load_dwordx4 v12, s[24:27], s55 offen offset:2048 lds\n"
  "v_mfma_f32_16x16x32_bf16 v[84:87], a[132:135], a[184:187], v[84:87]\n"
  "ds_read_b64_tr_b16 a[236:237], v5 offset:8192\n"
  "v_mfma_f32_16x16x32_bf16 v[92:95], a[132:135], a[188:191], v[92:95]\n"
  "v_mfma_f32_16x16x32_bf16 v[96:99], a[160:163], a[136:139], v[96:99]\n"
  "ds_read_b64_tr_b16 a[238:239], v5 offset:9216\n"
  "v_mfma_f32_16x16x32_bf16 v[100:103], a[164:167], a[136:139], v[100:103]\n"
  "v_mfma_f32_16x16x32_bf16 v[112:115], a[168:171], a[136:139], v[112:115]\n"
  "buffer_load_dwordx4 v13, s[24:27], s55 offen offset:3072 lds\n"
  "ds_read_b64_tr_b16 a[240:241], v2 offset:24576\n"
  "v_mfma_f32_16x16x32_bf16 v[116:119], a[172:175], a[136:139], v[116:119]\n"
  "v_mfma_f32_16x16x32_bf16 v[128:131], a[136:139], a[176:179], v[128:131]\n"
  "ds_read_b64_tr_b16 a[242:243], v2 offset:25600\n"
  "v_mfma_f32_16x16x32_bf16 v[136:139], a[136:139], a[180:183], v[136:139]\n"
  "v_mfma_f32_16x16x32_bf16 v[144:147], a[136:139], a[184:187], v[144:147]\n"
  "ds_read_b64_tr_b16 a[244:245], v3 offset:24576\n"
  "v_mfma_f32_16x16x32_bf16 v[152:155], a[136:139], a[188:191], v[152:155]\n"
  "v_mfma_f32_16x16x32_bf16 v[104:107], a[160:163], a[140:143], v[104:107]\n"
  "ds_read_b64_tr_b16 a[246:247], v3 offset:25600\n"
  "v_mfma_f32_16x16x32_bf16 v[108:111], a[164:167], a[140:143], v[108:111]\n"
  "v_mfma_f32_16x16x32_bf16 v[120:123], a[168:171], a[140:143], v[120:123]\n"
  "ds_read_b64_tr_b16 a[248:249], v4 offset:24576\n"
  "v_mfma_f32_16x16x32_bf16 v[124:127], a[172:175], a[140:143], v[124:127]\n"
  "v_mfma_f32_16x16x32_bf16 v[132:135], a[140:143], a[176:179], v[132:135]\n"
  "ds_read_b64_tr_b16 a[250:251], v4 offset:25600\n"
  "v_mfma_f32_16x16x32_bf16 v[140:143], a[140:143], a[180:183], v[140:143]\n"
  "v_mfma_f32_16x16x32_bf16 v[148:151], a[140:143], a[184:187], v[148:151]\n"
  "ds_read_b64_tr_b16 a[252:253], v5 offset:24576\n"
  "v_mfma_f32_16x16x32_bf16 v[156:159], a[140:143], a[188:191], v[156:159]\n"
  "v_mfma_f32_16x16x32_bf16 v[160:163], a[160:163], a[144:147], v[160:163]\n"
  "ds_read_b64_tr_b16 a[254:255], v5 offset:25600\n"
  "v_mfma_f32_16x16x32_bf16 v[164:167], a[164:167], a[144:147], v[164:167]\n"
  "v_mfma_f32_16x16x32_bf16 v[176:179], a[168:171], a[144:147], v[176:179]\n"
  "ds_read_b128 a[196:199], v1 offset:1024\n"
  "v_mfma_f32_16x16x32_bf16 v[180:183], a[172:175], a[144:147], v[180:183]\n"
  "v_mfma_f32_16x16x32_bf16 v[192:195], a[144:147], a[176:179], v[192:195]\n"
  "ds_read_b128 a[200:203], v1 offset:4096\n"
  "v_mfma_f32_16x16x32_bf16 v[200:203], a[144:147], a[180:183], v[200:203]\n"
  "v_mfma_f32_16x16x32_bf16 v[208:211], a[144:147], a[184:187], v[208:211]\n"
  "ds_read_b128 a[204:207], v1 offset:5120\n"
  "v_mfma_f32_16x16x32_bf16 v[216:219], a[144:147], a[188:191], v[216:219]\n"
  "v_mfma_f32_16x16x32_bf16 v[168:171], a[160:163], a[148:151], v[168:171]\n"
  "ds_read_b128 a[208:211], v1 offset:8192\n"
  "v_mfma_f32_16x16x32_bf16 v[172:175], a[164:167], a[148:151], v[172:175]\n"
  "v_lshlrev_b32 v31, 3, v30\n"
  "v_mfma_f32_16x16x32_bf16 v[184:187], a[168:171], a[148:151], v[184:187]\n"
  "ds_read_b128 a[212:215], v1 offset:9216\n"
  "v_mfma_f32_16x16x32_bf16 v[188:191], a[172:175], a[148:151], v[188:191]\n"
  "v_mfma_f32_16x16x32_bf16 v[196:199], a[148:151], a[176:179], v[196:199]\n"
  "ds_read_b128 a[216:219], v1 offset:12288\n"
  "v_mfma_f32_16x16x32_bf16 v[204:207], a[148:151], a[180:183], v[204:207]\n"
  "buffer_load_dword a127, v31, s[40:43], s78 offen\n"
  "s_add_u32 s78, s78, 0x2000\n"
  "v_mfma_f32_16x16x32_bf16 v[212:215], a[148:151], a[184:187], v[212:215]\n"
  "ds_read_b128 a[220:223], v1 offset:13312\n"
  "v_mfma_f32_16x16x32_bf16 v[220:223], a[148:151], a[188:191], v[220:223]\n"
  "v_mfma_f32_16x16x32_bf16 v[224:227], a[160:163], a[152:155], v[224:227]\n"
  "v_mfma_f32_16x16x32_bf16 v[228:231], a[164:167], a[152:155], v[228:231]\n"
  "v_mfma_f32_16x16x32_bf16 v[240:243], a[168:171], a[152:155], v[240:243]\n"
  "v_mfma_f32_16x16x32_bf16 v[244:247], a[172:175], a[152:155], v[244:247]\n"
  "v_mfma_f32_16x16x32_bf16 a[0:3], a[152:155], a[176:179], a[0:3]\n"
  "v_mfma_f32_16x16x32_bf16 a[8:11], a[152:155], a[180:183], a[8:11]\n"
  "v_mfma_f32_16x16x32_bf16 a[16:19], a[152:155], a[184:187], a[16:19]\n"
  "v_mfma_f32_16x16x32_bf16 a[24:27], a[152:155], a[188:191], a[24:27]\n"
  "v_mfma_f32_16x16x32_bf16 v[232:235], a[160:163], a[156:159], v[232:235]\n"
  "v_mfma_f32_16x16x32_bf16 v[236:239], a[164:167], a[156:159], v[236:239]\n"
  "v_mfma_f32_16x16x32_bf16 v[248:251], a[168:171], a[156:159], v[248:251]\n"
  "v_mfma_f32_16x16x32_bf16 v[252:255], a[172:175], a[156:159], v[252:255]\n"
  "v_mfma_f32_16x16x32_bf16 a[4:7], a[156:159], a[176:179], a[4:7]\n"
  "v_mfma_f32_16x16x32_bf16 a[12:15], a[156:159], a[180:183], a[12:15]\n"
  "v_mfma_f32_16x16x32_bf16 a[20:23], a[156:159], a[184:187], a[20:23]\n"
  "v_mfma_f32_16x16x32_bf16 a[28:31], a[156:159], a[188:191], a[28:31]\n"
  "s_waitcnt lgkmcnt(0)\n"
  "v_mfma_f32_16x16x32_bf16 v[32:35], a[224:227], a[192:195], v[32:35]\n"
  "v_xor_b32 v0, 0x10000, v0\n"
  "v_xor_b32 v2, 0x10000, v2\n"
  "v_mfma_f32_16x16x32_bf16 v[36:39], a[228:231], a[192:195], v[36:39]\n"
  "v_xor_b32 v3, 0x10000, v3\n"
  "v_xor_b32 v4, 0x10000, v4\n"
  "v_mfma_f32_16x16x32_bf16 v[48:51], a[232:235], a[192:195], v[48:51]\n"
  "v_xor_b32 v5, 0x10000, v5\n"
  "v_xor_b32 v1, 0x10000, v1\n"
  "v_mfma_f32_16x16x32_bf16 v[52:55], a[236:239], a[192:195], v[52:55]\n"
  "s_waitcnt vmcnt(1) lgkmcnt(0)\n"
  "s_barrier\n"
  "v_mfma_f32_16x16x32_bf16 v[64:67], a[192:195], a[240:243], v[64:67]\n"
  "s_mov_b32 s55, 0x300\n"
  "s_mov_b32 s56, 0x300000\n"
  "ds_read_b128 a[128:131], v0 offset:0\n"
  "v_mfma_f32_16x16x32_bf16 v[72:75], a[192:195], a[244:247], v[72:75]\n"
  "s_add_u32 m0, s53, 0x18000\n"
  "v_mfma_f32_16x16x32_bf16 v[80:83], a[192:195], a[248:251], v[80:83]\n"
  "ds_read_b64_tr_b16 a[160:161], v2 offset:0\n"
  "v_mfma_f32_16x16x32_bf16 v[88:91], a[192:195], a[252:255], v[88:91]\n"
  "buffer_load_dwordx4 v14, s[28:31], s56 offen offset:0 lds\n"
  "v_mfma_f32_16x16x32_bf16 v[40:43], a[224:227], a[196:199], v[40:43]\n"
  "ds_read_b64_tr_b16 a[162:163], v2 offset:1024\n"
  "v_mfma_f32_16x16x32_bf16 v[44:47], a[228:231], a[196:199], v[44:47]\n"
  "v_mfma_f32_16x16x32_bf16 v[56:59], a[232:235], a[196:199], v[56:59]\n"
  "ds_read_b64_tr_b16 a[164:165], v3 offset:0\n"
  "v_mfma_f32_16x16x32_bf16 v[60:63], a[236:239], a[196:199], v[60:63]\n"
  "v_mfma_f32_16x16x32_bf16 v[68:71], a[196:199], a[240:243], v[68:71]\n"
  "buffer_load_dwordx4 v15, s[28:31], s56 offen offset:1024 lds\n"
  "ds_read_b64_tr_b16 a[166:167], v3 offset:1024\n"
  "v_mfma_f32_16x16x32_bf16 v[76:79], a[196:199], a[244:247], v[76:79]\n"
  "v_mfma_f32_16x16x32_bf16 v[84:87], a[196:199], a[248:251], v[84:87]\n"
  "ds_read_b64_tr_b16 a[168:169], v4 offset:0\n"
  "v_mfma_f32_16x16x32_bf16 v[92:95], a[196:199], a[252:255], v[92:95]\n"
  "v_mfma_f32_16x16x32_bf16 v[96:99], a[224:227], a[200:203], v[96:99]\n"
  "ds_read_b64_tr_b16 a[170:171], v4 offset:1024\n"
  "v_mfma_f32_16x16x32_bf16 v[100:103], a[228:231], a[200:203], v[100:103]\n"
  "buffer_load_dwordx4 v16, s[28:31], s56 offen offset:2048 lds\n"
  "v_mfma_f32_16x16x32_bf16 v[112:115], a[232:235], a[200:203], v[112:115]\n"
  "ds_read_b64_tr_b16 a[172:173], v5 offset:0\n"
  "v_mfma_f32_16x16x32_bf16 v[116:119], a[236:239], a[200:203], v[116:119]\n"
  "v_mfma_f32_16x16x32_bf16 v[128:131], a[200:203], a[240:243], v[128:131]\n"
  "ds_read_b64_tr_b16 a[174:175], v5 offset:1024\n"
  "v_mfma_f32_16x16x32_bf16 v[136:139], a[200:203], a[244:247], v[136:139]\n"
  "v_mfma_f32_16x16x32_bf16 v[144:147], a[200:203], a[248:251], v[144:147]\n"
  "buffer_load_dwordx4 v17, s[28:31], s56 offen offset:3072 lds\n"
  "ds_read_b64_tr_b16 a[176:177], v2 offset:16384\n"
  "v_mfma_f32_16x16x32_bf16 v[152:155], a[200:203], a[252:255], v[152:155]\n"
  "v_mfma_f32_16x16x32_bf16 v[104:107], a[224:227], a[204:207], v[104:107]\n"
  "ds_read_b64_tr_b16 a[178:179], v2 offset:17408\n"
  "v_mfma_f32_16x16x32_bf16 v[108:111], a[228:231], a[204:207], v[108:111]\n"
  "s_add_u32 m0, s52, 0x10000\n"
  "v_mfma_f32_16x16x32_bf16 v[120:123], a[232:235], a[204:207], v[120:123]\n"
  "ds_read_b64_tr_b16 a[180:181], v3 offset:16384\n"
  "v_mfma_f32_16x16x32_bf16 v[124:127], a[236:239], a[204:207], v[124:127]\n"
  "buffer_load_dwordx4 v6, s[24:27], s55 offen offset:0 lds\n"
  "v_mfma_f32_16x16x32_bf16 v[132:135], a[204:207], a[240:243], v[132:135]\n"
  "ds_read_b64_tr_b16 a[182:183], v3 offset:17408\n"
  "v_mfma_f32_16x16x32_bf16 v[140:143], a[204:207], a[244:247], v[140:143]\n"
  "v_mfma_f32_16x16x32_bf16 v[148:151], a[204:207], a[248:251], v[148:151]\n"
  "ds_read_b64_tr_b16 a[184:185], v4 offset:16384\n"
  "v_mfma_f32_16x16x32_bf16 v[156:159], a[204:207], a[252:255], v[156:159]\n"
  "v_mfma_f32_16x16x32_bf16 v[160:163], a[224:227], a[208:211], v[160:163]\n"
  "buffer_load_dwordx4 v7, s[24:27], s55 offen offset:1024 lds\n"
  "ds_read_b64_tr_b16 a[186:187], v4 offset:17408\n"
  "v_mfma_f32_16x16x32_bf16 v[164:167], a[228:231], a[208:211], v[164:167]\n"
  "v_mfma_f32_16x16x32_bf16 v[176:179], a[232:235], a[208:211], v[176:179]\n"
  "ds_read_b64_tr_b16 a[188:189], v5 offset:16384\n"
  "v_mfma_f32_16x16x32_bf16 v[180:183], a[236:239], a[208:211], v[180:183]\n"
  "v_mfma_f32_16x16x32_bf16 v[192:195], a[208:211], a[240:243], v[192:195]\n"
  "ds_read_b64_tr_b16 a[190:191], v5 offset:17408\n"
  "v_mfma_f32_16x16x32_bf16 v[200:203], a[208:211], a[244:247], v[200:203]\n"
  "buffer_load_dwordx4 v8, s[24:27], s55 offen offset:2048 lds\n"
  "v_mfma_f32_16x16x32_bf16 v[208:211], a[208:211], a[248:251], v[208:211]\n"
  "ds_read_b128 a[132:135], v0 offset:1024\n"
  "v_mfma_f32_16x16x32_bf16 v[216:219], a[208:211], a[252:255], v[216:219]\n"
  "v_mfma_f32_16x16x32_bf16 v[168:171], a[224:227], a[212:215], v[168:171]\n"
  "ds_read_b128 a[136:139], v0 offset:4096\n"
  "v_mfma_f32_16x16x32_bf16 v[172:175], a[228:231], a[212:215], v[172:175]\n"
  "v_mfma_f32_16x16x32_bf16 v[184:187], a[232:235], a[212:215], v[184:187]\n"
  "buffer_load_dwordx4 v9, s[24:27], s55 offen offset:3072 lds\n"
  "ds_read_b128 a[140:143], v0 offset:5120\n"
  "v_mfma_f32_16x16x32_bf16 v[188:191], a[236:239], a[212:215], v[188:191]\n"
  "v_mfma_f32_16x16x32_bf16 v[196:199], a[212:215], a[240:243], v[196:199]\n"
  "ds_read_b128 a[144:147], v0 offset:8192\n"
  "v_mfma_f32_16x16x32_bf16 v[204:207], a[212:215], a[244:247], v[204:207]\n"
  "s_add_u32 m0, s53, 0x1c000\n"
  "v_mfma_f32_16x16x32_bf16 v[212:215], a[212:215], a[248:251], v[212:215]\n"
  "ds_read_b128 a[148:151], v0 offset:9216\n"
  "v_mfma_f32_16x16x32_bf16 v[220:223], a[212:215], a[252:255], v[220:223]\n"
  "buffer_load_dwordx4 v14, s[32:35], s56 offen offset:0 lds\n"
  "v_mfma_f32_16x16x32_bf16 v[224:227], a[224:227], a[216:219], v[224:227]\n"
  "ds_read_b128 a[152:155], v0 offset:12288\n"
  "v_mfma_f32_16x16x32_bf16 v[228:231], a[228:231], a[216:219], v[228:231]\n"
  "v_mfma_f32_16x16x32_bf16 v[240:243], a[232:235], a[216:219], v[240:243]\n"
  "ds_read_b128 a[156:159], v0 offset:13312\n"
  "v_mfma_f32_16x16x32_bf16 v[244:247], a[236:239], a[216:219], v[244:247]\n"
  "v_mfma_f32_16x16x32_bf16 a[0:3], a[216:219], a[240:243], a[0:3]\n"
  "buffer_load_dwordx4 v15, s[32:35], s56 offen offset:1024 lds\n"
  "v_mfma_f32_16x16x32_bf16 a[8:11], a[216:219], a[244:247], a[8:11]\n"
  "v_mfma_f32_16x16x32_bf16 a[16:19], a[216:219], a[248:251], a[16:19]\n"
  "v_mfma_f32_16x16x32_bf16 a[24:27], a[216:219], a[252:255], a[24:27]\n"
  "v_mfma_f32_16x16x32_bf16 v[232:235], a[224:227], a[220:223], v[232:235]\n"
  "v_mfma_f32_16x16x32_bf16 v[236:239], a[228:231], a[220:223], v[236:239]\n"
  "buffer_load_dwordx4 v16, s[32:35], s56 offen offset:2048 lds\n"
  "v_mfma_f32_16x16x32_bf16 v[248:251], a[232:235], a[220:223], v[248:251]\n"
  "v_mfma_f32_16x16x32_bf16 v[252:255], a[236:239], a[220:223], v[252:255]\n"
  "v_mfma_f32_16x16x32_bf16 a[4:7], a[220:223], a[240:243], a[4:7]\n"
  "v_mfma_f32_16x16x32_bf16 a[12:15], a[220:223], a[244:247], a[12:15]\n"
  "v_mfma_f32_16x16x32_bf16 a[20:23], a[220:223], a[248:251], a[20:23]\n"
  "buffer_load_dwordx4 v17, s[32:35], s56 offen offset:3072 lds\n"
  "v_mfma_f32_16x16x32_bf16 a[28:31], a[220:223], a[252:255], a[28:31]\n"
  "s_waitcnt lgkmcnt(0)\n"
  "v_mfma_f32_16x16x32_bf16 v[32:35], a[160:163], a[128:131], v[32:35]\n"
  "ds_read_b128 a[192:195], v1 offset:0\n"
  "v_mfma_f32_16x16x32_bf16 v[36:39], a[164:167], a[128:131], v[36:39]\n"
  "s_add_u32 m0, s52, 0x11000\n"
  "v_mfma_f32_16x16x32_bf16 v[48:51], a[168:171], a[128:131], v[48:51]\n"
  "ds_read_b64_tr_b16 a[224:225], v2 offset:8192\n"
  "v_mfma_f32_16x16x32_bf16 v[52:55], a[172:175], a[128:131], v[52:55]\n"
  "buffer_load_dwordx4 v10, s[24:27], s55 offen offset:0 lds\n"
  "v_mfma_f32_16x16x32_bf16 v[64:67], a[128:131], a[176:179], v[64:67]\n"
  "ds_read_b64_tr_b16 a[226:227], v2 offset:9216\n"
  "v_mfma_f32_16x16x32_bf16 v[72:75], a[128:131], a[180:183], v[72:75]\n"
  "v_mfma_f32_16x16x32_bf16 v[80:83], a[128:131], a[184:187], v[80:83]\n"
  "ds_read_b64_tr_b16 a[228:229], v3 offset:8192\n"
  "v_mfma_f32_16x16x32_bf16 v[88:91], a[128:131], a[188:191], v[88:91]\n"
  "v_mfma_f32_16x16x32_bf16 v[40:43], a[160:163], a[132:135], v[40:43]\n"
  "buffer_load_dwordx4 v11, s[24:27], s55 offen offset:1024 lds\n"
  "ds_read_b64_tr_b16 a[230:231], v3 offset:9216\n"
  "v_mfma_f32_16x16x32_bf16 v[44:47], a[164:167], a[132:135], v[44:47]\n"
  "v_mfma_f32_16x16x32_bf16 v[56:59], a[168:171], a[132:135], v[56:59]\n"
  "ds_read_b64_tr_b16 a[232:233], v4 offset:8192\n"
  "v_mfma_f32_16x16x32_bf16 v[60:63], a[172:175], a[132:135], v[60:63]\n"
  "v_mfma_f32_16x16x32_bf16 v[68:71], a[132:135], a[176:179], v[68:71]\n"
  "ds_read_b64_tr_b16 a[234:235], v4 offset:9216\n"
  "v_mfma_f32_16x16x32_bf16 v[76:79], a[132:135], a[180:183], v[76:79]\n"
  "buffer_load_dwordx4 v12, s[24:27], s55 offen offset:2048 lds\n"
  "v_mfma_f32_16x16x32_bf16 v[84:87], a[132:135], a[184:187], v[84:87]\n"
  "ds_read_b64_tr_b16 a[236:237], v5 offset:8192\n"
  "v_mfma_f32_16x16x32_bf16 v[92:95], a[132:135], a[188:191], v[92:95]\n"
  "v_mfma_f32_16x16x32_bf16 v[96:99], a[160:163], a[136:139], v[96:99]\n"
  "ds_read_b64_tr_b16 a[238:239], v5 offset:9216\n"
  "v_mfma_f32_16x16x32_bf16 v[100:103], a[164:167], a[136:139], v[100:103]\n"
  "v_mfma_f32_16x16x32_bf16 v[112:115], a[168:171], a[136:139], v[112:115]\n"
  "buffer_load_dwordx4 v13, s[24:27], s55 offen offset:3072 lds\n"
  "ds_read_b64_tr_b16 a[240:241], v2 offset:24576\n"
  "v_mfma_f32_16x16x32_bf16 v[116:119], a[172:175], a[136:139], v[116:119]\n"
  "v_mfma_f32_16x16x32_bf16 v[128:131], a[136:139], a[176:179], v[128:131]\n"
  "ds_read_b64_tr_b16 a[242:243], v2 offset:25600\n"
  "v_mfma_f32_16x16x32_bf16 v[136:139], a[136:139], a[180:183], v[136:139]\n"
  "v_mfma_f32_16x16x32_bf16 v[144:147], a[136:139], a[184:187], v[144:147]\n"
  "ds_read_b64_tr_b16 a[244:245], v3 offset:24576\n"
  "v_mfma_f32_16x16x32_bf16 v[152:155], a[136:139], a[188:191], v[152:155]\n"
  "v_mfma_f32_16x16x32_bf16 v[104:107], a[160:163], a[140:143], v[104:107]\n"
  "ds_read_b64_tr_b16 a[246:247], v3 offset:25600\n"
  "v_mfma_f32_16x16x32_bf16 v[108:111], a[164:167], a[140:143], v[108:111]\n"
  "v_mfma_f32_16x16x32_bf16 v[120:123], a[168:171], a[140:143], v[120:123]\n"
  "ds_read_b64_tr_b16 a[248:249], v4 offset:24576\n"
  "v_mfma_f32_16x16x32_bf16 v[124:127], a[172:175], a[140:143], v[124:127]\n"
  "v_mfma_f32_16x16x32_bf16 v[132:135], a[140:143], a[176:179], v[132:135]\n"
  "ds_read_b64_tr_b16 a[250:251], v4 offset:25600\n"
  "v_mfma_f32_16x16x32_bf16 v[140:143], a[140:143], a[180:183], v[140:143]\n"
  "v_mfma_f32_16x16x32_bf16 v[148:151], a[140:143], a[184:187], v[148:151]\n"
  "ds_read_b64_tr_b16 a[252:253], v5 offset:24576\n"
  "v_mfma_f32_16x16x32_bf16 v[156:159], a[140:143], a[188:191], v[156:159]\n"
  "v_mfma_f32_16x16x32_bf16 v[160:163], a[160:163], a[144:147], v[160:163]\n"
  "ds_read_b64_tr_b16 a[254:255], v5 offset:25600\n"
  "v_mfma_f32_16x16x32_bf16 v[164:167], a[164:167], a[144:147], v[164:167]\n"
  "v_mfma_f32_16x16x32_bf16 v[176:179], a[168:171], a[144:147], v[176:179]\n"
  "ds_read_b128 a[196:199], v1 offset:1024\n"
  "v_mfma_f32_16x16x32_bf16 v[180:183], a[172:175], a[144:147], v[180:183]\n"
  "v_mfma_f32_16x16x32_bf16 v[192:195], a[144:147], a[176:179], v[192:195]\n"
  "ds_read_b128 a[200:203], v1 offset:4096\n"
  "v_mfma_f32_16x16x32_bf16 v[200:203], a[144:147], a[180:183], v[200:203]\n"
  "v_mfma_f32_16x16x32_bf16 v[208:211], a[144:147], a[184:187], v[208:211]\n"
  "ds_read_b128 a[204:207], v1 offset:5120\n"
  "v_mfma_f32_16x16x32_bf16 v[216:219], a[144:147], a[188:191], v[216:219]\n"
  "v_mfma_f32_16x16x32_bf16 v[168:171], a[160:163], a[148:151], v[168:171]\n"
  "ds_read_b128 a[208:211], v1 offset:8192\n"
  "v_mfma_f32_16x16x32_bf16 v[172:175], a[164:167], a[148:151], v[172:175]\n"
  "v_accvgpr_read_b32 v31, a120\n"
  "v_mfma_f32_16x16x32_bf16 v[184:187], a[168:171], a[148:151], v[184:187]\n"
  "ds_read_b128 a[212:215], v1 offset:9216\n"
  "v_mfma_f32_16x16x32_bf16 v[188:191], a[172:175], a[148:151], v[188:191]\n"
  "v_mfma_f32_16x16x32_bf16 v[196:199], a[148:151], a[176:179], v[196:199]\n"
  "ds_read_b128 a[216:219], v1 offset:12288\n"
  "v_mfma_f32_16x16x32_bf16 v[204:207], a[148:151], a[180:183], v[204:207]\n"
  "buffer_load_dwordx4 a[108:111], v31, s[84:87], 0 offen\n"
  "v_mfma_f32_16x16x32_bf16 v[212:215], a[148:151], a[184:187], v[212:215]\n"
  "ds_read_b128 a[220:223], v1 offset:13312\n"
  "v_mfma_f32_16x16x32_bf16 v[220:223], a[148:151], a[188:191], v[220:223]\n"
  "v_mfma_f32_16x16x32_bf16 v[224:227], a[160:163], a[152:155], v[224:227]\n"
  "v_mfma_f32_16x16x32_bf16 v[228:231], a[164:167], a[152:155], v[228:231]\n"
  "v_accvgpr_read_b32 v31, a121\n"
  "v_mfma_f32_16x16x32_bf16 v[240:243], a[168:171], a[152:155], v[240:243]\n"
  "v_mfma_f32_16x16x32_bf16 v[244:247], a[172:175], a[152:155], v[244:247]\n"
  "v_mfma_f32_16x16x32_bf16 a[0:3], a[152:155], a[176:179], a[0:3]\n"
  "v_mfma_f32_16x16x32_bf16 a[8:11], a[152:155], a[180:183], a[8:11]\n"
  "buffer_load_dwordx4 a[112:115], v31, s[84:87], 0 offen\n"
  "v_mfma_f32_16x16x32_bf16 a[16:19], a[152:155], a[184:187], a[16:19]\n"
  "v_mfma_f32_16x16x32_bf16 a[24:27], a[152:155], a[188:191], a[24:27]\n"
  "v_mfma_f32_16x16x32_bf16 v[232:235], a[160:163], a[156:159], v[232:235]\n"
  "v_mfma_f32_16x16x32_bf16 v[236:239], a[164:167], a[156:159], v[236:239]\n"
  "v_accvgpr_read_b32 v31, a122\n"
  "v_mfma_f32_16x16x32_bf16 v[248:251], a[168:171], a[156:159], v[248:251]\n"
  "v_mfma_f32_16x16x32_bf16 v[252:255], a[172:175], a[156:159], v[252:255]\n"
  "v_mfma_f32_16x16x32_bf16 a[4:7], a[156:159], a[176:179], a[4:7]\n"
  "v_mfma_f32_16x16x32_bf16 a[12:15], a[156:159], a[180:183], a[12:15]\n"
  "buffer_load_dwordx4 a[116:119], v31, s[84:87], 0 offen\n"
  "v_mfma_f32_16x16x32_bf16 a[20:23], a[156:159], a[184:187], a[20:23]\n"
  "v_mfma_f32_16x16x32_bf16 a[28:31], a[156:159], a[188:191], a[28:31]\n"
  "s_waitcnt lgkmcnt(0)\n"
  "v_mfma_f32_16x16x32_bf16 v[32:35], a[224:227], a[192:195], v[32:35]\n"
  "v_xor_b32 v0, 0x10000, v0\n"
  "v_xor_b32 v2, 0x10000, v2\n"
  "v_mfma_f32_16x16x32_bf16 v[36:39], a[228:231], a[192:195], v[36:39]\n"
  "v_xor_b32 v3, 0x10000, v3\n"
  "v_xor_b32 v4, 0x10000, v4\n"
  "v_mfma_f32_16x16x32_bf16 v[48:51], a[232:235], a[192:195], v[48:51]\n"
  "v_xor_b32 v5, 0x10000, v5\n"
  "v_xor_b32 v1, 0x10000, v1\n"
  "v_mfma_f32_16x16x32_bf16 v[52:55], a[236:239], a[192:195], v[52:55]\n"
  "s_waitcnt vmcnt(3) lgkmcnt(0)\n"
  "s_barrier\n"
  "v_mfma_f32_16x16x32_bf16 v[64:67], a[192:195], a[240:243], v[64:67]\n"
  "s_mov_b32 s55, 0x380\n"
  "s_mov_b32 s56, 0x380000\n"
  "ds_read_b128 a[128:131], v0 offset:0\n"
  "v_mfma_f32_16x16x32_bf16 v[72:75], a[192:195], a[244:247], v[72:75]\n"
  "s_add_u32 m0, s53, 0x8000\n"
  "v_mfma_f32_16x16x32_bf16 v[80:83], a[192:195], a[248:251], v[80:83]\n"
  "ds_read_b64_tr_b16 a[160:161], v2 offset:0\n"
  "v_mfma_f32_16x16x32_bf16 v[88:91], a[192:195], a[252:255], v[88:91]\n"
  "buffer_load_dwordx4 v14, s[28:31], s56 offen offset:0 lds\n"
  "v_mfma_f32_16x16x32_bf16 v[40:43], a[224:227], a[196:199], v[40:43]\n"
  "ds_read_b64_tr_b16 a[162:163], v2 offset:1024\n"
  "v_mfma_f32_16x16x32_bf16 v[44:47], a[228:231], a[196:199], v[44:47]\n"
  "v_mfma_f32_16x16x32_bf16 v[56:59], a[232:235], a[196:199], v[56:59]\n"
  "ds_read_b64_tr_b16 a[164:165], v3 offset:0\n"
  "v_mfma_f32_16x16x32_bf16 v[60:63], a[236:239], a[196:199], v[60:63]\n"
  "v_mfma_f32_16x16x32_bf16 v[68:71], a[196:199], a[240:243], v[68:71]\n"
  "buffer_load_dwordx4 v15, s[28:31], s56 offen offset:1024 lds\n"
  "ds_read_b64_tr_b16 a[166:167], v3 offset:1024\n"
  "v_mfma_f32_16x16x32_bf16 v[76:79], a[196:199], a[244:247], v[76:79]\n"
  "v_mfma_f32_16x16x32_bf16 v[84:87], a[196:199], a[248:251], v[84:87]\n"
  "ds_read_b64_tr_b16 a[168:169], v4 offset:0\n"
  "v_mfma_f32_16x16x32_bf16 v[92:95], a[196:199], a[252:255], v[92:95]\n"
  "v_mfma_f32_16x16x32_bf16 v[96:99], a[224:227], a[200:203], v[96:99]\n"
  "ds_read_b64_tr_b16 a[170:171], v4 offset:1024\n"
  "v_mfma_f32_16x16x32_bf16 v[100:103], a[228:231], a[200:203], v[100:103]\n"
  "buffer_load_dwordx4 v16, s[28:31], s56 offen offset:2048 lds\n"
  "v_mfma_f32_16x16x32_bf16 v[112:115], a[232:235], a[200:203], v[112:115]\n"
  "ds_read_b64_tr_b16 a[172:173], v5 offset:0\n"
  "v_mfma_f32_16x16x32_bf16 v[116:119], a[236:239], a[200:203], v[116:119]\n"
  "v_mfma_f32_16x16x32_bf16 v[128:131], a[200:203], a[240:243], v[128:131]\n"
  "ds_read_b64_tr_b16 a[174:175], v5 offset:1024\n"
  "v_mfma_f32_16x16x32_bf16 v[136:139], a[200:203], a[244:247], v[136:139]\n"
  "v_mfma_f32_16x16x32_bf16 v[144:147], a[200:203], a[248:251], v[144:147]\n"
  "buffer_load_dwordx4 v17, s[28:31], s56 offen offset:3072 lds\n"
  "ds_read_b64_tr_b16 a[176:177], v2 offset:16384\n"
  "v_mfma_f32_16x16x32_bf16 v[152:155], a[200:203], a[252:255], v[152:155]\n"
  "v_mfma_f32_16x16x32_bf16 v[104:107], a[224:227], a[204:207], v[104:107]\n"
  "ds_read_b64_tr_b16 a[178:179], v2 offset:17408\n"
  "v_mfma_f32_16x16x32_bf16 v[108:111], a[228:231], a[204:207], v[108:111]\n"
  "s_add_u32 m0, s52, 0x0\n"
  "v_mfma_f32_16x16x32_bf16 v[120:123], a[232:235], a[204:207], v[120:123]\n"
  "ds_read_b64_tr_b16 a[180:181], v3 offset:16384\n"
  "v_mfma_f32_16x16x32_bf16 v[124:127], a[236:239], a[204:207], v[124:127]\n"
  "buffer_load_dwordx4 v6, s[24:27], s55 offen offset:0 lds\n"
  "v_mfma_f32_16x16x32_bf16 v[132:135], a[204:207], a[240:243], v[132:135]\n"
  "ds_read_b64_tr_b16 a[182:183], v3 offset:17408\n"
  "v_mfma_f32_16x16x32_bf16 v[140:143], a[204:207], a[244:247], v[140:143]\n"
  "v_mfma_f32_16x16x32_bf16 v[148:151], a[204:207], a[248:251], v[148:151]\n"
  "ds_read_b64_tr_b16 a[184:185], v4 offset:16384\n"
  "v_mfma_f32_16x16x32_bf16 v[156:159], a[204:207], a[252:255], v[156:159]\n"
  "v_mfma_f32_16x16x32_bf16 v[160:163], a[224:227], a[208:211], v[160:163]\n"
  "buffer_load_dwordx4 v7, s[24:27], s55 offen offset:1024 lds\n"
  "ds_read_b64_tr_b16 a[186:187], v4 offset:17408\n"
  "v_mfma_f32_16x16x32_bf16 v[164:167], a[228:231], a[208:211], v[164:167]\n"
  "v_mfma_f32_16x16x32_bf16 v[176:179], a[232:235], a[208:211], v[176:179]\n"
  "ds_read_b64_tr_b16 a[188:189], v5 offset:16384\n"
  "v_mfma_f32_16x16x32_bf16 v[180:183], a[236:239], a[208:211], v[180:183]\n"
  "v_mfma_f32_16x16x32_bf16 v[192:195], a[208:211], a[240:243], v[192:195]\n"
  "ds_read_b64_tr_b16 a[190:191], v5 offset:17408\n"
  "v_mfma_f32_16x16x32_bf16 v[200:203], a[208:211], a[244:247], v[200:203]\n"
  "buffer_load_dwordx4 v8, s[24:27], s55 offen offset:2048 lds\n"
  "v_mfma_f32_16x16x32_bf16 v[208:211], a[208:211], a[248:251], v[208:211]\n"
  "ds_read_b128 a[132:135], v0 offset:1024\n"
  "v_mfma_f32_16x16x32_bf16 v[216:219], a[208:211], a[252:255], v[216:219]\n"
  "v_mfma_f32_16x16x32_bf16 v[168:171], a[224:227], a[212:215], v[168:171]\n"
  "ds_read_b128 a[136:139], v0 offset:4096\n"
  "v_mfma_f32_16x16x32_bf16 v[172:175], a[228:231], a[212:215], v[172:175]\n"
  "v_mfma_f32_16x16x32_bf16 v[184:187], a[232:235], a[212:215], v[184:187]\n"
  "buffer_load_dwordx4 v9, s[24:27], s55 offen offset:3072 lds\n"
  "ds_read_b128 a[140:143], v0 offset:5120\n"
  "v_mfma_f32_16x16x32_bf16 v[188:191], a[236:239], a[212:215], v[188:191]\n"
  "v_mfma_f32_16x16x32_bf16 v[196:199], a[212:215], a[240:243], v[196:199]\n"
  "ds_read_b128 a[144:147], v0 offset:8192\n"
  "v_mfma_f32_16x16x32_bf16 v[204:207], a[212:215], a[244:247], v[204:207]\n"
  "s_add_u32 m0, s53, 0xc000\n"
  "v_mfma_f32_16x16x32_bf16 v[212:215], a[212:215], a[248:251], v[212:215]\n"
  "ds_read_b128 a[148:151], v0 offset:9216\n"
  "v_mfma_f32_16x16x32_bf16 v[220:223], a[212:215], a[252:255], v[220:223]\n"
  "buffer_load_dwordx4 v14, s[32:35], s56 offen offset:0 lds\n"
  "v_mfma_f32_16x16x32_bf16 v[224:227], a[224:227], a[216:219], v[224:227]\n"
  "ds_read_b128 a[152:155], v0 offset:12288\n"
  "v_mfma_f32_16x16x32_bf16 v[228:231], a[228:231], a[216:219], v[228:231]\n"
  "v_mfma_f32_16x16x32_bf16 v[240:243], a[232:235], a[216:219], v[240:243]\n"
  "ds_read_b128 a[156:159], v0 offset:13312\n"
  "v_mfma_f32_16x16x32_bf16 v[244:247], a[236:239], a[216:219], v[244:247]\n"
  "v_mfma_f32_16x16x32_bf16 a[0:3], a[216:219], a[240:243], a[0:3]\n"
  "buffer_load_dwordx4 v15, s[32:35], s56 offen offset:1024 lds\n"
  "v_mfma_f32_16x16x32_bf16 a[8:11], a[216:219], a[244:247], a[8:11]\n"
  "v_mfma_f32_16x16x32_bf16 a[16:19], a[216:219], a[248:251], a[16:19]\n"
  "v_mfma_f32_16x16x32_bf16 a[24:27], a[216:219], a[252:255], a[24:27]\n"
  "v_mfma_f32_16x16x32_bf16 v[232:235], a[224:227], a[220:223], v[232:235]\n"
  "v_mfma_f32_16x16x32_bf16 v[236:239], a[228:231], a[220:223], v[236:239]\n"
  "buffer_load_dwordx4 v16, s[32:35], s56 offen offset:2048 lds\n"
  "v_mfma_f32_16x16x32_bf16 v[248:251], a[232:235], a[220:223], v[248:251]\n"
  "v_mfma_f32_16x16x32_bf16 v[252:255], a[236:239], a[220:223], v[252:255]\n"
  "v_mfma_f32_16x16x32_bf16 a[4:7], a[220:223], a[240:243], a[4:7]\n"
  "v_mfma_f32_16x16x32_bf16 a[12:15], a[220:223], a[244:247], a[12:15]\n"
  "v_mfma_f32_16x16x32_bf16 a[20:23], a[220:223], a[248:251], a[20:23]\n"
  "buffer_load_dwordx4 v17, s[32:35], s56 offen offset:3072 lds\n"
  "v_mfma_f32_16x16x32_bf16 a[28:31], a[220:223], a[252:255], a[28:31]\n"
  "s_waitcnt lgkmcnt(0)\n"
  "v_mfma_f32_16x16x32_bf16 v[32:35], a[160:163], a[128:131], v[32:35]\n"
  "ds_read_b128 a[192:195], v1 offset:0\n"
  "v_mfma_f32_16x16x32_bf16 v[36:39], a[164:167], a[128:131], v[36:39]\n"
  "s_add_u32 m0, s52, 0x1000\n"
  "v_mfma_f32_16x16x32_bf16 v[48:51], a[168:171], a[128:131], v[48:51]\n"
  "ds_read_b64_tr_b16 a[224:225], v2 offset:8192\n"
  "v_mfma_f32_16x16x32_bf16 v[52:55], a[172:175], a[128:131], v[52:55]\n"
  "buffer_load_dwordx4 v10, s[24:27], s55 offen offset:0 lds\n"
  "v_mfma_f32_16x16x32_bf16 v[64:67], a[128:131], a[176:179], v[64:67]\n"
  "ds_read_b64_tr_b16 a[226:227], v2 offset:9216\n"
  "v_mfma_f32_16x16x32_bf16 v[72:75], a[128:131], a[180:183], v[72:75]\n"
  "v_mfma_f32_16x16x32_bf16 v[80:83], a[128:131], a[184:187], v[80:83]\n"
  "ds_read_b64_tr_b16 a[228:229], v3 offset:8192\n"
  "v_mfma_f32_16x16x32_bf16 v[88:91], a[128:131], a[188:191], v[88:91]\n"
  "buffer_load_dwordx4 v11, s[24:27], s55 offen offset:1024 lds\n"
  "v_mfma_f32_16x16x32_bf16 v[40:43], a[160:163], a[132:135], v[40:43]\n"
  "ds_read_b64_tr_b16 a[230:231], v3 offset:9216\n"
  "v_mfma_f32_16x16x32_bf16 v[44:47], a[164:167], a[132:135], v[44:47]\n"
  "v_mfma_f32_16x16x32_bf16 v[56:59], a[168:171], a[132:135], v[56:59]\n"
  "ds_read_b64_tr_b16 a[232:233], v4 offset:8192\n"
  "v_mfma_f32_16x16x32_bf16 v[60:63], a[172:175], a[132:135], v[60:63]\n"
  "buffer_load_dwordx4 v12, s[24:27], s55 offen offset:2048 lds\n"
  "v_mfma_f32_16x16x32_bf16 v[68:71], a[132:135], a[176:179], v[68:71]\n"
  "ds_read_b64_tr_b16 a[234:235], v4 offset:9216\n"
  "v_mfma_f32_16x16x32_bf16 v[76:79], a[132:135], a[180:183], v[76:79]\n"
  "v_mfma_f32_16x16x32_bf16 v[84:87], a[132:135], a[184:187], v[84:87]\n"
  "ds_read_b64_tr_b16 a[236:237], v5 offset:8192\n"
  "v_mfma_f32_16x16x32_bf16 v[92:95], a[132:135], a[188:191], v[92:95]\n"
  "buffer_load_dwordx4 v13, s[24:27], s55 offen offset:3072 lds\n"
  "v_mfma_f32_16x16x32_bf16 v[96:99], a[160:163], a[136:139], v[96:99]\n"
  "ds_read_b64_tr_b16 a[238:239], v5 offset:9216\n"
  "v_mfma_f32_16x16x32_bf16 v[100:103], a[164:167], a[136:139], v[100:103]\n"
  "v_mfma_f32_16x16x32_bf16 v[112:115], a[168:171], a[136:139], v[112:115]\n"
  "ds_read_b64_tr_b16 a[240:241], v2 offset:24576\n"
  "v_mfma_f32_16x16x32_bf16 v[116:119], a[172:175], a[136:139], v[116:119]\n"
  "v_mfma_f32_16x16x32_bf16 v[128:131], a[136:139], a[176:179], v[128:131]\n"
  "ds_read_b64_tr_b16 a[242:243], v2 offset:25600\n"
  "v_mfma_f32_16x16x32_bf16 v[136:139], a[136:139], a[180:183], v[136:139]\n"
  "v_mfma_f32_16x16x32_bf16 v[144:147], a[136:139], a[184:187], v[144:147]\n"
  "ds_read_b64_tr_b16 a[244:245], v3 offset:24576\n"
  "v_mfma_f32_16x16x32_bf16 v[152:155], a[136:139], a[188:191], v[152:155]\n"
  "v_mfma_f32_16x16x32_bf16 v[104:107], a[160:163], a[140:143], v[104:107]\n"
  "ds_read_b64_tr_b16 a[246:247], v3 offset:25600\n"
  "v_mfma_f32_16x16x32_bf16 v[108:111], a[164:167], a[140:143], v[108:111]\n"
  "v_mfma_f32_16x16x32_bf16 v[120:123], a[168:171], a[140:143], v[120:123]\n"
  "ds_read_b64_tr_b16 a[248:249], v4 offset:24576\n"
  "v_mfma_f32_16x16x32_bf16 v[124:127], a[172:175], a[140:143], v[124:127]\n"
  "v_mfma_f32_16x16x32_bf16 v[132:135], a[140:143], a[176:179], v[132:135]\n"
  "ds_read_b64_tr_b16 a[250:251], v4 offset:25600\n"
  "v_mfma_f32_16x16x32_bf16 v[140:143], a[140:143], a[180:183], v[140:143]\n"
  "v_mfma_f32_16x16x32_bf16 v[148:151], a[140:143], a[184:187], v[148:151]\n"
  "ds_read_b64_tr_b16 a[252:253], v5 offset:24576\n"
  "v_mfma_f32_16x16x32_bf16 v[156:159], a[140:143], a[188:191], v[156:159]\n"
  "v_mfma_f32_16x16x32_bf16 v[160:163], a[160:163], a[144:147], v[160:163]\n"
  "ds_read_b64_tr_b16 a[254:255], v5 offset:25600\n"
  "v_mfma_f32_16x16x32_bf16 v[164:167], a[164:167], a[144:147], v[164:167]\n"
  "v_mfma_f32_16x16x32_bf16 v[176:179], a[168:171], a[144:147], v[176:179]\n"
  "ds_read_b128 a[196:199], v1 offset:1024\n"
  "v_mfma_f32_16x16x32_bf16 v[180:183], a[172:175], a[144:147], v[180:183]\n"
  "v_mfma_f32_16x16x32_bf16 v[192:195], a[144:147], a[176:179], v[192:195]\n"
  "ds_read_b128 a[200:203], v1 offset:4096\n"
  "v_mfma_f32_16x16x32_bf16 v[200:203], a[144:147], a[180:183], v[200:203]\n"
  "v_mfma_f32_16x16x32_bf16 v[208:211], a[144:147], a[184:187], v[208:211]\n"
  "ds_read_b128 a[204:207], v1 offset:5120\n"
  "v_mfma_f32_16x16x32_bf16 v[216:219], a[144:147], a[188:191], v[216:219]\n"
  "v_mfma_f32_16x16x32_bf16 v[168:171], a[160:163], a[148:151], v[168:171]\n"
  "ds_read_b128 a[208:211], v1 offset:8192\n"
  "v_mfma_f32_16x16x32_bf16 v[172:175], a[164:167], a[148:151], v[172:175]\n"
  "v_mfma_f32_16x16x32_bf16 v[184:187], a[168:171], a[148:151], v[184:187]\n"
  "ds_read_b128 a[212:215], v1 offset:9216\n"
  "v_mfma_f32_16x16x32_bf16 v[188:191], a[172:175], a[148:151], v[188:191]\n"
  "v_mfma_f32_16x16x32_bf16 v[196:199], a[148:151], a[176:179], v[196:199]\n"
  "ds_read_b128 a[216:219], v1 offset:12288\n"
  "v_mfma_f32_16x16x32_bf16 v[204:207], a[148:151], a[180:183], v[204:207]\n"
  "v_mfma_f32_16x16x32_bf16 v[212:215], a[148:151], a[184:187], v[212:215]\n"
  "ds_read_b128 a[220:223], v1 offset:13312\n"
  "v_mfma_f32_16x16x32_bf16 v[220:223], a[148:151], a[188:191], v[220:223]\n"
  "v_mfma_f32_16x16x32_bf16 v[224:227], a[160:163], a[152:155], v[224:227]\n"
  "v_mfma_f32_16x16x32_bf16 v[228:231], a[164:167], a[152:155], v[228:231]\n"
  "v_mfma_f32_16x16x32_bf16 v[240:243], a[168:171], a[152:155], v[240:243]\n"
  "v_mfma_f32_16x16x32_bf16 v[244:247], a[172:175], a[152:155], v[244:247]\n"
  "v_mfma_f32_16x16x32_bf16 a[0:3], a[152:155], a[176:179], a[0:3]\n"
  "v_mfma_f32_16x16x32_bf16 a[8:11], a[152:155], a[180:183], a[8:11]\n"
  "v_mfma_f32_16x16x32_bf16 a[16:19], a[152:155], a[184:187], a[16:19]\n"
  "v_mfma_f32_16x16x32_bf16 a[24:27], a[152:155], a[188:191], a[24:27]\n"
  "v_mfma_f32_16x16x32_bf16 v[232:235], a[160:163], a[156:159], v[232:235]\n"
  "v_mfma_f32_16x16x32_bf16 v[236:239], a[164:167], a[156:159], v[236:239]\n"
  "v_mfma_f32_16x16x32_bf16 v[248:251], a[168:171], a[156:159], v[248:251]\n"
  "v_mfma_f32_16x16x32_bf16 v[252:255], a[172:175], a[156:159], v[252:255]\n"
  "v_mfma_f32_16x16x32_bf16 a[4:7], a[156:159], a[176:179], a[4:7]\n"
  "v_mfma_f32_16x16x32_bf16 a[12:15], a[156:159], a[180:183], a[12:15]\n"
  "v_mfma_f32_16x16x32_bf16 a[20:23], a[156:159], a[184:187], a[20:23]\n"
  "v_mfma_f32_16x16x32_bf16 a[28:31], a[156:159], a[188:191], a[28:31]\n"
  "s_waitcnt lgkmcnt(0)\n"
  "v_mfma_f32_16x16x32_bf16 v[32:35], a[224:227], a[192:195], v[32:35]\n"
  "v_xor_b32 v0, 0x10000, v0\n"
  "v_xor_b32 v2, 0x10000, v2\n"
  "v_mfma_f32_16x16x32_bf16 v[36:39], a[228:231], a[192:195], v[36:39]\n"
  "v_xor_b32 v3, 0x10000, v3\n"
  "v_xor_b32 v4, 0x10000, v4\n"
  "v_mfma_f32_16x16x32_bf16 v[48:51], a[232:235], a[192:195], v[48:51]\n"
  "v_xor_b32 v5, 0x10000, v5\n"
  "v_xor_b32 v1, 0x10000, v1\n"
  "v_mfma_f32_16x16x32_bf16 v[52:55], a[236:239], a[192:195], v[52:55]\n"
  "s_waitcnt vmcnt(0) lgkmcnt(0)\n"
  "s_barrier\n"
  "v_mfma_f32_16x16x32_bf16 v[64:67], a[192:195], a[240:243], v[64:67]\n"
  "s_mov_b32 s55, 0x0\n"
  "s_mov_b32 s56, 0x0\n"
  "ds_read_b128 a[128:131], v0 offset:0\n"
  "v_mfma_f32_16x16x32_bf16 v[72:75], a[192:195], a[244:247], v[72:75]\n"
  "s_add_u32 m0, s53, 0x24000\n"
  "v_mfma_f32_16x16x32_bf16 v[80:83], a[192:195], a[248:251], v[80:83]\n"
  "ds_read_b64_tr_b16 a[160:161], v2 offset:0\n"
  "v_mfma_f32_16x16x32_bf16 v[88:91], a[192:195], a[252:255], v[88:91]\n"
  "buffer_load_dwordx4 v26, s[44:47], s56 offen offset:0 lds\n"
  "v_mfma_f32_16x16x32_bf16 v[40:43], a[224:227], a[196:199], v[40:43]\n"
  "ds_read_b64_tr_b16 a[162:163], v2 offset:1024\n"
  "v_mfma_f32_16x16x32_bf16 v[44:47], a[228:231], a[196:199], v[44:47]\n"
  "v_mfma_f32_16x16x32_bf16 v[56:59], a[232:235], a[196:199], v[56:59]\n"
  "ds_read_b64_tr_b16 a[164:165], v3 offset:0\n"
  "v_mfma_f32_16x16x32_bf16 v[60:63], a[236:239], a[196:199], v[60:63]\n"
  "buffer_load_dwordx4 v27, s[44:47], s56 offen offset:1024 lds\n"
  "v_mfma_f32_16x16x32_bf16 v[68:71], a[196:199], a[240:243], v[68:71]\n"
  "ds_read_b64_tr_b16 a[166:167], v3 offset:1024\n"
  "v_mfma_f32_16x16x32_bf16 v[76:79], a[196:199], a[244:247], v[76:79]\n"
  "v_mfma_f32_16x16x32_bf16 v[84:87], a[196:199], a[248:251], v[84:87]\n"
  "ds_read_b64_tr_b16 a[168:169], v4 offset:0\n"
  "v_mfma_f32_16x16x32_bf16 v[92:95], a[196:199], a[252:255], v[92:95]\n"
  "buffer_load_dwordx4 v28, s[44:47], s56 offen offset:2048 lds\n"
  "v_mfma_f32_16x16x32_bf16 v[96:99], a[224:227], a[200:203], v[96:99]\n"
  "ds_read_b64_tr_b16 a[170:171], v4 offset:1024\n"
  "v_mfma_f32_16x16x32_bf16 v[100:103], a[228:231], a[200:203], v[100:103]\n"
  "v_mfma_f32_16x16x32_bf16 v[112:115], a[232:235], a[200:203], v[112:115]\n"
  "ds_read_b64_tr_b16 a[172:173], v5 offset:0\n"
  "v_mfma_f32_16x16x32_bf16 v[116:119], a[236:239], a[200:203], v[116:119]\n"
  "buffer_load_dwordx4 v29, s[44:47], s56 offen offset:3072 lds\n"
  "v_mfma_f32_16x16x32_bf16 v[128:131], a[200:203], a[240:243], v[128:131]\n"
  "ds_read_b64_tr_b16 a[174:175], v5 offset:1024\n"
  "v_mfma_f32_16x16x32_bf16 v[136:139], a[200:203], a[244:247], v[136:139]\n"
  "s_add_u32 m0, s52, 0x1c000\n"
  "v_mfma_f32_16x16x32_bf16 v[144:147], a[200:203], a[248:251], v[144:147]\n"
  "ds_read_b64_tr_b16 a[176:177], v2 offset:16384\n"
  "v_mfma_f32_16x16x32_bf16 v[152:155], a[200:203], a[252:255], v[152:155]\n"
  "buffer_load_dwordx4 v18, s[40:43], s55 offen offset:0 lds\n"
  "v_mfma_f32_16x16x32_bf16 v[104:107], a[224:227], a[204:207], v[104:107]\n"
  "ds_read_b64_tr_b16 a[178:179], v2 offset:17408\n"
  "v_mfma_f32_16x16x32_bf16 v[108:111], a[228:231], a[204:207], v[108:111]\n"
  "v_mfma_f32_16x16x32_bf16 v[120:123], a[232:235], a[204:207], v[120:123]\n"
  "ds_read_b64_tr_b16 a[180:181], v3 offset:16384\n"
  "v_mfma_f32_16x16x32_bf16 v[124:127], a[236:239], a[204:207], v[124:127]\n"
  "buffer_load_dwordx4 v19, s[40:43], s55 offen offset:1024 lds\n"
  "v_mfma_f32_16x16x32_bf16 v[132:135], a[204:207], a[240:243], v[132:135]\n"
  "ds_read_b64_tr_b16 a[182:183], v3 offset:17408\n"
  "v_mfma_f32_16x16x32_bf16 v[140:143], a[204:207], a[244:247], v[140:143]\n"
  "v_mfma_f32_16x16x32_bf16 v[148:151], a[204:207], a[248:251], v[148:151]\n"
  "ds_read_b64_tr_b16 a[184:185], v4 offset:16384\n"
  "v_mfma_f32_16x16x32_bf16 v[156:159], a[204:207], a[252:255], v[156:159]\n"
  "buffer_load_dwordx4 v20, s[40:43], s55 offen offset:2048 lds\n"
  "v_mfma_f32_16x16x32_bf16 v[160:163], a[224:227], a[208:211], v[160:163]\n"
  "ds_read_b64_tr_b16 a[186:187], v4 offset:17408\n"
  "v_mfma_f32_16x16x32_bf16 v[164:167], a[228:231], a[208:211], v[164:167]\n"
  "v_mfma_f32_16x16x32_bf16 v[176:179], a[232:235], a[208:211], v[176:179]\n"
  "ds_read_b64_tr_b16 a[188:189], v5 offset:16384\n"
  "v_mfma_f32_16x16x32_bf16 v[180:183], a[236:239], a[208:211], v[180:183]\n"
  "buffer_load_dwordx4 v21, s[40:43], s55 offen offset:3072 lds\n"
  "v_mfma_f32_16x16x32_bf16 v[192:195], a[208:211], a[240:243], v[192:195]\n"
  "ds_read_b64_tr_b16 a[190:191], v5 offset:17408\n"
  "v_mfma_f32_16x16x32_bf16 v[200:203], a[208:211], a[244:247], v[200:203]\n"
  "s_add_u32 m0, s52, 0x1d000\n"
  "v_mfma_f32_16x16x32_bf16 v[208:211], a[208:211], a[248:251], v[208:211]\n"
  "ds_read_b128 a[132:135], v0 offset:1024\n"
  "v_mfma_f32_16x16x32_bf16 v[216:219], a[208:211], a[252:255], v[216:219]\n"
  "buffer_load_dwordx4 v22, s[40:43], s55 offen offset:0 lds\n"
  "v_mfma_f32_16x16x32_bf16 v[168:171], a[224:227], a[212:215], v[168:171]\n"
  "ds_read_b128 a[136:139], v0 offset:4096\n"
  "v_mfma_f32_16x16x32_bf16 v[172:175], a[228:231], a[212:215], v[172:175]\n"
  "v_mfma_f32_16x16x32_bf16 v[184:187], a[232:235], a[212:215], v[184:187]\n"
  "ds_read_b128 a[140:143], v0 offset:5120\n"
  "v_mfma_f32_16x16x32_bf16 v[188:191], a[236:239], a[212:215], v[188:191]\n"
  "buffer_load_dwordx4 v23, s[40:43], s55 offen offset:1024 lds\n"
  "v_mfma_f32_16x16x32_bf16 v[196:199], a[212:215], a[240:243], v[196:199]\n"
  "ds_read_b128 a[144:147], v0 offset:8192\n"
  "v_mfma_f32_16x16x32_bf16 v[204:207], a[212:215], a[244:247], v[204:207]\n"
  "v_mfma_f32_16x16x32_bf16 v[212:215], a[212:215], a[248:251], v[212:215]\n"
  "ds_read_b128 a[148:151], v0 offset:9216\n"
  "v_mfma_f32_16x16x32_bf16 v[220:223], a[212:215], a[252:255], v[220:223]\n"
  "buffer_load_dwordx4 v24, s[40:43], s55 offen offset:2048 lds\n"
  "v_mfma_f32_16x16x32_bf16 v[224:227], a[224:227], a[216:219], v[224:227]\n"
  "ds_read_b128 a[152:155], v0 offset:12288\n"
  "v_mfma_f32_16x16x32_bf16 v[228:231], a[228:231], a[216:219], v[228:231]\n"
  "v_mfma_f32_16x16x32_bf16 v[240:243], a[232:235], a[216:219], v[240:243]\n"
  "ds_read_b128 a[156:159], v0 offset:13312\n"
  "v_mfma_f32_16x16x32_bf16 v[244:247], a[236:239], a[216:219], v[244:247]\n"
  "buffer_load_dwordx4 v25, s[40:43], s55 offen offset:3072 lds\n"
  "v_mfma_f32_16x16x32_bf16 a[0:3], a[216:219], a[240:243], a[0:3]\n"
  "s_mov_b32 s55, 0x80\n"
  "s_mov_b32 s56, 0x80000\n"
  "v_mfma_f32_16x16x32_bf16 a[8:11], a[216:219], a[244:247], a[8:11]\n"
  "s_add_u32 m0, s53, 0x18000\n"
  "v_mfma_f32_16x16x32_bf16 a[16:19], a[216:219], a[248:251], a[16:19]\n"
  "v_mfma_f32_16x16x32_bf16 a[24:27], a[216:219], a[252:255], a[24:27]\n"
  "buffer_load_dwordx4 v26, s[44:47], s56 offen offset:0 lds\n"
  "v_mfma_f32_16x16x32_bf16 v[232:235], a[224:227], a[220:223], v[232:235]\n"
  "v_mfma_f32_16x16x32_bf16 v[236:239], a[228:231], a[220:223], v[236:239]\n"
  "v_mfma_f32_16x16x32_bf16 v[248:251], a[232:235], a[220:223], v[248:251]\n"
  "v_mfma_f32_16x16x32_bf16 v[252:255], a[236:239], a[220:223], v[252:255]\n"
  "buffer_load_dwordx4 v27, s[44:47], s56 offen offset:1024 lds\n"
  "v_mfma_f32_16x16x32_bf16 a[4:7], a[220:223], a[240:243], a[4:7]\n"
  "v_mfma_f32_16x16x32_bf16 a[12:15], a[220:223], a[244:247], a[12:15]\n"
  "v_mfma_f32_16x16x32_bf16 a[20:23], a[220:223], a[248:251], a[20:23]\n"
  "v_mfma_f32_16x16x32_bf16 a[28:31], a[220:223], a[252:255], a[28:31]\n"
  "buffer_load_dwordx4 v28, s[44:47], s56 offen offset:2048 lds\n"
  "s_waitcnt lgkmcnt(0)\n"
  "v_mfma_f32_16x16x32_bf16 v[32:35], a[160:163], a[128:131], v[32:35]\n"
  "ds_read_b128 a[192:195], v1 offset:0\n"
  "v_mfma_f32_16x16x32_bf16 v[36:39], a[164:167], a[128:131], v[36:39]\n"
  "v_mfma_f32_16x16x32_bf16 v[48:51], a[168:171], a[128:131], v[48:51]\n"
  "ds_read_b64_tr_b16 a[224:225], v2 offset:8192\n"
  "v_mfma_f32_16x16x32_bf16 v[52:55], a[172:175], a[128:131], v[52:55]\n"
  "buffer_load_dwordx4 v29, s[44:47], s56 offen offset:3072 lds\n"
  "v_mfma_f32_16x16x32_bf16 v[64:67], a[128:131], a[176:179], v[64:67]\n"
  "ds_read_b64_tr_b16 a[226:227], v2 offset:9216\n"
  "v_mfma_f32_16x16x32_bf16 v[72:75], a[128:131], a[180:183], v[72:75]\n"
  "s_add_u32 m0, s52, 0x10000\n"
  "v_mfma_f32_16x16x32_bf16 v[80:83], a[128:131], a[184:187], v[80:83]\n"
  "ds_read_b64_tr_b16 a[228:229], v3 offset:8192\n"
  "v_mfma_f32_16x16x32_bf16 v[88:91], a[128:131], a[188:191], v[88:91]\n"
  "buffer_load_dwordx4 v18, s[40:43], s55 offen offset:0 lds\n"
  "v_mfma_f32_16x16x32_bf16 v[40:43], a[160:163], a[132:135], v[40:43]\n"
  "ds_read_b64_tr_b16 a[230:231], v3 offset:9216\n"
  "v_mfma_f32_16x16x32_bf16 v[44:47], a[164:167], a[132:135], v[44:47]\n"
  "v_mfma_f32_16x16x32_bf16 v[56:59], a[168:171], a[132:135], v[56:59]\n"
  "ds_read_b64_tr_b16 a[232:233], v4 offset:8192\n"
  "v_mfma_f32_16x16x32_bf16 v[60:63], a[172:175], a[132:135], v[60:63]\n"
  "buffer_load_dwordx4 v19, s[40:43], s55 offen offset:1024 lds\n"
  "v_mfma_f32_16x16x32_bf16 v[68:71], a[132:135], a[176:179], v[68:71]\n"
  "ds_read_b64_tr_b16 a[234:235], v4 offset:9216\n"
  "v_mfma_f32_16x16x32_bf16 v[76:79], a[132:135], a[180:183], v[76:79]\n"
  "v_mfma_f32_16x16x32_bf16 v[84:87], a[132:135], a[184:187], v[84:87]\n"
  "ds_read_b64_tr_b16 a[236:237], v5 offset:8192\n"
  "v_mfma_f32_16x16x32_bf16 v[92:95], a[132:135], a[188:191], v[92:95]\n"
  "buffer_load_dwordx4 v20, s[40:43], s55 offen offset:2048 lds\n"
  "v_mfma_f32_16x16x32_bf16 v[96:99], a[160:163], a[136:139], v[96:99]\n"
  "ds_read_b64_tr_b16 a[238:239], v5 offset:9216\n"
  "v_mfma_f32_16x16x32_bf16 v[100:103], a[164:167], a[136:139], v[100:103]\n"
  "v_mfma_f32_16x16x32_bf16 v[112:115], a[168:171], a[136:139], v[112:115]\n"
  "ds_read_b64_tr_b16 a[240:241], v2 offset:24576\n"
  "v_mfma_f32_16x16x32_bf16 v[116:119], a[172:175], a[136:139], v[116:119]\n"
  "buffer_load_dwordx4 v21, s[40:43], s55 offen offset:3072 lds\n"
  "v_mfma_f32_16x16x32_bf16 v[128:131], a[136:139], a[176:179], v[128:131]\n"
  "ds_read_b64_tr_b16 a[242:243], v2 offset:25600\n"
  "v_mfma_f32_16x16x32_bf16 v[136:139], a[136:139], a[180:183], v[136:139]\n"
  "s_add_u32 m0, s52, 0x11000\n"
  "v_mfma_f32_16x16x32_bf16 v[144:147], a[136:139], a[184:187], v[144:147]\n"
  "ds_read_b64_tr_b16 a[244:245], v3 offset:24576\n"
  "v_mfma_f32_16x16x32_bf16 v[152:155], a[136:139], a[188:191], v[152:155]\n"
  "buffer_load_dwordx4 v22, s[40:43], s55 offen offset:0 lds\n"
  "v_mfma_f32_16x16x32_bf16 v[104:107], a[160:163], a[140:143], v[104:107]\n"
  "ds_read_b64_tr_b16 a[246:247], v3 offset:25600\n"
  "v_mfma_f32_16x16x32_bf16 v[108:111], a[164:167], a[140:143], v[108:111]\n"
  "v_mfma_f32_16x16x32_bf16 v[120:123], a[168:171], a[140:143], v[120:123]\n"
  "ds_read_b64_tr_b16 a[248:249], v4 offset:24576\n"
  "v_mfma_f32_16x16x32_bf16 v[124:127], a[172:175], a[140:143], v[124:127]\n"
  "buffer_load_dwordx4 v23, s[40:43], s55 offen offset:1024 lds\n"
  "v_mfma_f32_16x16x32_bf16 v[132:135], a[140:143], a[176:179], v[132:135]\n"
  "ds_read_b64_tr_b16 a[250:251], v4 offset:25600\n"
  "v_mfma_f32_16x16x32_bf16 v[140:143], a[140:143], a[180:183], v[140:143]\n"
  "v_mfma_f32_16x16x32_bf16 v[148:151], a[140:143], a[184:187], v[148:151]\n"
  "ds_read_b64_tr_b16 a[252:253], v5 offset:24576\n"
  "v_mfma_f32_16x16x32_bf16 v[156:159], a[140:143], a[188:191], v[156:159]\n"
  "buffer_load_dwordx4 v24, s[40:43], s55 offen offset:2048 lds\n"
  "v_mfma_f32_16x16x32_bf16 v[160:163], a[160:163], a[144:147], v[160:163]\n"
  "ds_read_b64_tr_b16 a[254:255], v5 offset:25600\n"
  "v_mfma_f32_16x16x32_bf16 v[164:167], a[164:167], a[144:147], v[164:167]\n"
  "v_mfma_f32_16x16x32_bf16 v[176:179], a[168:171], a[144:147], v[176:179]\n"
  "ds_read_b128 a[196:199], v1 offset:1024\n"
  "v_mfma_f32_16x16x32_bf16 v[180:183], a[172:175], a[144:147], v[180:183]\n"
  "buffer_load_dwordx4 v25, s[40:43], s55 offen offset:3072 lds\n"
  "v_mfma_f32_16x16x32_bf16 v[192:195], a[144:147], a[176:179], v[192:195]\n"
  "ds_read_b128 a[200:203], v1 offset:4096\n"
  "v_mfma_f32_16x16x32_bf16 v[200:203], a[144:147], a[180:183], v[200:203]\n"
  "v_mfma_f32_16x16x32_bf16 v[208:211], a[144:147], a[184:187], v[208:211]\n"
  "ds_read_b128 a[204:207], v1 offset:5120\n"
  "v_mfma_f32_16x16x32_bf16 v[216:219], a[144:147], a[188:191], v[216:219]\n"
  "v_mfma_f32_16x16x32_bf16 v[168:171], a[160:163], a[148:151], v[168:171]\n"
  "ds_read_b128 a[208:211], v1 offset:8192\n"
  "v_mfma_f32_16x16x32_bf16 v[172:175], a[164:167], a[148:151], v[172:175]\n"
  "s_waitcnt vmcnt(40)\n"
  "buffer_store_dwordx4 a[108:111], v30, s[36:39], s21 offen offset:0 sc1\n"
  "v_mfma_f32_16x16x32_bf16 v[184:187], a[168:171], a[148:151], v[184:187]\n"
  "ds_read_b128 a[212:215], v1 offset:9216\n"
  "v_mfma_f32_16x16x32_bf16 v[188:191], a[172:175], a[148:151], v[188:191]\n"
  "v_mfma_f32_16x16x32_bf16 v[196:199], a[148:151], a[176:179], v[196:199]\n"
  "ds_read_b128 a[216:219], v1 offset:12288\n"
  "v_mfma_f32_16x16x32_bf16 v[204:207], a[148:151], a[180:183], v[204:207]\n"
  "buffer_store_dwordx4 a[112:115], v30, s[36:39], s21 offen offset:1024 sc1\n"
  "v_mfma_f32_16x16x32_bf16 v[212:215], a[148:151], a[184:187], v[212:215]\n"
  "ds_read_b128 a[220:223], v1 offset:13312\n"
  "v_mfma_f32_16x16x32_bf16 v[220:223], a[148:151], a[188:191], v[220:223]\n"
  "v_mfma_f32_16x16x32_bf16 v[224:227], a[160:163], a[152:155], v[224:227]\n"
  "v_mfma_f32_16x16x32_bf16 v[228:231], a[164:167], a[152:155], v[228:231]\n"
  "buffer_store_dwordx4 a[116:119], v30, s[36:39], s21 offen offset:1536 sc1\n"
  "v_mfma_f32_16x16x32_bf16 v[240:243], a[168:171], a[152:155], v[240:243]\n"
  "v_mfma_f32_16x16x32_bf16 v[244:247], a[172:175], a[152:155], v[244:247]\n"
  "v_mfma_f32_16x16x32_bf16 a[0:3], a[152:155], a[176:179], a[0:3]\n"
  "v_mfma_f32_16x16x32_bf16 a[8:11], a[152:155], a[180:183], a[8:11]\n"
  "v_mfma_f32_16x16x32_bf16 a[16:19], a[152:155], a[184:187], a[16:19]\n"
  "v_mfma_f32_16x16x32_bf16 a[24:27], a[152:155], a[188:191], a[24:27]\n"
  "v_mfma_f32_16x16x32_bf16 v[232:235], a[160:163], a[156:159], v[232:235]\n"
  "v_mfma_f32_16x16x32_bf16 v[236:239], a[164:167], a[156:159], v[236:239]\n"
  "v_mfma_f32_16x16x32_bf16 v[248:251], a[168:171], a[156:159], v[248:251]\n"
  "v_mfma_f32_16x16x32_bf16 v[252:255], a[172:175], a[156:159], v[252:255]\n"
  "v_mfma_f32_16x16x32_bf16 a[4:7], a[156:159], a[176:179], a[4:7]\n"
  "v_mfma_f32_16x16x32_bf16 a[12:15], a[156:159], a[180:183], a[12:15]\n"
  "v_mfma_f32_16x16x32_bf16 a[20:23], a[156:159], a[184:187], a[20:23]\n"
  "v_mfma_f32_16x16x32_bf16 a[28:31], a[156:159], a[188:191], a[28:31]\n"
  "s_waitcnt lgkmcnt(0)\n"
  "v_mfma_f32_16x16x32_bf16 v[32:35], a[224:227], a[192:195], v[32:35]\n"
  "v_mfma_f32_16x16x32_bf16 v[36:39], a[228:231], a[192:195], v[36:39]\n"
  "v_mfma_f32_16x16x32_bf16 v[48:51], a[232:235], a[192:195], v[48:51]\n"
  "v_mfma_f32_16x16x32_bf16 v[52:55], a[236:239], a[192:195], v[52:55]\n"
  "s_waitcnt lgkmcnt(0)\n"
  "s_barrier\n"
  "v_mfma_f32_16x16x32_bf16 v[64:67], a[192:195], a[240:243], v[64:67]\n"
  "s_mov_b32 s55, 0x100\n"
  "s_mov_b32 s56, 0x100000\n"
  "v_accvgpr_read_b32 v0, a96\n"
  "v_mfma_f32_16x16x32_bf16 v[72:75], a[192:195], a[244:247], v[72:75]\n"
  "s_add_u32 m0, s53, 0x8000\n"
  "v_accvgpr_read_b32 v1, a97\n"
  "v_mfma_f32_16x16x32_bf16 v[80:83], a[192:195], a[248:251], v[80:83]\n"
  "v_accvgpr_read_b32 v2, a98\n"
  "v_mfma_f32_16x16x32_bf16 v[88:91], a[192:195], a[252:255], v[88:91]\n"
  "buffer_load_dwordx4 v26, s[44:47], s56 offen offset:0 lds\n"
  "v_accvgpr_read_b32 v3, a99\n"
  "v_mfma_f32_16x16x32_bf16 v[40:43], a[224:227], a[196:199], v[40:43]\n"
  "v_accvgpr_read_b32 v4, a100\n"
  "v_mfma_f32_16x16x32_bf16 v[44:47], a[228:231], a[196:199], v[44:47]\n"
  "v_accvgpr_read_b32 v5, a101\n"
  "v_mfma_f32_16x16x32_bf16 v[56:59], a[232:235], a[196:199], v[56:59]\n"
  "v_accvgpr_read_b32 v6, a102\n"
  "v_mfma_f32_16x16x32_bf16 v[60:63], a[236:239], a[196:199], v[60:63]\n"
  "buffer_load_dwordx4 v27, s[44:47], s56 offen offset:1024 lds\n"
  "v_accvgpr_read_b32 v7, a103\n"
  "v_mfma_f32_16x16x32_bf16 v[68:71], a[196:199], a[240:243], v[68:71]\n"
  "v_accvgpr_read_b32 v31, a106\n"
  "v_mfma_f32_16x16x32_bf16 v[76:79], a[196:199], a[244:247], v[76:79]\n"
  "v_accvgpr_read_b32 v17, a107\n"
  "v_mfma_f32_16x16x32_bf16 v[84:87], a[196:199], a[248:251], v[84:87]\n"
  "v_mfma_f32_16x16x32_bf16 v[92:95], a[196:199], a[252:255], v[92:95]\n"
  "buffer_load_dwordx4 v28, s[44:47], s56 offen offset:2048 lds\n"
  "v_mfma_f32_16x16x32_bf16 v[96:99], a[224:227], a[200:203], v[96:99]\n"
  "v_mfma_f32_16x16x32_bf16 v[100:103], a[228:231], a[200:203], v[100:103]\n"
  "v_mfma_f32_16x16x32_bf16 v[112:115], a[232:235], a[200:203], v[112:115]\n"
  "v_mfma_f32_16x16x32_bf16 v[116:119], a[236:239], a[200:203], v[116:119]\n"
  "buffer_load_dwordx4 v29, s[44:47], s56 offen offset:3072 lds\n"
  "v_mfma_f32_16x16x32_bf16 v[128:131], a[200:203], a[240:243], v[128:131]\n"
  "v_mfma_f32_16x16x32_bf16 v[136:139], a[200:203], a[244:247], v[136:139]\n"
  "s_add_u32 m0, s52, 0x0\n"
  "v_mfma_f32_16x16x32_bf16 v[144:147], a[200:203], a[248:251], v[144:147]\n"
  "v_mfma_f32_16x16x32_bf16 v[152:155], a[200:203], a[252:255], v[152:155]\n"
  "buffer_load_dwordx4 v18, s[40:43], s55 offen offset:0 lds\n"
  "v_mfma_f32_16x16x32_bf16 v[104:107], a[224:227], a[204:207], v[104:107]\n"
  "v_mfma_f32_16x16x32_bf16 v[108:111], a[228:231], a[204:207], v[108:111]\n"
  "v_mfma_f32_16x16x32_bf16 v[120:123], a[232:235], a[204:207], v[120:123]\n"
  "v_mfma_f32_16x16x32_bf16 v[124:127], a[236:239], a[204:207], v[124:127]\n"
  "buffer_load_dwordx4 v19, s[40:43], s55 offen offset:1024 lds\n"
  "v_mfma_f32_16x16x32_bf16 v[132:135], a[204:207], a[240:243], v[132:135]\n"
  "v_mfma_f32_16x16x32_bf16 v[140:143], a[204:207], a[244:247], v[140:143]\n"
  "v_mfma_f32_16x16x32_bf16 v[148:151], a[204:207], a[248:251], v[148:151]\n"
  "v_mfma_f32_16x16x32_bf16 v[156:159], a[204:207], a[252:255], v[156:159]\n"
  "buffer_load_dwordx4 v20, s[40:43], s55 offen offset:2048 lds\n"
  "v_mfma_f32_16x16x32_bf16 v[160:163], a[224:227], a[208:211], v[160:163]\n"
  "v_mfma_f32_16x16x32_bf16 v[164:167], a[228:231], a[208:211], v[164:167]\n"
  "v_mfma_f32_16x16x32_bf16 v[176:179], a[232:235], a[208:211], v[176:179]\n"
  "v_mfma_f32_16x16x32_bf16 v[180:183], a[236:239], a[208:211], v[180:183]\n"
  "buffer_load_dwordx4 v21, s[40:43], s55 offen offset:3072 lds\n"
  "v_mfma_f32_16x16x32_bf16 v[192:195], a[208:211], a[240:243], v[192:195]\n"
  "s_waitcnt vmcnt(23)\n"
  "s_barrier\n"
  "v_mfma_f32_16x16x32_bf16 v[200:203], a[208:211], a[244:247], v[200:203]\n"
  "s_add_u32 m0, s52, 0x1000\n"
  "ds_read_b128 a[48:51], v0 offset:0\n"
  "v_mfma_f32_16x16x32_bf16 v[208:211], a[208:211], a[248:251], v[208:211]\n"
  "v_mfma_f32_16x16x32_bf16 v[216:219], a[208:211], a[252:255], v[216:219]\n"
  "buffer_load_dwordx4 v22, s[40:43], s55 offen offset:0 lds\n"
  "ds_read_b64_tr_b16 a[32:33], v4 offset:0\n"
  "v_mfma_f32_16x16x32_bf16 v[168:171], a[224:227], a[212:215], v[168:171]\n"
  "v_mfma_f32_16x16x32_bf16 v[172:175], a[228:231], a[212:215], v[172:175]\n"
  "ds_read_b64_tr_b16 a[34:35], v4 offset:1024\n"
  "v_mfma_f32_16x16x32_bf16 v[184:187], a[232:235], a[212:215], v[184:187]\n"
  "v_mfma_f32_16x16x32_bf16 v[188:191], a[236:239], a[212:215], v[188:191]\n"
  "buffer_load_dwordx4 v23, s[40:43], s55 offen offset:1024 lds\n"
  "ds_read_b128 a[52:55], v0 offset:4096\n"
  "v_mfma_f32_16x16x32_bf16 v[196:199], a[212:215], a[240:243], v[196:199]\n"
  "v_mfma_f32_16x16x32_bf16 v[204:207], a[212:215], a[244:247], v[204:207]\n"
  "ds_read_b64_tr_b16 a[36:37], v5 offset:0\n"
  "v_mfma_f32_16x16x32_bf16 v[212:215], a[212:215], a[248:251], v[212:215]\n"
  "v_mfma_f32_16x16x32_bf16 v[220:223], a[212:215], a[252:255], v[220:223]\n"
  "buffer_load_dwordx4 v24, s[40:43], s55 offen offset:2048 lds\n"
  "ds_read_b64_tr_b16 a[38:39], v5 offset:1024\n"
  "v_mfma_f32_16x16x32_bf16 v[224:227], a[224:227], a[216:219], v[224:227]\n"
  "v_mfma_f32_16x16x32_bf16 v[228:231], a[228:231], a[216:219], v[228:231]\n"
  "ds_read_b64_tr_b16 a[40:41], v6 offset:0\n"
  "v_mfma_f32_16x16x32_bf16 v[240:243], a[232:235], a[216:219], v[240:243]\n"
  "v_mfma_f32_16x16x32_bf16 v[244:247], a[236:239], a[216:219], v[244:247]\n"
  "buffer_load_dwordx4 v25, s[40:43], s55 offen offset:3072 lds\n"
  "ds_read_b64_tr_b16 a[42:43], v6 offset:1024\n"
  "v_mfma_f32_16x16x32_bf16 a[0:3], a[216:219], a[240:243], a[0:3]\n"
  "v_mfma_f32_16x16x32_bf16 a[8:11], a[216:219], a[244:247], a[8:11]\n"
  "ds_read_b64_tr_b16 a[44:45], v7 offset:0\n"
  "v_mfma_f32_16x16x32_bf16 a[16:19], a[216:219], a[248:251], a[16:19]\n"
  "v_mfma_f32_16x16x32_bf16 a[24:27], a[216:219], a[252:255], a[24:27]\n"
  "ds_read_b64_tr_b16 a[46:47], v7 offset:1024\n"
  "v_mfma_f32_16x16x32_bf16 v[232:235], a[224:227], a[220:223], v[232:235]\n"
  "v_mfma_f32_16x16x32_bf16 v[236:239], a[228:231], a[220:223], v[236:239]\n"
  "v_mfma_f32_16x16x32_bf16 v[248:251], a[232:235], a[220:223], v[248:251]\n"
  "v_mfma_f32_16x16x32_bf16 v[252:255], a[236:239], a[220:223], v[252:255]\n"
  "v_mfma_f32_16x16x32_bf16 a[4:7], a[220:223], a[240:243], a[4:7]\n"
  "v_mfma_f32_16x16x32_bf16 a[12:15], a[220:223], a[244:247], a[12:15]\n"
  "v_mfma_f32_16x16x32_bf16 a[20:23], a[220:223], a[248:251], a[20:23]\n"
  "v_mfma_f32_16x16x32_bf16 a[28:31], a[220:223], a[252:255], a[28:31]\n"
  "s_waitcnt lgkmcnt(7)\n"
  "v_mfma_f32_32x32x16_bf16 a[128:143], a[32:35], a[48:51], 0\n"
  "ds_read_b128 a[72:75], v1 offset:0\n"
  "ds_read_b64_tr_b16 a[56:57], v4 offset:4096\n"
  "s_waitcnt lgkmcnt(8)\n"
  "v_mfma_f32_32x32x16_bf16 a[192:207], a[32:35], a[52:55], 0\n"
  "ds_read_b64_tr_b16 a[58:59], v4 offset:5120\n"
  "ds_read_b128 a[76:79], v1 offset:4096\n"
  "s_waitcnt lgkmcnt(8)\n"
  "v_mfma_f32_32x32x16_bf16 a[144:159], a[36:39], a[48:51], 0\n"
  "ds_read_b64_tr_b16 a[60:61], v5 offset:4096\n"
  "v_mfma_f32_32x32x16_bf16 a[208:223], a[36:39], a[52:55], 0\n"
  "s_waitcnt lgkmcnt(5)\n"
  "ds_read_b64_tr_b16 a[62:63], v5 offset:5120\n"
  "ds_read_b64_tr_b16 a[64:65], v6 offset:4096\n"
  "v_mfma_f32_32x32x16_bf16 a[160:175], a[40:43], a[48:51], 0\n"
  "ds_read_b64_tr_b16 a[66:67], v6 offset:5120\n"
  "ds_read_b64_tr_b16 a[68:69], v7 offset:4096\n"
  "v_mfma_f32_32x32x16_bf16 a[224:239], a[40:43], a[52:55], 0\n"
  "ds_read_b64_tr_b16 a[70:71], v7 offset:5120\n"
  "v_mfma_f32_32x32x16_bf16 a[176:191], a[44:47], a[48:51], 0\n"
  "v_mfma_f32_32x32x16_bf16 a[240:255], a[44:47], a[52:55], 0\n"
  "s_waitcnt lgkmcnt(4)\n"
  "v_mfma_f32_32x32x16_bf16 a[128:143], a[56:59], a[72:75], a[128:143]\n"
  "ds_read_b128 a[48:51], v2 offset:0\n"
  "ds_read_b64_tr_b16 a[32:33], v4 offset:8192\n"
  "v_accvgpr_read_b32 v8, a0\n"
  "v_accvgpr_read_b32 v9, a1\n"
  "v_accvgpr_read_b32 v10, a2\n"
  "v_accvgpr_read_b32 v11, a3\n"
  "v_accvgpr_read_b32 v12, a4\n"
  "v_accvgpr_read_b32 v13, a5\n"
  "v_accvgpr_read_b32 v14, a6\n"
  "v_accvgpr_read_b32 v15, a7\n"
  "v_cvt_pk_bf16_f32 v8, v8, v9\n"
  "v_cvt_pk_bf16_f32 v9, v10, v11\n"
  "v_cvt_pk_bf16_f32 v10, v12, v13\n"
  "v_cvt_pk_bf16_f32 v11, v14, v15\n"
  "v_mfma_f32_32x32x16_bf16 a[192:207], a[56:59], a[76:79], a[192:207]\n"
  "s_add_u32 s57, s59, 0x6000\n"
  "buffer_store_dwordx4 v[8:11], v17, s[36:39], s57 offen sc1\n"
  "ds_read_b64_tr_b16 a[34:35], v4 offset:9216\n"
  "ds_read_b128 a[52:55], v2 offset:4096\n"
  "v_mfma_f32_32x32x16_bf16 a[144:159], a[60:63], a[72:75], a[144:159]\n"
  "ds_read_b64_tr_b16 a[36:37], v5 offset:8192\n"
  "v_accvgpr_read_b32 v8, a8\n"
  "v_accvgpr_read_b32 v9, a9\n"
  "v_accvgpr_read_b32 v10, a10\n"
  "v_accvgpr_read_b32 v11, a11\n"
  "v_accvgpr_read_b32 v12, a12\n"
  "v_accvgpr_read_b32 v13, a13\n"
  "v_accvgpr_read_b32 v14, a14\n"
  "v_accvgpr_read_b32 v15, a15\n"
  "v_cvt_pk_bf16_f32 v8, v8, v9\n"
  "v_cvt_pk_bf16_f32 v9, v10, v11\n"
  "v_cvt_pk_bf16_f32 v10, v12, v13\n"
  "v_cvt_pk_bf16_f32 v11, v14, v15\n"
  "v_mfma_f32_32x32x16_bf16 a[208:223], a[60:63], a[76:79], a[208:223]\n"
  "s_waitcnt lgkmcnt(5)\n"
  "s_add_u32 s57, s59, 0x6080\n"
  "buffer_store_dwordx4 v[8:11], v17, s[36:39], s57 offen sc1\n"
  "ds_read_b64_tr_b16 a[38:39], v5 offset:9216\n"
  "ds_read_b64_tr_b16 a[40:41], v6 offset:8192\n"
  "v_mfma_f32_32x32x16_bf16 a[160:175], a[64:67], a[72:75], a[160:175]\n"
  "ds_read_b64_tr_b16 a[42:43], v6 offset:9216\n"
  "ds_read_b64_tr_b16 a[44:45], v7 offset:8192\n"
  "v_mfma_f32_32x32x16_bf16 a[224:239], a[64:67], a[76:79], a[224:239]\n"
  "ds_read_b64_tr_b16 a[46:47], v7 offset:9216\n"
  "v_mfma_f32_32x32x16_bf16 a[176:191], a[68:71], a[72:75], a[176:191]\n"
  "v_mfma_f32_32x32x16_bf16 a[240:255], a[68:71], a[76:79], a[240:255]\n"
  "s_waitcnt lgkmcnt(4)\n"
  "v_mfma_f32_32x32x16_bf16 a[128:143], a[32:35], a[48:51], a[128:143]\n"
  "ds_read_b128 a[72:75], v3 offset:0\n"
  "ds_read_b64_tr_b16 a[56:57], v4 offset:12288\n"
  "v_accvgpr_read_b32 v8, a16\n"
  "v_accvgpr_read_b32 v9, a17\n"
  "v_accvgpr_read_b32 v10, a18\n"
  "v_accvgpr_read_b32 v11, a19\n"
  "v_accvgpr_read_b32 v12, a20\n"
  "v_accvgpr_read_b32 v13, a21\n"
  "v_accvgpr_read_b32 v14, a22\n"
  "v_accvgpr_read_b32 v15, a23\n"
  "v_cvt_pk_bf16_f32 v8, v8, v9\n"
  "v_cvt_pk_bf16_f32 v9, v10, v11\n"
  "v_cvt_pk_bf16_f32 v10, v12, v13\n"
  "v_cvt_pk_bf16_f32 v11, v14, v15\n"
  "v_mfma_f32_32x32x16_bf16 a[192:207], a[32:35], a[52:55], a[192:207]\n"
  "s_add_u32 s57, s59, 0x6400\n"
  "buffer_store_dwordx4 v[8:11], v17, s[36:39], s57 offen sc1\n"
  "ds_read_b64_tr_b16 a[58:59], v4 offset:13312\n"
  "ds_read_b128 a[76:79], v3 offset:4096\n"
  "v_mfma_f32_32x32x16_bf16 a[144:159], a[36:39], a[48:51], a[144:159]\n"
  "ds_read_b64_tr_b16 a[60:61], v5 offset:12288\n"
  "v_accvgpr_read_b32 v8, a24\n"
  "v_accvgpr_read_b32 v9, a25\n"
  "v_accvgpr_read_b32 v10, a26\n"
  "v_accvgpr_read_b32 v11, a27\n"
  "v_accvgpr_read_b32 v12, a28\n"
  "v_accvgpr_read_b32 v13, a29\n"
  "v_accvgpr_read_b32 v14, a30\n"
  "v_accvgpr_read_b32 v15, a31\n"
  "v_cvt_pk_bf16_f32 v8, v8, v9\n"
  "v_cvt_pk_bf16_f32 v9, v10, v11\n"
  "v_cvt_pk_bf16_f32 v10, v12, v13\n"
  "v_cvt_pk_bf16_f32 v11, v14, v15\n"
  "v_mfma_f32_32x32x16_bf16 a[208:223], a[36:39], a[52:55], a[208:223]\n"
  "s_waitcnt lgkmcnt(5)\n"
  "s_add_u32 s57, s59, 0x6480\n"
  "buffer_store_dwordx4 v[8:11], v17, s[36:39], s57 offen sc1\n"
  "ds_read_b64_tr_b16 a[62:63], v5 offset:13312\n"
  "ds_read_b64_tr_b16 a[64:65], v6 offset:12288\n"
  "v_mfma_f32_32x32x16_bf16 a[160:175], a[40:43], a[48:51], a[160:175]\n"
  "ds_read_b64_tr_b16 a[66:67], v6 offset:13312\n"
  "ds_read_b64_tr_b16 a[68:69], v7 offset:12288\n"
  "v_mfma_f32_32x32x16_bf16 a[224:239], a[40:43], a[52:55], a[224:239]\n"
  "ds_read_b64_tr_b16 a[70:71], v7 offset:13312\n"
  "v_mfma_f32_32x32x16_bf16 a[176:191], a[44:47], a[48:51], a[176:191]\n"
  "v_mfma_f32_32x32x16_bf16 a[240:255], a[44:47], a[52:55], a[240:255]\n"
  "s_waitcnt lgkmcnt(4)\n"
  "v_mfma_f32_32x32x16_bf16 a[128:143], a[56:59], a[72:75], a[128:143]\n"
  "s_waitcnt vmcnt(19) lgkmcnt(0)\n"
  "s_barrier\n"
  "v_add_u32 v0, 0xffff4000, v0\n"
  "ds_read_b128 a[48:51], v0 offset:0\n"
  "v_add_u32 v4, 0xffff4000, v4\n"
  "ds_read_b64_tr_b16 a[32:33], v4 offset:0\n"
  "v_cvt_pk_bf16_f32 v32, v32, v33\n"
  "v_cvt_pk_bf16_f32 v33, v34, v35\n"
  "v_cvt_pk_bf16_f32 v34, v36, v37\n"
  "v_cvt_pk_bf16_f32 v35, v38, v39\n"
  "v_mfma_f32_32x32x16_bf16 a[192:207], a[56:59], a[76:79], a[192:207]\n"
  "s_mov_b32 s55, 0x180\n"
  "s_mov_b32 s56, 0x180000\n"
  "s_add_u32 m0, s53, 0x24000\n"
  "s_nop 0\n"
  "buffer_load_dwordx4 v26, s[44:47], s56 offen offset:0 lds\n"
  "s_add_u32 s57, s58, 0x0\n"
  "buffer_store_dwordx4 v[32:35], v31, s[36:39], s57 offen sc1\n"
  "ds_read_b64_tr_b16 a[34:35], v4 offset:1024\n"
  "ds_read_b128 a[52:55], v0 offset:4096\n"
  "v_add_u32 v5, 0xffff4000, v5\n"
  "v_mfma_f32_32x32x16_bf16 a[144:159], a[60:63], a[72:75], a[144:159]\n"
  "ds_read_b64_tr_b16 a[36:37], v5 offset:0\n"
  "v_add_u32 v6, 0xffff4000, v6\n"
  "v_cvt_pk_bf16_f32 v40, v40, v41\n"
  "v_cvt_pk_bf16_f32 v41, v42, v43\n"
  "v_cvt_pk_bf16_f32 v42, v44, v45\n"
  "v_cvt_pk_bf16_f32 v43, v46, v47\n"
  "v_mfma_f32_32x32x16_bf16 a[208:223], a[60:63], a[76:79], a[208:223]\n"
  "buffer_load_dwordx4 v27, s[44:47], s56 offen offset:1024 lds\n"
  "s_add_u32 s57, s58, 0x80\n"
  "buffer_store_dwordx4 v[40:43], v31, s[36:39], s57 offen sc1\n"
  "ds_read_b64_tr_b16 a[38:39], v5 offset:1024\n"
  "ds_read_b64_tr_b16 a[40:41], v6 offset:0\n"
  "v_add_u32 v7, 0xffff4000, v7\n"
  "v_mfma_f32_32x32x16_bf16 a[160:175], a[64:67], a[72:75], a[160:175]\n"
  "ds_read_b64_tr_b16 a[42:43], v6 offset:1024\n"
  "ds_read_b64_tr_b16 a[44:45], v7 offset:0\n"
  "v_mfma_f32_32x32x16_bf16 a[224:239], a[64:67], a[76:79], a[224:239]\n"
  "buffer_load_dwordx4 v28, s[44:47], s56 offen offset:2048 lds\n"
  "ds_read_b64_tr_b16 a[46:47], v7 offset:1024\n"
  "v_add_u32 v1, 0xffff4000, v1\n"
  "v_mfma_f32_32x32x16_bf16 a[176:191], a[68:71], a[72:75], a[176:191]\n"
  "v_add_u32 v2, 0xffff4000, v2\n"
  "v_mfma_f32_32x32x16_bf16 a[240:255], a[68:71], a[76:79], a[240:255]\n"
  "buffer_load_dwordx4 v29, s[44:47], s56 offen offset:3072 lds\n"
  "s_waitcnt lgkmcnt(4)\n"
  "v_add_u32 v3, 0xffff4000, v3\n"
  "v_mfma_f32_32x32x16_bf16 a[128:143], a[32:35], a[48:51], a[128:143]\n"
  "ds_read_b128 a[72:75], v1 offset:0\n"
  "ds_read_b64_tr_b16 a[56:57], v4 offset:4096\n"
  "v_cvt_pk_bf16_f32 v48, v48, v49\n"
  "v_cvt_pk_bf16_f32 v49, v50, v51\n"
  "v_cvt_pk_bf16_f32 v50, v52, v53\n"
  "v_cvt_pk_bf16_f32 v51, v54, v55\n"
  "v_mfma_f32_32x32x16_bf16 a[192:207], a[32:35], a[52:55], a[192:207]\n"
  "s_add_u32 m0, s52, 0x1c000\n"
  "s_nop 0\n"
  "buffer_load_dwordx4 v18, s[40:43], s55 offen offset:0 lds\n"
  "s_add_u32 s57, s58, 0x800\n"
  "buffer_store_dwordx4 v[48:51], v31, s[36:39], s57 offen sc1\n"
  "ds_read_b64_tr_b16 a[58:59], v4 offset:5120\n"
  "ds_read_b128 a[76:79], v1 offset:4096\n"
  "v_mfma_f32_32x32x16_bf16 a[144:159], a[36:39], a[48:51], a[144:159]\n"
  "ds_read_b64_tr_b16 a[60:61], v5 offset:4096\n"
  "v_cvt_pk_bf16_f32 v56, v56, v57\n"
  "v_cvt_pk_bf16_f32 v57, v58, v59\n"
  "v_cvt_pk_bf16_f32 v58, v60, v61\n"
  "v_cvt_pk_bf16_f32 v59, v62, v63\n"
  "v_mfma_f32_32x32x16_bf16 a[208:223], a[36:39], a[52:55], a[208:223]\n"
  "s_waitcnt lgkmcnt(5)\n"
  "buffer_load_dwordx4 v19, s[40:43], s55 offen offset:1024 lds\n"
  "s_add_u32 s57, s58, 0x880\n"
  "buffer_store_dwordx4 v[56:59], v31, s[36:39], s57 offen sc1\n"
  "ds_read_b64_tr_b16 a[62:63], v5 offset:5120\n"
  "ds_read_b64_tr_b16 a[64:65], v6 offset:4096\n"
  "v_mfma_f32_32x32x16_bf16 a[160:175], a[40:43], a[48:51], a[160:175]\n"
  "ds_read_b64_tr_b16 a[66:67], v6 offset:5120\n"
  "ds_read_b64_tr_b16 a[68:69], v7 offset:4096\n"
  "v_mfma_f32_32x32x16_bf16 a[224:239], a[40:43], a[52:55], a[224:239]\n"
  "buffer_load_dwordx4 v20, s[40:43], s55 offen offset:2048 lds\n"
  "ds_read_b64_tr_b16 a[70:71], v7 offset:5120\n"
  "v_mfma_f32_32x32x16_bf16 a[176:191], a[44:47], a[48:51], a[176:191]\n"
  "v_mfma_f32_32x32x16_bf16 a[240:255], a[44:47], a[52:55], a[240:255]\n"
  "buffer_load_dwordx4 v21, s[40:43], s55 offen offset:3072 lds\n"
  "s_waitcnt lgkmcnt(4)\n"
  "v_mfma_f32_32x32x16_bf16 a[128:143], a[56:59], a[72:75], a[128:143]\n"
  "ds_read_b128 a[48:51], v2 offset:0\n"
  "ds_read_b64_tr_b16 a[32:33], v4 offset:8192\n"
  "v_cvt_pk_bf16_f32 v64, v64, v65\n"
  "v_cvt_pk_bf16_f32 v65, v66, v67\n"
  "v_cvt_pk_bf16_f32 v66, v68, v69\n"
  "v_cvt_pk_bf16_f32 v67, v70, v71\n"
  "v_mfma_f32_32x32x16_bf16 a[192:207], a[56:59], a[76:79], a[192:207]\n"
  "s_add_u32 m0, s52, 0x1d000\n"
  "s_nop 0\n"
  "buffer_load_dwordx4 v22, s[40:43], s55 offen offset:0 lds\n"
  "s_add_u32 s57, s59, 0x0\n"
  "buffer_store_dwordx4 v[64:67], v17, s[36:39], s57 offen sc1\n"
  "ds_read_b64_tr_b16 a[34:35], v4 offset:9216\n"
  "ds_read_b128 a[52:55], v2 offset:4096\n"
  "v_mfma_f32_32x32x16_bf16 a[144:159], a[60:63], a[72:75], a[144:159]\n"
  "ds_read_b64_tr_b16 a[36:37], v5 offset:8192\n"
  "v_cvt_pk_bf16_f32 v72, v72, v73\n"
  "v_cvt_pk_bf16_f32 v73, v74, v75\n"
  "v_cvt_pk_bf16_f32 v74, v76, v77\n"
  "v_cvt_pk_bf16_f32 v75, v78, v79\n"
  "v_mfma_f32_32x32x16_bf16 a[208:223], a[60:63], a[76:79], a[208:223]\n"
  "s_waitcnt lgkmcnt(5)\n"
  "buffer_load_dwordx4 v23, s[40:43], s55 offen offset:1024 lds\n"
  "s_add_u32 s57, s59, 0x80\n"
  "buffer_store_dwordx4 v[72:75], v17, s[36:39], s57 offen sc1\n"
  "ds_read_b64_tr_b16 a[38:39], v5 offset:9216\n"
  "ds_read_b64_tr_b16 a[40:41], v6 offset:8192\n"
  "v_mfma_f32_32x32x16_bf16 a[160:175], a[64:67], a[72:75], a[160:175]\n"
  "ds_read_b64_tr_b16 a[42:43], v6 offset:9216\n"
  "ds_read_b64_tr_b16 a[44:45], v7 offset:8192\n"
  "v_mfma_f32_32x32x16_bf16 a[224:239], a[64:67], a[76:79], a[224:239]\n"
  "buffer_load_dwordx4 v24, s[40:43], s55 offen offset:2048 lds\n"
  "ds_read_b64_tr_b16 a[46:47], v7 offset:9216\n"
  "v_mfma_f32_32x32x16_bf16 a[176:191], a[68:71], a[72:75], a[176:191]\n"
  "v_mfma_f32_32x32x16_bf16 a[240:255], a[68:71], a[76:79], a[240:255]\n"
  "buffer_load_dwordx4 v25, s[40:43], s55 offen offset:3072 lds\n"
  "s_waitcnt lgkmcnt(4)\n"
  "v_mfma_f32_32x32x16_bf16 a[128:143], a[32:35], a[48:51], a[128:143]\n"
  "ds_read_b128 a[72:75], v3 offset:0\n"
  "ds_read_b64_tr_b16 a[56:57], v4 offset:12288\n"
  "v_cvt_pk_bf16_f32 v80, v80, v81\n"
  "v_cvt_pk_bf16_f32 v81, v82, v83\n"
  "v_cvt_pk_bf16_f32 v82, v84, v85\n"
  "v_cvt_pk_bf16_f32 v83, v86, v87\n"
  "v_mfma_f32_32x32x16_bf16 a[192:207], a[32:35], a[52:55], a[192:207]\n"
  "s_add_u32 s57, s59, 0x400\n"
  "buffer_store_dwordx4 v[80:83], v17, s[36:39], s57 offen sc1\n"
  "ds_read_b64_tr_b16 a[58:59], v4 offset:13312\n"
  "ds_read_b128 a[76:79], v3 offset:4096\n"
  "v_mfma_f32_32x32x16_bf16 a[144:159], a[36:39], a[48:51], a[144:159]\n"
  "ds_read_b64_tr_b16 a[60:61], v5 offset:12288\n"
  "v_mfma_f32_32x32x16_bf16 a[208:223], a[36:39], a[52:55], a[208:223]\n"
  "s_waitcnt lgkmcnt(5)\n"
  "ds_read_b64_tr_b16 a[62:63], v5 offset:13312\n"
  "ds_read_b64_tr_b16 a[64:65], v6 offset:12288\n"
  "v_mfma_f32_32x32x16_bf16 a[160:175], a[40:43], a[48:51], a[160:175]\n"
  "ds_read_b64_tr_b16 a[66:67], v6 offset:13312\n"
  "ds_read_b64_tr_b16 a[68:69], v7 offset:12288\n"
  "v_mfma_f32_32x32x16_bf16 a[224:239], a[40:43], a[52:55], a[224:239]\n"
  "ds_read_b64_tr_b16 a[70:71], v7 offset:13312\n"
  "v_mfma_f32_32x32x16_bf16 a[176:191], a[44:47], a[48:51], a[176:191]\n"
  "v_mfma_f32_32x32x16_bf16 a[240:255], a[44:47], a[52:55], a[240:255]\n"
  "s_waitcnt lgkmcnt(4)\n"
  "v_mfma_f32_32x32x16_bf16 a[128:143], a[56:59], a[72:75], a[128:143]\n"
  "s_waitcnt vmcnt(23) lgkmcnt(0)\n"
  "s_barrier\n"
  "v_add_u32 v0, 0xffff0000, v0\n"
  "ds_read_b128 a[48:51], v0 offset:0\n"
  "v_add_u32 v4, 0xffff0000, v4\n"
  "ds_read_b64_tr_b16 a[32:33], v4 offset:0\n"
  "v_cvt_pk_bf16_f32 v88, v88, v89\n"
  "v_cvt_pk_bf16_f32 v89, v90, v91\n"
  "v_cvt_pk_bf16_f32 v90, v92, v93\n"
  "v_cvt_pk_bf16_f32 v91, v94, v95\n"
  "v_mfma_f32_32x32x16_bf16 a[192:207], a[56:59], a[76:79], a[192:207]\n"
  "s_mov_b32 s55, 0x200\n"
  "s_mov_b32 s56, 0x200000\n"
  "s_add_u32 m0, s53, 0x18000\n"
  "s_nop 0\n"
  "buffer_load_dwordx4 v26, s[44:47], s56 offen offset:0 lds\n"
  "s_add_u32 s57, s59, 0x480\n"
  "buffer_store_dwordx4 v[88:91], v17, s[36:39], s57 offen sc1\n"
  "ds_read_b64_tr_b16 a[34:35], v4 offset:1024\n"
  "ds_read_b128 a[52:55], v0 offset:4096\n"
  "v_add_u32 v5, 0xffff0000, v5\n"
  "v_mfma_f32_32x32x16_bf16 a[144:159], a[60:63], a[72:75], a[144:159]\n"
  "ds_read_b64_tr_b16 a[36:37], v5 offset:0\n"
  "v_add_u32 v6, 0xffff0000, v6\n"
  "v_mfma_f32_32x32x16_bf16 a[208:223], a[60:63], a[76:79], a[208:223]\n"
  "buffer_load_dwordx4 v27, s[44:47], s56 offen offset:1024 lds\n"
  "ds_read_b64_tr_b16 a[38:39], v5 offset:1024\n"
  "ds_read_b64_tr_b16 a[40:41], v6 offset:0\n"
  "v_add_u32 v7, 0xffff0000, v7\n"
  "v_mfma_f32_32x32x16_bf16 a[160:175], a[64:67], a[72:75], a[160:175]\n"
  "ds_read_b64_tr_b16 a[42:43], v6 offset:1024\n"
  "ds_read_b64_tr_b16 a[44:45], v7 offset:0\n"
  "v_mfma_f32_32x32x16_bf16 a[224:239], a[64:67], a[76:79], a[224:239]\n"
  "buffer_load_dwordx4 v28, s[44:47], s56 offen offset:2048 lds\n"
  "ds_read_b64_tr_b16 a[46:47], v7 offset:1024\n"
  "v_add_u32 v1, 0xffff0000, v1\n"
  "v_mfma_f32_32x32x16_bf16 a[176:191], a[68:71], a[72:75], a[176:191]\n"
  "v_add_u32 v2, 0xffff0000, v2\n"
  "v_mfma_f32_32x32x16_bf16 a[240:255], a[68:71], a[76:79], a[240:255]\n"
  "buffer_load_dwordx4 v29, s[44:47], s56 offen offset:3072 lds\n"
  "s_waitcnt lgkmcnt(4)\n"
  "v_add_u32 v3, 0xffff0000, v3\n"
  "v_mfma_f32_32x32x16_bf16 a[128:143], a[32:35], a[48:51], a[128:143]\n"
  "ds_read_b128 a[72:75], v1 offset:0\n"
  "ds_read_b64_tr_b16 a[56:57], v4 offset:4096\n"
  "v_cvt_pk_bf16_f32 v96, v96, v97\n"
  "v_cvt_pk_bf16_f32 v97, v98, v99\n"
  "v_cvt_pk_bf16_f32 v98, v100, v101\n"
  "v_cvt_pk_bf16_f32 v99, v102, v103\n"
  "v_mfma_f32_32x32x16_bf16 a[192:207], a[32:35], a[52:55], a[192:207]\n"
  "s_add_u32 m0, s52, 0x10000\n"
  "s_nop 0\n"
  "buffer_load_dwordx4 v18, s[40:43], s55 offen offset:0 lds\n"
  "s_add_u32 s57, s58, 0x2000\n"
  "buffer_store_dwordx4 v[96:99], v31, s[36:39], s57 offen sc1\n"
  "ds_read_b64_tr_b16 a[58:59], v4 offset:5120\n"
  "ds_read_b128 a[76:79], v1 offset:4096\n"
  "v_mfma_f32_32x32x16_bf16 a[144:159], a[36:39], a[48:51], a[144:159]\n"
  "ds_read_b64_tr_b16 a[60:61], v5 offset:4096\n"
  "v_mfma_f32_32x32x16_bf16 a[208:223], a[36:39], a[52:55], a[208:223]\n"
  "s_waitcnt lgkmcnt(5)\n"
  "buffer_load_dwordx4 v19, s[40:43], s55 offen offset:1024 lds\n"
  "ds_read_b64_tr_b16 a[62:63], v5 offset:5120\n"
  "ds_read_b64_tr_b16 a[64:65], v6 offset:4096\n"
  "v_mfma_f32_32x32x16_bf16 a[160:175], a[40:43], a[48:51], a[160:175]\n"
  "ds_read_b64_tr_b16 a[66:67], v6 offset:5120\n"
  "ds_read_b64_tr_b16 a[68:69], v7 offset:4096\n"
  "v_mfma_f32_32x32x16_bf16 a[224:239], a[40:43], a[52:55], a[224:239]\n"
  "buffer_load_dwordx4 v20, s[40:43], s55 offen offset:2048 lds\n"
  "ds_read_b64_tr_b16 a[70:71], v7 offset:5120\n"
  "v_mfma_f32_32x32x16_bf16 a[176:191], a[44:47], a[48:51], a[176:191]\n"
  "v_mfma_f32_32x32x16_bf16 a[240:255], a[44:47], a[52:55], a[240:255]\n"
  "buffer_load_dwordx4 v21, s[40:43], s55 offen offset:3072 lds\n"
  "s_waitcnt lgkmcnt(4)\n"
  "v_mfma_f32_32x32x16_bf16 a[128:143], a[56:59], a[72:75], a[128:143]\n"
  "ds_read_b128 a[48:51], v2 offset:0\n"
  "ds_read_b64_tr_b16 a[32:33], v4 offset:8192\n"
  "v_cvt_pk_bf16_f32 v104, v104, v105\n"
  "v_cvt_pk_bf16_f32 v105, v106, v107\n"
  "v_cvt_pk_bf16_f32 v106, v108, v109\n"
  "v_cvt_pk_bf16_f32 v107, v110, v111\n"
  "v_mfma_f32_32x32x16_bf16 a[192:207], a[56:59], a[76:79], a[192:207]\n"
  "s_add_u32 m0, s52, 0x11000\n"
  "s_nop 0\n"
  "buffer_load_dwordx4 v22, s[40:43], s55 offen offset:0 lds\n"
  "s_add_u32 s57, s58, 0x2080\n"
  "buffer_store_dwordx4 v[104:107], v31, s[36:39], s57 offen sc1\n"
  "ds_read_b64_tr_b16 a[34:35], v4 offset:9216\n"
  "ds_read_b128 a[52:55], v2 offset:4096\n"
  "v_mfma_f32_32x32x16_bf16 a[144:159], a[60:63], a[72:75], a[144:159]\n"
  "ds_read_b64_tr_b16 a[36:37], v5 offset:8192\n"
  "v_mfma_f32_32x32x16_bf16 a[208:223], a[60:63], a[76:79], a[208:223]\n"
  "s_waitcnt lgkmcnt(5)\n"
  "buffer_load_dwordx4 v23, s[40:43], s55 offen offset:1024 lds\n"
  "ds_read_b64_tr_b16 a[38:39], v5 offset:9216\n"
  "ds_read_b64_tr_b16 a[40:41], v6 offset:8192\n"
  "v_mfma_f32_32x32x16_bf16 a[160:175], a[64:67], a[72:75], a[160:175]\n"
  "ds_read_b64_tr_b16 a[42:43], v6 offset:9216\n"
  "ds_read_b64_tr_b16 a[44:45], v7 offset:8192\n"
  "v_mfma_f32_32x32x16_bf16 a[224:239], a[64:67], a[76:79], a[224:239]\n"
  "buffer_load_dwordx4 v24, s[40:43], s55 offen offset:2048 lds\n"
  "ds_read_b64_tr_b16 a[46:47], v7 offset:9216\n"
  "v_mfma_f32_32x32x16_bf16 a[176:191], a[68:71], a[72:75], a[176:191]\n"
  "v_mfma_f32_32x32x16_bf16 a[240:255], a[68:71], a[76:79], a[240:255]\n"
  "buffer_load_dwordx4 v25, s[40:43], s55 offen offset:3072 lds\n"
  "s_waitcnt lgkmcnt(4)\n"
  "v_mfma_f32_32x32x16_bf16 a[128:143], a[32:35], a[48:51], a[128:143]\n"
  "ds_read_b128 a[72:75], v3 offset:0\n"
  "ds_read_b64_tr_b16 a[56:57], v4 offset:12288\n"
  "v_cvt_pk_bf16_f32 v112, v112, v113\n"
  "v_cvt_pk_bf16_f32 v113, v114, v115\n"
  "v_cvt_pk_bf16_f32 v114, v116, v117\n"
  "v_cvt_pk_bf16_f32 v115, v118, v119\n"
  "v_mfma_f32_32x32x16_bf16 a[192:207], a[32:35], a[52:55], a[192:207]\n"
  "s_add_u32 s57, s58, 0x2800\n"
  "buffer_store_dwordx4 v[112:115], v31, s[36:39], s57 offen sc1\n"
  "ds_read_b64_tr_b16 a[58:59], v4 offset:13312\n"
  "ds_read_b128 a[76:79], v3 offset:4096\n"
  "v_mfma_f32_32x32x16_bf16 a[144:159], a[36:39], a[48:51], a[144:159]\n"
  "ds_read_b64_tr_b16 a[60:61], v5 offset:12288\n"
  "v_mfma_f32_32x32x16_bf16 a[208:223], a[36:39], a[52:55], a[208:223]\n"
  "s_waitcnt lgkmcnt(5)\n"
  "ds_read_b64_tr_b16 a[62:63], v5 offset:13312\n"
  "ds_read_b64_tr_b16 a[64:65], v6 offset:12288\n"
  "v_mfma_f32_32x32x16_bf16 a[160:175], a[40:43], a[48:51], a[160:175]\n"
  "ds_read_b64_tr_b16 a[66:67], v6 offset:13312\n"
  "ds_read_b64_tr_b16 a[68:69], v7 offset:12288\n"
  "v_mfma_f32_32x32x16_bf16 a[224:239], a[40:43], a[52:55], a[224:239]\n"
  "ds_read_b64_tr_b16 a[70:71], v7 offset:13312\n"
  "v_mfma_f32_32x32x16_bf16 a[176:191], a[44:47], a[48:51], a[176:191]\n"
  "v_mfma_f32_32x32x16_bf16 a[240:255], a[44:47], a[52:55], a[240:255]\n"
  "s_waitcnt lgkmcnt(4)\n"
  "v_mfma_f32_32x32x16_bf16 a[128:143], a[56:59], a[72:75], a[128:143]\n"
  "s_waitcnt vmcnt(17) lgkmcnt(0)\n"
  "s_barrier\n"
  "v_add_u32 v0, 0x1c000, v0\n"
  "ds_read_b128 a[48:51], v0 offset:0\n"
  "v_add_u32 v4, 0x1c000, v4\n"
  "ds_read_b64_tr_b16 a[32:33], v4 offset:0\n"
  "v_cvt_pk_bf16_f32 v120, v120, v121\n"
  "v_cvt_pk_bf16_f32 v121, v122, v123\n"
  "v_cvt_pk_bf16_f32 v122, v124, v125\n"
  "v_cvt_pk_bf16_f32 v123, v126, v127\n"
  "v_mfma_f32_32x32x16_bf16 a[192:207], a[56:59], a[76:79], a[192:207]\n"
  "s_mov_b32 s55, 0x280\n"
  "s_mov_b32 s56, 0x280000\n"
  "s_add_u32 m0, s53, 0x8000\n"
  "s_nop 0\n"
  "buffer_load_dwordx4 v26, s[44:47], s56 offen offset:0 lds\n"
  "s_add_u32 s57, s58, 0x2880\n"
  "buffer_store_dwordx4 v[120:123], v31, s[36:39], s57 offen sc1\n"
  "ds_read_b64_tr_b16 a[34:35], v4 offset:1024\n"
  "ds_read_b128 a[52:55], v0 offset:4096\n"
  "v_add_u32 v5, 0x1c000, v5\n"
  "v_mfma_f32_32x32x16_bf16 a[144:159], a[60:63], a[72:75], a[144:159]\n"
  "ds_read_b64_tr_b16 a[36:37], v5 offset:0\n"
  "v_add_u32 v6, 0x1c000, v6\n"
  "v_mfma_f32_32x32x16_bf16 a[208:223], a[60:63], a[76:79], a[208:223]\n"
  "buffer_load_dwordx4 v27, s[44:47], s56 offen offset:1024 lds\n"
  "ds_read_b64_tr_b16 a[38:39], v5 offset:1024\n"
  "ds_read_b64_tr_b16 a[40:41], v6 offset:0\n"
  "v_add_u32 v7, 0x1c000, v7\n"
  "v_mfma_f32_32x32x16_bf16 a[160:175], a[64:67], a[72:75], a[160:175]\n"
  "ds_read_b64_tr_b16 a[42:43], v6 offset:1024\n"
  "ds_read_b64_tr_b16 a[44:45], v7 offset:0\n"
  "v_mfma_f32_32x32x16_bf16 a[224:239], a[64:67], a[76:79], a[224:239]\n"
  "buffer_load_dwordx4 v28, s[44:47], s56 offen offset:2048 lds\n"
  "ds_read_b64_tr_b16 a[46:47], v7 offset:1024\n"
  "v_add_u32 v1, 0x1c000, v1\n"
  "v_mfma_f32_32x32x16_bf16 a[176:191], a[68:71], a[72:75], a[176:191]\n"
  "v_add_u32 v2, 0x1c000, v2\n"
  "v_mfma_f32_32x32x16_bf16 a[240:255], a[68:71], a[76:79], a[240:255]\n"
  "buffer_load_dwordx4 v29, s[44:47], s56 offen offset:3072 lds\n"
  "s_waitcnt lgkmcnt(4)\n"
  "v_add_u32 v3, 0x1c000, v3\n"
  "v_mfma_f32_32x32x16_bf16 a[128:143], a[32:35], a[48:51], a[128:143]\n"
  "ds_read_b128 a[72:75], v1 offset:0\n"
  "ds_read_b64_tr_b16 a[56:57], v4 offset:4096\n"
  "v_cvt_pk_bf16_f32 v128, v128, v129\n"
  "v_cvt_pk_bf16_f32 v129, v130, v131\n"
  "v_cvt_pk_bf16_f32 v130, v132, v133\n"
  "v_cvt_pk_bf16_f32 v131, v134, v135\n"
  "v_mfma_f32_32x32x16_bf16 a[192:207], a[32:35], a[52:55], a[192:207]\n"
  "s_add_u32 m0, s52, 0x0\n"
  "s_nop 0\n"
  "buffer_load_dwordx4 v18, s[40:43], s55 offen offset:0 lds\n"
  "s_add_u32 s57, s59, 0x2000\n"
  "buffer_store_dwordx4 v[128:131], v17, s[36:39], s57 offen sc1\n"
  "ds_read_b64_tr_b16 a[58:59], v4 offset:5120\n"
  "ds_read_b128 a[76:79], v1 offset:4096\n"
  "v_mfma_f32_32x32x16_bf16 a[144:159], a[36:39], a[48:51], a[144:159]\n"
  "ds_read_b64_tr_b16 a[60:61], v5 offset:4096\n"
  "v_mfma_f32_32x32x16_bf16 a[208:223], a[36:39], a[52:55], a[208:223]\n"
  "s_waitcnt lgkmcnt(5)\n"
  "buffer_load_dwordx4 v19, s[40:43], s55 offen offset:1024 lds\n"
  "ds_read_b64_tr_b16 a[62:63], v5 offset:5120\n"
  "ds_read_b64_tr_b16 a[64:65], v6 offset:4096\n"
  "v_mfma_f32_32x32x16_bf16 a[160:175], a[40:43], a[48:51], a[160:175]\n"
  "ds_read_b64_tr_b16 a[66:67], v6 offset:5120\n"
  "ds_read_b64_tr_b16 a[68:69], v7 offset:4096\n"
  "v_mfma_f32_32x32x16_bf16 a[224:239], a[40:43], a[52:55], a[224:239]\n"
  "buffer_load_dwordx4 v20, s[40:43], s55 offen offset:2048 lds\n"
  "ds_read_b64_tr_b16 a[70:71], v7 offset:5120\n"
  "v_mfma_f32_32x32x16_bf16 a[176:191], a[44:47], a[48:51], a[176:191]\n"
  "v_mfma_f32_32x32x16_bf16 a[240:255], a[44:47], a[52:55], a[240:255]\n"
  "buffer_load_dwordx4 v21, s[40:43], s55 offen offset:3072 lds\n"
  "s_waitcnt lgkmcnt(4)\n"
  "v_mfma_f32_32x32x16_bf16 a[128:143], a[56:59], a[72:75], a[128:143]\n"
  "ds_read_b128 a[48:51], v2 offset:0\n"
  "ds_read_b64_tr_b16 a[32:33], v4 offset:8192\n"
  "v_cvt_pk_bf16_f32 v136, v136, v137\n"
  "v_cvt_pk_bf16_f32 v137, v138, v139\n"
  "v_cvt_pk_bf16_f32 v138, v140, v141\n"
  "v_cvt_pk_bf16_f32 v139, v142, v143\n"
  "v_mfma_f32_32x32x16_bf16 a[192:207], a[56:59], a[76:79], a[192:207]\n"
  "s_add_u32 m0, s52, 0x1000\n"
  "s_nop 0\n"
  "buffer_load_dwordx4 v22, s[40:43], s55 offen offset:0 lds\n"
  "s_add_u32 s57, s59, 0x2080\n"
  "buffer_store_dwordx4 v[136:139], v17, s[36:39], s57 offen sc1\n"
  "ds_read_b64_tr_b16 a[34:35], v4 offset:9216\n"
  "ds_read_b128 a[52:55], v2 offset:4096\n"
  "v_mfma_f32_32x32x16_bf16 a[144:159], a[60:63], a[72:75], a[144:159]\n"
  "ds_read_b64_tr_b16 a[36:37], v5 offset:8192\n"
  "v_mfma_f32_32x32x16_bf16 a[208:223], a[60:63], a[76:79], a[208:223]\n"
  "s_waitcnt lgkmcnt(5)\n"
  "buffer_load_dwordx4 v23, s[40:43], s55 offen offset:1024 lds\n"
  "ds_read_b64_tr_b16 a[38:39], v5 offset:9216\n"
  "ds_read_b64_tr_b16 a[40:41], v6 offset:8192\n"
  "v_mfma_f32_32x32x16_bf16 a[160:175], a[64:67], a[72:75], a[160:175]\n"
  "ds_read_b64_tr_b16 a[42:43], v6 offset:9216\n"
  "ds_read_b64_tr_b16 a[44:45], v7 offset:8192\n"
  "v_mfma_f32_32x32x16_bf16 a[224:239], a[64:67], a[76:79], a[224:239]\n"
  "buffer_load_dwordx4 v24, s[40:43], s55 offen offset:2048 lds\n"
  "ds_read_b64_tr_b16 a[46:47], v7 offset:9216\n"
  "v_mfma_f32_32x32x16_bf16 a[176:191], a[68:71], a[72:75], a[176:191]\n"
  "v_mfma_f32_32x32x16_bf16 a[240:255], a[68:71], a[76:79], a[240:255]\n"
  "buffer_load_dwordx4 v25, s[40:43], s55 offen offset:3072 lds\n"
  "s_waitcnt lgkmcnt(4)\n"
  "v_mfma_f32_32x32x16_bf16 a[128:143], a[32:35], a[48:51], a[128:143]\n"
  "ds_read_b128 a[72:75], v3 offset:0\n"
  "ds_read_b64_tr_b16 a[56:57], v4 offset:12288\n"
  "v_cvt_pk_bf16_f32 v144, v144, v145\n"
  "v_cvt_pk_bf16_f32 v145, v146, v147\n"
  "v_cvt_pk_bf16_f32 v146, v148, v149\n"
  "v_cvt_pk_bf16_f32 v147, v150, v151\n"
  "v_mfma_f32_32x32x16_bf16 a[192:207], a[32:35], a[52:55], a[192:207]\n"
  "s_add_u32 s57, s59, 0x2400\n"
  "buffer_store_dwordx4 v[144:147], v17, s[36:39], s57 offen sc1\n"
  "ds_read_b64_tr_b16 a[58:59], v4 offset:13312\n"
  "ds_read_b128 a[76:79], v3 offset:4096\n"
  "v_mfma_f32_32x32x16_bf16 a[144:159], a[36:39], a[48:51], a[144:159]\n"
  "ds_read_b64_tr_b16 a[60:61], v5 offset:12288\n"
  "v_mfma_f32_32x32x16_bf16 a[208:223], a[36:39], a[52:55], a[208:223]\n"
  "s_waitcnt lgkmcnt(5)\n"
  "ds_read_b64_tr_b16 a[62:63], v5 offset:13312\n"
  "ds_read_b64_tr_b16 a[64:65], v6 offset:12288\n"
  "v_mfma_f32_32x32x16_bf16 a[160:175], a[40:43], a[48:51], a[160:175]\n"
  "ds_read_b64_tr_b16 a[66:67], v6 offset:13312\n"
  "ds_read_b64_tr_b16 a[68:69], v7 offset:12288\n"
  "v_mfma_f32_32x32x16_bf16 a[224:239], a[40:43], a[52:55], a[224:239]\n"
  "ds_read_b64_tr_b16 a[70:71], v7 offset:13312\n"
  "v_mfma_f32_32x32x16_bf16 a[176:191], a[44:47], a[48:51], a[176:191]\n"
  "v_mfma_f32_32x32x16_bf16 a[240:255], a[44:47], a[52:55], a[240:255]\n"
  "s_waitcnt lgkmcnt(4)\n"
  "v_mfma_f32_32x32x16_bf16 a[128:143], a[56:59], a[72:75], a[128:143]\n"
  "s_waitcnt vmcnt(17) lgkmcnt(0)\n"
  "s_barrier\n"
  "v_add_u32 v0, 0xffff4000, v0\n"
  "ds_read_b128 a[48:51], v0 offset:0\n"
  "v_add_u32 v4, 0xffff4000, v4\n"
  "ds_read_b64_tr_b16 a[32:33], v4 offset:0\n"
  "v_cvt_pk_bf16_f32 v152, v152, v153\n"
  "v_cvt_pk_bf16_f32 v153, v154, v155\n"
  "v_cvt_pk_bf16_f32 v154, v156, v157\n"
  "v_cvt_pk_bf16_f32 v155, v158, v159\n"
  "v_mfma_f32_32x32x16_bf16 a[192:207], a[56:59], a[76:79], a[192:207]\n"
  "s_mov_b32 s55, 0x300\n"
  "s_mov_b32 s56, 0x300000\n"
  "s_add_u32 m0, s53, 0x24000\n"
  "s_nop 0\n"
  "buffer_load_dwordx4 v26, s[44:47], s56 offen offset:0 lds\n"
  "s_add_u32 s57, s59, 0x2480\n"
  "buffer_store_dwordx4 v[152:155], v17, s[36:39], s57 offen sc1\n"
  "ds_read_b64_tr_b16 a[34:35], v4 offset:1024\n"
  "ds_read_b128 a[52:55], v0 offset:4096\n"
  "v_add_u32 v5, 0xffff4000, v5\n"
  "v_mfma_f32_32x32x16_bf16 a[144:159], a[60:63], a[72:75], a[144:159]\n"
  "ds_read_b64_tr_b16 a[36:37], v5 offset:0\n"
  "v_add_u32 v6, 0xffff4000, v6\n"
  "v_mfma_f32_32x32x16_bf16 a[208:223], a[60:63], a[76:79], a[208:223]\n"
  "buffer_load_dwordx4 v27, s[44:47], s56 offen offset:1024 lds\n"
  "ds_read_b64_tr_b16 a[38:39], v5 offset:1024\n"
  "ds_read_b64_tr_b16 a[40:41], v6 offset:0\n"
  "v_add_u32 v7, 0xffff4000, v7\n"
  "v_mfma_f32_32x32x16_bf16 a[160:175], a[64:67], a[72:75], a[160:175]\n"
  "ds_read_b64_tr_b16 a[42:43], v6 offset:1024\n"
  "ds_read_b64_tr_b16 a[44:45], v7 offset:0\n"
  "v_mfma_f32_32x32x16_bf16 a[224:239], a[64:67], a[76:79], a[224:239]\n"
  "buffer_load_dwordx4 v28, s[44:47], s56 offen offset:2048 lds\n"
  "ds_read_b64_tr_b16 a[46:47], v7 offset:1024\n"
  "v_add_u32 v1, 0xffff4000, v1\n"
  "v_mfma_f32_32x32x16_bf16 a[176:191], a[68:71], a[72:75], a[176:191]\n"
  "v_add_u32 v2, 0xffff4000, v2\n"
  "v_mfma_f32_32x32x16_bf16 a[240:255], a[68:71], a[76:79], a[240:255]\n"
  "buffer_load_dwordx4 v29, s[44:47], s56 offen offset:3072 lds\n"
  "s_waitcnt lgkmcnt(4)\n"
  "v_add_u32 v3, 0xffff4000, v3\n"
  "v_mfma_f32_32x32x16_bf16 a[128:143], a[32:35], a[48:51], a[128:143]\n"
  "ds_read_b128 a[72:75], v1 offset:0\n"
  "ds_read_b64_tr_b16 a[56:57], v4 offset:4096\n"
  "v_cvt_pk_bf16_f32 v160, v160, v161\n"
  "v_cvt_pk_bf16_f32 v161, v162, v163\n"
  "v_cvt_pk_bf16_f32 v162, v164, v165\n"
  "v_cvt_pk_bf16_f32 v163, v166, v167\n"
  "v_mfma_f32_32x32x16_bf16 a[192:207], a[32:35], a[52:55], a[192:207]\n"
  "s_add_u32 m0, s52, 0x1c000\n"
  "s_nop 0\n"
  "buffer_load_dwordx4 v18, s[40:43], s55 offen offset:0 lds\n"
  "s_add_u32 s57, s58, 0x4000\n"
  "buffer_store_dwordx4 v[160:163], v31, s[36:39], s57 offen sc1\n"
  "ds_read_b64_tr_b16 a[58:59], v4 offset:5120\n"
  "ds_read_b128 a[76:79], v1 offset:4096\n"
  "v_mfma_f32_32x32x16_bf16 a[144:159], a[36:39], a[48:51], a[144:159]\n"
  "ds_read_b64_tr_b16 a[60:61], v5 offset:4096\n"
  "v_mfma_f32_32x32x16_bf16 a[208:223], a[36:39], a[52:55], a[208:223]\n"
  "s_waitcnt lgkmcnt(5)\n"
  "buffer_load_dwordx4 v19, s[40:43], s55 offen offset:1024 lds\n"
  "ds_read_b64_tr_b16 a[62:63], v5 offset:5120\n"
  "ds_read_b64_tr_b16 a[64:65], v6 offset:4096\n"
  "v_mfma_f32_32x32x16_bf16 a[160:175], a[40:43], a[48:51], a[160:175]\n"
  "ds_read_b64_tr_b16 a[66:67], v6 offset:5120\n"
  "ds_read_b64_tr_b16 a[68:69], v7 offset:4096\n"
  "v_mfma_f32_32x32x16_bf16 a[224:239], a[40:43], a[52:55], a[224:239]\n"
  "buffer_load_dwordx4 v20, s[40:43], s55 offen offset:2048 lds\n"
  "ds_read_b64_tr_b16 a[70:71], v7 offset:5120\n"
  "v_mfma_f32_32x32x16_bf16 a[176:191], a[44:47], a[48:51], a[176:191]\n"
  "v_mfma_f32_32x32x16_bf16 a[240:255], a[44:47], a[52:55], a[240:255]\n"
  "buffer_load_dwordx4 v21, s[40:43], s55 offen offset:3072 lds\n"
  "s_waitcnt lgkmcnt(4)\n"
  "v_mfma_f32_32x32x16_bf16 a[128:143], a[56:59], a[72:75], a[128:143]\n"
  "ds_read_b128 a[48:51], v2 offset:0\n"
  "ds_read_b64_tr_b16 a[32:33], v4 offset:8192\n"
  "v_cvt_pk_bf16_f32 v168, v168, v169\n"
  "v_cvt_pk_bf16_f32 v169, v170, v171\n"
  "v_cvt_pk_bf16_f32 v170, v172, v173\n"
  "v_cvt_pk_bf16_f32 v171, v174, v175\n"
  "v_mfma_f32_32x32x16_bf16 a[192:207], a[56:59], a[76:79], a[192:207]\n"
  "s_add_u32 m0, s52, 0x1d000\n"
  "s_nop 0\n"
  "buffer_load_dwordx4 v22, s[40:43], s55 offen offset:0 lds\n"
  "s_add_u32 s57, s58, 0x4080\n"
  "buffer_store_dwordx4 v[168:171], v31, s[36:39], s57 offen sc1\n"
  "ds_read_b64_tr_b16 a[34:35], v4 offset:9216\n"
  "ds_read_b128 a[52:55], v2 offset:4096\n"
  "v_mfma_f32_32x32x16_bf16 a[144:159], a[60:63], a[72:75], a[144:159]\n"
  "ds_read_b64_tr_b16 a[36:37], v5 offset:8192\n"
  "v_mfma_f32_32x32x16_bf16 a[208:223], a[60:63], a[76:79], a[208:223]\n"
  "s_waitcnt lgkmcnt(5)\n"
  "buffer_load_dwordx4 v23, s[40:43], s55 offen offset:1024 lds\n"
  "ds_read_b64_tr_b16 a[38:39], v5 offset:9216\n"
  "ds_read_b64_tr_b16 a[40:41], v6 offset:8192\n"
  "v_mfma_f32_32x32x16_bf16 a[160:175], a[64:67], a[72:75], a[160:175]\n"
  "ds_read_b64_tr_b16 a[42:43], v6 offset:9216\n"
  "ds_read_b64_tr_b16 a[44:45], v7 offset:8192\n"
  "v_mfma_f32_32x32x16_bf16 a[224:239], a[64:67], a[76:79], a[224:239]\n"
  "buffer_load_dwordx4 v24, s[40:43], s55 offen offset:2048 lds\n"
  "ds_read_b64_tr_b16 a[46:47], v7 offset:9216\n"
  "v_mfma_f32_32x32x16_bf16 a[176:191], a[68:71], a[72:75], a[176:191]\n"
  "v_mfma_f32_32x32x16_bf16 a[240:255], a[68:71], a[76:79], a[240:255]\n"
  "buffer_load_dwordx4 v25, s[40:43], s55 offen offset:3072 lds\n"
  "s_waitcnt lgkmcnt(4)\n"
  "v_mfma_f32_32x32x16_bf16 a[128:143], a[32:35], a[48:51], a[128:143]\n"
  "ds_read_b128 a[72:75], v3 offset:0\n"
  "ds_read_b64_tr_b16 a[56:57], v4 offset:12288\n"
  "v_cvt_pk_bf16_f32 v176, v176, v177\n"
  "v_cvt_pk_bf16_f32 v177, v178, v179\n"
  "v_cvt_pk_bf16_f32 v178, v180, v181\n"
  "v_cvt_pk_bf16_f32 v179, v182, v183\n"
  "v_mfma_f32_32x32x16_bf16 a[192:207], a[32:35], a[52:55], a[192:207]\n"
  "s_add_u32 s57, s58, 0x4800\n"
  "buffer_store_dwordx4 v[176:179], v31, s[36:39], s57 offen sc1\n"
  "ds_read_b64_tr_b16 a[58:59], v4 offset:13312\n"
  "ds_read_b128 a[76:79], v3 offset:4096\n"
  "v_mfma_f32_32x32x16_bf16 a[144:159], a[36:39], a[48:51], a[144:159]\n"
  "ds_read_b64_tr_b16 a[60:61], v5 offset:12288\n"
  "v_mfma_f32_32x32x16_bf16 a[208:223], a[36:39], a[52:55], a[208:223]\n"
  "s_waitcnt lgkmcnt(5)\n"
  "ds_read_b64_tr_b16 a[62:63], v5 offset:13312\n"
  "ds_read_b64_tr_b16 a[64:65], v6 offset:12288\n"
  "v_mfma_f32_32x32x16_bf16 a[160:175], a[40:43], a[48:51], a[160:175]\n"
  "ds_read_b64_tr_b16 a[66:67], v6 offset:13312\n"
  "ds_read_b64_tr_b16 a[68:69], v7 offset:12288\n"
  "v_mfma_f32_32x32x16_bf16 a[224:239], a[40:43], a[52:55], a[224:239]\n"
  "ds_read_b64_tr_b16 a[70:71], v7 offset:13312\n"
  "v_mfma_f32_32x32x16_bf16 a[176:191], a[44:47], a[48:51], a[176:191]\n"
  "v_mfma_f32_32x32x16_bf16 a[240:255], a[44:47], a[52:55], a[240:255]\n"
  "s_waitcnt lgkmcnt(4)\n"
  "v_mfma_f32_32x32x16_bf16 a[128:143], a[56:59], a[72:75], a[128:143]\n"
  "s_waitcnt vmcnt(17) lgkmcnt(0)\n"
  "s_barrier\n"
  "v_add_u32 v0, 0xffff0000, v0\n"
  "ds_read_b128 a[48:51], v0 offset:0\n"
  "v_add_u32 v4, 0xffff0000, v4\n"
  "ds_read_b64_tr_b16 a[32:33], v4 offset:0\n"
  "v_cvt_pk_bf16_f32 v184, v184, v185\n"
  "v_cvt_pk_bf16_f32 v185, v186, v187\n"
  "v_cvt_pk_bf16_f32 v186, v188, v189\n"
  "v_cvt_pk_bf16_f32 v187, v190, v191\n"
  "v_mfma_f32_32x32x16_bf16 a[192:207], a[56:59], a[76:79], a[192:207]\n"
  "s_mov_b32 s55, 0x380\n"
  "s_mov_b32 s56, 0x380000\n"
  "s_add_u32 m0, s53, 0x18000\n"
  "s_nop 0\n"
  "buffer_load_dwordx4 v26, s[44:47], s56 offen offset:0 lds\n"
  "s_add_u32 s57, s58, 0x4880\n"
  "buffer_store_dwordx4 v[184:187], v31, s[36:39], s57 offen sc1\n"
  "ds_read_b64_tr_b16 a[34:35], v4 offset:1024\n"
  "ds_read_b128 a[52:55], v0 offset:4096\n"
  "v_add_u32 v5, 0xffff0000, v5\n"
  "v_mfma_f32_32x32x16_bf16 a[144:159], a[60:63], a[72:75], a[144:159]\n"
  "ds_read_b64_tr_b16 a[36:37], v5 offset:0\n"
  "v_add_u32 v6, 0xffff0000, v6\n"
  "v_mfma_f32_32x32x16_bf16 a[208:223], a[60:63], a[76:79], a[208:223]\n"
  "buffer_load_dwordx4 v27, s[44:47], s56 offen offset:1024 lds\n"
  "ds_read_b64_tr_b16 a[38:39], v5 offset:1024\n"
  "ds_read_b64_tr_b16 a[40:41], v6 offset:0\n"
  "v_add_u32 v7, 0xffff0000, v7\n"
  "v_mfma_f32_32x32x16_bf16 a[160:175], a[64:67], a[72:75], a[160:175]\n"
  "ds_read_b64_tr_b16 a[42:43], v6 offset:1024\n"
  "ds_read_b64_tr_b16 a[44:45], v7 offset:0\n"
  "v_mfma_f32_32x32x16_bf16 a[224:239], a[64:67], a[76:79], a[224:239]\n"
  "buffer_load_dwordx4 v28, s[44:47], s56 offen offset:2048 lds\n"
  "ds_read_b64_tr_b16 a[46:47], v7 offset:1024\n"
  "v_add_u32 v1, 0xffff0000, v1\n"
  "v_mfma_f32_32x32x16_bf16 a[176:191], a[68:71], a[72:75], a[176:191]\n"
  "v_add_u32 v2, 0xffff0000, v2\n"
  "v_mfma_f32_32x32x16_bf16 a[240:255], a[68:71], a[76:79], a[240:255]\n"
  "buffer_load_dwordx4 v29, s[44:47], s56 offen offset:3072 lds\n"
  "s_waitcnt lgkmcnt(4)\n"
  "v_add_u32 v3, 0xffff0000, v3\n"
  "v_mfma_f32_32x32x16_bf16 a[128:143], a[32:35], a[48:51], a[128:143]\n"
  "ds_read_b128 a[72:75], v1 offset:0\n"
  "ds_read_b64_tr_b16 a[56:57], v4 offset:4096\n"
  "v_cvt_pk_bf16_f32 v192, v192, v193\n"
  "v_cvt_pk_bf16_f32 v193, v194, v195\n"
  "v_cvt_pk_bf16_f32 v194, v196, v197\n"
  "v_cvt_pk_bf16_f32 v195, v198, v199\n"
  "v_mfma_f32_32x32x16_bf16 a[192:207], a[32:35], a[52:55], a[192:207]\n"
  "s_add_u32 m0, s52, 0x10000\n"
  "s_nop 0\n"
  "buffer_load_dwordx4 v18, s[40:43], s55 offen offset:0 lds\n"
  "s_add_u32 s57, s59, 0x4000\n"
  "buffer_store_dwordx4 v[192:195], v17, s[36:39], s57 offen sc1\n"
  "ds_read_b64_tr_b16 a[58:59], v4 offset:5120\n"
  "ds_read_b128 a[76:79], v1 offset:4096\n"
  "v_mfma_f32_32x32x16_bf16 a[144:159], a[36:39], a[48:51], a[144:159]\n"
  "ds_read_b64_tr_b16 a[60:61], v5 offset:4096\n"
  "v_accvgpr_write_b32 a126, 0\n"
  "v_mfma_f32_32x32x16_bf16 a[208:223], a[36:39], a[52:55], a[208:223]\n"
  "s_waitcnt lgkmcnt(5)\n"
  "buffer_load_dwordx4 v19, s[40:43], s55 offen offset:1024 lds\n"
  "ds_read_b64_tr_b16 a[62:63], v5 offset:5120\n"
  "ds_read_b64_tr_b16 a[64:65], v6 offset:4096\n"
  "v_accvgpr_write_b32 a125, 0\n"
  "v_mfma_f32_32x32x16_bf16 a[160:175], a[40:43], a[48:51], a[160:175]\n"
  "ds_read_b64_tr_b16 a[66:67], v6 offset:5120\n"
  "ds_read_b64_tr_b16 a[68:69], v7 offset:4096\n"
  "v_accvgpr_write_b32 a124, 0\n"
  "v_mfma_f32_32x32x16_bf16 a[224:239], a[40:43], a[52:55], a[224:239]\n"
  "buffer_load_dwordx4 v20, s[40:43], s55 offen offset:2048 lds\n"
  "ds_read_b64_tr_b16 a[70:71], v7 offset:5120\n"
  "v_accvgpr_write_b32 a123, 0\n"
  "v_mfma_f32_32x32x16_bf16 a[176:191], a[44:47], a[48:51], a[176:191]\n"
  "v_accvgpr_write_b32 a122, 0\n"
  "v_mfma_f32_32x32x16_bf16 a[240:255], a[44:47], a[52:55], a[240:255]\n"
  "buffer_load_dwordx4 v21, s[40:43], s55 offen offset:3072 lds\n"
  "s_waitcnt lgkmcnt(4)\n"
  "v_accvgpr_write_b32 a121, 0\n"
  "v_mfma_f32_32x32x16_bf16 a[128:143], a[56:59], a[72:75], a[128:143]\n"
  "ds_read_b128 a[48:51], v2 offset:0\n"
  "ds_read_b64_tr_b16 a[32:33], v4 offset:8192\n"
  "v_cvt_pk_bf16_f32 v200, v200, v201\n"
  "v_cvt_pk_bf16_f32 v201, v202, v203\n"
  "v_cvt_pk_bf16_f32 v202, v204, v205\n"
  "v_cvt_pk_bf16_f32 v203, v206, v207\n"
  "v_mfma_f32_32x32x16_bf16 a[192:207], a[56:59], a[76:79], a[192:207]\n"
  "s_add_u32 m0, s52, 0x11000\n"
  "s_nop 0\n"
  "buffer_load_dwordx4 v22, s[40:43], s55 offen offset:0 lds\n"
  "s_add_u32 s57, s59, 0x4080\n"
  "buffer_store_dwordx4 v[200:203], v17, s[36:39], s57 offen sc1\n"
  "ds_read_b64_tr_b16 a[34:35], v4 offset:9216\n"
  "ds_read_b128 a[52:55], v2 offset:4096\n"
  "v_mfma_f32_32x32x16_bf16 a[144:159], a[60:63], a[72:75], a[144:159]\n"
  "ds_read_b64_tr_b16 a[36:37], v5 offset:8192\n"
  "v_accvgpr_write_b32 a120, 0\n"
  "v_mfma_f32_32x32x16_bf16 a[208:223], a[60:63], a[76:79], a[208:223]\n"
  "s_waitcnt lgkmcnt(5)\n"
  "buffer_load_dwordx4 v23, s[40:43], s55 offen offset:1024 lds\n"
  "ds_read_b64_tr_b16 a[38:39], v5 offset:9216\n"
  "ds_read_b64_tr_b16 a[40:41], v6 offset:8192\n"
  "v_accvgpr_write_b32 a119, 0\n"
  "v_mfma_f32_32x32x16_bf16 a[160:175], a[64:67], a[72:75], a[160:175]\n"
  "ds_read_b64_tr_b16 a[42:43], v6 offset:9216\n"
  "ds_read_b64_tr_b16 a[44:45], v7 offset:8192\n"
  "v_accvgpr_write_b32 a118, 0\n"
  "v_mfma_f32_32x32x16_bf16 a[224:239], a[64:67], a[76:79], a[224:239]\n"
  "buffer_load_dwordx4 v24, s[40:43], s55 offen offset:2048 lds\n"
  "ds_read_b64_tr_b16 a[46:47], v7 offset:9216\n"
  "v_accvgpr_write_b32 a117, 0\n"
  "v_mfma_f32_32x32x16_bf16 a[176:191], a[68:71], a[72:75], a[176:191]\n"
  "v_accvgpr_write_b32 a116, 0\n"
  "v_mfma_f32_32x32x16_bf16 a[240:255], a[68:71], a[76:79], a[240:255]\n"
  "buffer_load_dwordx4 v25, s[40:43], s55 offen offset:3072 lds\n"
  "s_waitcnt lgkmcnt(4)\n"
  "v_accvgpr_write_b32 a115, 0\n"
  "v_mfma_f32_32x32x16_bf16 a[128:143], a[32:35], a[48:51], a[128:143]\n"
  "ds_read_b128 a[72:75], v3 offset:0\n"
  "ds_read_b64_tr_b16 a[56:57], v4 offset:12288\n"
  "v_cvt_pk_bf16_f32 v208, v208, v209\n"
  "v_cvt_pk_bf16_f32 v209, v210, v211\n"
  "v_cvt_pk_bf16_f32 v210, v212, v213\n"
  "v_cvt_pk_bf16_f32 v211, v214, v215\n"
  "v_mfma_f32_32x32x16_bf16 a[192:207], a[32:35], a[52:55], a[192:207]\n"
  "s_add_u32 s57, s59, 0x4400\n"
  "buffer_store_dwordx4 v[208:211], v17, s[36:39], s57 offen sc1\n"
  "ds_read_b64_tr_b16 a[58:59], v4 offset:13312\n"
  "ds_read_b128 a[76:79], v3 offset:4096\n"
  "v_mfma_f32_32x32x16_bf16 a[144:159], a[36:39], a[48:51], a[144:159]\n"
  "ds_read_b64_tr_b16 a[60:61], v5 offset:12288\n"
  "v_accvgpr_write_b32 a114, 0\n"
  "v_mfma_f32_32x32x16_bf16 a[208:223], a[36:39], a[52:55], a[208:223]\n"
  "s_waitcnt lgkmcnt(5)\n"
  "ds_read_b64_tr_b16 a[62:63], v5 offset:13312\n"
  "ds_read_b64_tr_b16 a[64:65], v6 offset:12288\n"
  "v_accvgpr_write_b32 a113, 0\n"
  "v_mfma_f32_32x32x16_bf16 a[160:175], a[40:43], a[48:51], a[160:175]\n"
  "ds_read_b64_tr_b16 a[66:67], v6 offset:13312\n"
  "ds_read_b64_tr_b16 a[68:69], v7 offset:12288\n"
  "v_accvgpr_write_b32 a112, 0\n"
  "v_mfma_f32_32x32x16_bf16 a[224:239], a[40:43], a[52:55], a[224:239]\n"
  "ds_read_b64_tr_b16 a[70:71], v7 offset:13312\n"
  "v_accvgpr_write_b32 a111, 0\n"
  "v_mfma_f32_32x32x16_bf16 a[176:191], a[44:47], a[48:51], a[176:191]\n"
  "v_accvgpr_write_b32 a110, 0\n"
  "v_mfma_f32_32x32x16_bf16 a[240:255], a[44:47], a[52:55], a[240:255]\n"
  "s_waitcnt lgkmcnt(4)\n"
  "v_accvgpr_write_b32 a109, 0\n"
  "v_mfma_f32_32x32x16_bf16 a[128:143], a[56:59], a[72:75], a[128:143]\n"
  "s_waitcnt vmcnt(17) lgkmcnt(0)\n"
  "s_barrier\n"
  "v_add_u32 v0, 0x1c000, v0\n"
  "ds_read_b128 a[48:51], v0 offset:0\n"
  "v_add_u32 v4, 0x1c000, v4\n"
  "ds_read_b64_tr_b16 a[32:33], v4 offset:0\n"
  "v_cvt_pk_bf16_f32 v216, v216, v217\n"
  "v_cvt_pk_bf16_f32 v217, v218, v219\n"
  "v_cvt_pk_bf16_f32 v218, v220, v221\n"
  "v_cvt_pk_bf16_f32 v219, v222, v223\n"
  "v_mfma_f32_32x32x16_bf16 a[192:207], a[56:59], a[76:79], a[192:207]\n"
  "s_mov_b32 s55, 0x400\n"
  "s_mov_b32 s56, 0x400000\n"
  "s_add_u32 m0, s53, 0x8000\n"
  "s_nop 0\n"
  "buffer_load_dwordx4 v26, s[44:47], s56 offen offset:0 lds\n"
  "s_add_u32 s57, s59, 0x4480\n"
  "buffer_store_dwordx4 v[216:219], v17, s[36:39], s57 offen sc1\n"
  "ds_read_b64_tr_b16 a[34:35], v4 offset:1024\n"
  "ds_read_b128 a[52:55], v0 offset:4096\n"
  "v_add_u32 v5, 0x1c000, v5\n"
  "v_mfma_f32_32x32x16_bf16 a[144:159], a[60:63], a[72:75], a[144:159]\n"
  "ds_read_b64_tr_b16 a[36:37], v5 offset:0\n"
  "v_add_u32 v6, 0x1c000, v6\n"
  "v_accvgpr_write_b32 a108, 0\n"
  "v_mfma_f32_32x32x16_bf16 a[208:223], a[60:63], a[76:79], a[208:223]\n"
  "buffer_load_dwordx4 v27, s[44:47], s56 offen offset:1024 lds\n"
  "ds_read_b64_tr_b16 a[38:39], v5 offset:1024\n"
  "ds_read_b64_tr_b16 a[40:41], v6 offset:0\n"
  "v_add_u32 v7, 0x1c000, v7\n"
  "v_accvgpr_write_b32 a107, 0\n"
  "v_mfma_f32_32x32x16_bf16 a[160:175], a[64:67], a[72:75], a[160:175]\n"
  "ds_read_b64_tr_b16 a[42:43], v6 offset:1024\n"
  "ds_read_b64_tr_b16 a[44:45], v7 offset:0\n"
  "v_accvgpr_write_b32 a106, 0\n"
  "v_mfma_f32_32x32x16_bf16 a[224:239], a[64:67], a[76:79], a[224:239]\n"
  "buffer_load_dwordx4 v28, s[44:47], s56 offen offset:2048 lds\n"
  "ds_read_b64_tr_b16 a[46:47], v7 offset:1024\n"
  "v_add_u32 v1, 0x1c000, v1\n"
  "v_accvgpr_write_b32 a103, 0\n"
  "v_mfma_f32_32x32x16_bf16 a[176:191], a[68:71], a[72:75], a[176:191]\n"
  "v_add_u32 v2, 0x1c000, v2\n"
  "v_accvgpr_write_b32 a102, 0\n"
  "v_mfma_f32_32x32x16_bf16 a[240:255], a[68:71], a[76:79], a[240:255]\n"
  "buffer_load_dwordx4 v29, s[44:47], s56 offen offset:3072 lds\n"
  "s_waitcnt lgkmcnt(4)\n"
  "v_add_u32 v3, 0x1c000, v3\n"
  "v_accvgpr_write_b32 a101, 0\n"
  "v_mfma_f32_32x32x16_bf16 a[128:143], a[32:35], a[48:51], a[128:143]\n"
  "ds_read_b128 a[72:75], v1 offset:0\n"
  "ds_read_b64_tr_b16 a[56:57], v4 offset:4096\n"
  "v_cvt_pk_bf16_f32 v224, v224, v225\n"
  "v_cvt_pk_bf16_f32 v225, v226, v227\n"
  "v_cvt_pk_bf16_f32 v226, v228, v229\n"
  "v_cvt_pk_bf16_f32 v227, v230, v231\n"
  "v_mfma_f32_32x32x16_bf16 a[192:207], a[32:35], a[52:55], a[192:207]\n"
  "s_add_u32 m0, s52, 0x0\n"
  "s_nop 0\n"
  "buffer_load_dwordx4 v18, s[40:43], s55 offen offset:0 lds\n"
  "s_add_u32 s57, s58, 0x6000\n"
  "buffer_store_dwordx4 v[224:227], v31, s[36:39], s57 offen sc1\n"
  "ds_read_b64_tr_b16 a[58:59], v4 offset:5120\n"
  "ds_read_b128 a[76:79], v1 offset:4096\n"
  "v_mfma_f32_32x32x16_bf16 a[144:159], a[36:39], a[48:51], a[144:159]\n"
  "ds_read_b64_tr_b16 a[60:61], v5 offset:4096\n"
  "v_accvgpr_write_b32 a100, 0\n"
  "v_mfma_f32_32x32x16_bf16 a[208:223], a[36:39], a[52:55], a[208:223]\n"
  "s_waitcnt lgkmcnt(5)\n"
  "buffer_load_dwordx4 v19, s[40:43], s55 offen offset:1024 lds\n"
  "ds_read_b64_tr_b16 a[62:63], v5 offset:5120\n"
  "ds_read_b64_tr_b16 a[64:65], v6 offset:4096\n"
  "v_accvgpr_write_b32 a99, 0\n"
  "v_mfma_f32_32x32x16_bf16 a[160:175], a[40:43], a[48:51], a[160:175]\n"
  "ds_read_b64_tr_b16 a[66:67], v6 offset:5120\n"
  "ds_read_b64_tr_b16 a[68:69], v7 offset:4096\n"
  "v_accvgpr_write_b32 a98, 0\n"
  "v_mfma_f32_32x32x16_bf16 a[224:239], a[40:43], a[52:55], a[224:239]\n"
  "buffer_load_dwordx4 v20, s[40:43], s55 offen offset:2048 lds\n"
  "ds_read_b64_tr_b16 a[70:71], v7 offset:5120\n"
  "v_accvgpr_write_b32 a97, 0\n"
  "v_mfma_f32_32x32x16_bf16 a[176:191], a[44:47], a[48:51], a[176:191]\n"
  "v_accvgpr_write_b32 a96, 0\n"
  "v_mfma_f32_32x32x16_bf16 a[240:255], a[44:47], a[52:55], a[240:255]\n"
  "buffer_load_dwordx4 v21, s[40:43], s55 offen offset:3072 lds\n"
  "s_waitcnt lgkmcnt(4)\n"
  "v_accvgpr_write_b32 a95, 0\n"
  "v_mfma_f32_32x32x16_bf16 a[128:143], a[56:59], a[72:75], a[128:143]\n"
  "ds_read_b128 a[48:51], v2 offset:0\n"
  "ds_read_b64_tr_b16 a[32:33], v4 offset:8192\n"
  "v_cvt_pk_bf16_f32 v232, v232, v233\n"
  "v_cvt_pk_bf16_f32 v233, v234, v235\n"
  "v_cvt_pk_bf16_f32 v234, v236, v237\n"
  "v_cvt_pk_bf16_f32 v235, v238, v239\n"
  "v_mfma_f32_32x32x16_bf16 a[192:207], a[56:59], a[76:79], a[192:207]\n"
  "s_add_u32 m0, s52, 0x1000\n"
  "s_nop 0\n"
  "buffer_load_dwordx4 v22, s[40:43], s55 offen offset:0 lds\n"
  "s_add_u32 s57, s58, 0x6080\n"
  "buffer_store_dwordx4 v[232:235], v31, s[36:39], s57 offen sc1\n"
  "ds_read_b64_tr_b16 a[34:35], v4 offset:9216\n"
  "ds_read_b128 a[52:55], v2 offset:4096\n"
  "v_mfma_f32_32x32x16_bf16 a[144:159], a[60:63], a[72:75], a[144:159]\n"
  "ds_read_b64_tr_b16 a[36:37], v5 offset:8192\n"
  "v_accvgpr_write_b32 a94, 0\n"
  "v_mfma_f32_32x32x16_bf16 a[208:223], a[60:63], a[76:79], a[208:223]\n"
  "s_waitcnt lgkmcnt(5)\n"
  "buffer_load_dwordx4 v23, s[40:43], s55 offen offset:1024 lds\n"
  "ds_read_b64_tr_b16 a[38:39], v5 offset:9216\n"
  "ds_read_b64_tr_b16 a[40:41], v6 offset:8192\n"
  "v_accvgpr_write_b32 a93, 0\n"
  "v_mfma_f32_32x32x16_bf16 a[160:175], a[64:67], a[72:75], a[160:175]\n"
  "ds_read_b64_tr_b16 a[42:43], v6 offset:9216\n"
  "ds_read_b64_tr_b16 a[44:45], v7 offset:8192\n"
  "v_accvgpr_write_b32 a92, 0\n"
  "v_mfma_f32_32x32x16_bf16 a[224:239], a[64:67], a[76:79], a[224:239]\n"
  "buffer_load_dwordx4 v24, s[40:43], s55 offen offset:2048 lds\n"
  "ds_read_b64_tr_b16 a[46:47], v7 offset:9216\n"
  "v_accvgpr_write_b32 a91, 0\n"
  "v_mfma_f32_32x32x16_bf16 a[176:191], a[68:71], a[72:75], a[176:191]\n"
  "v_accvgpr_write_b32 a90, 0\n"
  "v_mfma_f32_32x32x16_bf16 a[240:255], a[68:71], a[76:79], a[240:255]\n"
  "buffer_load_dwordx4 v25, s[40:43], s55 offen offset:3072 lds\n"
  "s_waitcnt lgkmcnt(4)\n"
  "v_accvgpr_write_b32 a89, 0\n"
  "v_mfma_f32_32x32x16_bf16 a[128:143], a[32:35], a[48:51], a[128:143]\n"
  "ds_read_b128 a[72:75], v3 offset:0\n"
  "ds_read_b64_tr_b16 a[56:57], v4 offset:12288\n"
  "v_cvt_pk_bf16_f32 v240, v240, v241\n"
  "v_cvt_pk_bf16_f32 v241, v242, v243\n"
  "v_cvt_pk_bf16_f32 v242, v244, v245\n"
  "v_cvt_pk_bf16_f32 v243, v246, v247\n"
  "v_mfma_f32_32x32x16_bf16 a[192:207], a[32:35], a[52:55], a[192:207]\n"
  "s_add_u32 s57, s58, 0x6800\n"
  "buffer_store_dwordx4 v[240:243], v31, s[36:39], s57 offen sc1\n"
  "ds_read_b64_tr_b16 a[58:59], v4 offset:13312\n"
  "ds_read_b128 a[76:79], v3 offset:4096\n"
  "v_mfma_f32_32x32x16_bf16 a[144:159], a[36:39], a[48:51], a[144:159]\n"
  "ds_read_b64_tr_b16 a[60:61], v5 offset:12288\n"
  "v_accvgpr_write_b32 a88, 0\n"
  "v_mfma_f32_32x32x16_bf16 a[208:223], a[36:39], a[52:55], a[208:223]\n"
  "s_waitcnt lgkmcnt(5)\n"
  "ds_read_b64_tr_b16 a[62:63], v5 offset:13312\n"
  "ds_read_b64_tr_b16 a[64:65], v6 offset:12288\n"
  "v_accvgpr_write_b32 a87, 0\n"
  "v_mfma_f32_32x32x16_bf16 a[160:175], a[40:43], a[48:51], a[160:175]\n"
  "ds_read_b64_tr_b16 a[66:67], v6 offset:13312\n"
  "ds_read_b64_tr_b16 a[68:69], v7 offset:12288\n"
  "v_accvgpr_write_b32 a86, 0\n"
  "v_mfma_f32_32x32x16_bf16 a[224:239], a[40:43], a[52:55], a[224:239]\n"
  "ds_read_b64_tr_b16 a[70:71], v7 offset:13312\n"
  "v_accvgpr_write_b32 a85, 0\n"
  "v_mfma_f32_32x32x16_bf16 a[176:191], a[44:47], a[48:51], a[176:191]\n"
  "v_accvgpr_write_b32 a84, 0\n"
  "v_mfma_f32_32x32x16_bf16 a[240:255], a[44:47], a[52:55], a[240:255]\n"
  "s_waitcnt lgkmcnt(4)\n"
  "v_accvgpr_write_b32 a83, 0\n"
  "v_mfma_f32_32x32x16_bf16 a[128:143], a[56:59], a[72:75], a[128:143]\n"
  "s_waitcnt vmcnt(17) lgkmcnt(0)\n"
  "s_barrier\n"
  "v_add_u32 v0, 0xffff4000, v0\n"
  "ds_read_b128 a[48:51], v0 offset:0\n"
  "v_add_u32 v4, 0xffff4000, v4\n"
  "ds_read_b64_tr_b16 a[32:33], v4 offset:0\n"
  "v_cvt_pk_bf16_f32 v248, v248, v249\n"
  "v_cvt_pk_bf16_f32 v249, v250, v251\n"
  "v_cvt_pk_bf16_f32 v250, v252, v253\n"
  "v_cvt_pk_bf16_f32 v251, v254, v255\n"
  "v_mfma_f32_32x32x16_bf16 a[192:207], a[56:59], a[76:79], a[192:207]\n"
  "s_mov_b32 s55, 0x480\n"
  "s_mov_b32 s56, 0x480000\n"
  "s_add_u32 m0, s53, 0x24000\n"
  "s_nop 0\n"
  "buffer_load_dwordx4 v26, s[44:47], s56 offen offset:0 lds\n"
  "s_add_u32 s57, s58, 0x6880\n"
  "buffer_store_dwordx4 v[248:251], v31, s[36:39], s57 offen sc1\n"
  "ds_read_b64_tr_b16 a[34:35], v4 offset:1024\n"
  "ds_read_b128 a[52:55], v0 offset:4096\n"
  "v_add_u32 v5, 0xffff4000, v5\n"
  "v_mfma_f32_32x32x16_bf16 a[144:159], a[60:63], a[72:75], a[144:159]\n"
  "ds_read_b64_tr_b16 a[36:37], v5 offset:0\n"
  "v_add_u32 v6, 0xffff4000, v6\n"
  "v_accvgpr_write_b32 a82, 0\n"
  "v_mfma_f32_32x32x16_bf16 a[208:223], a[60:63], a[76:79], a[208:223]\n"
  "buffer_load_dwordx4 v27, s[44:47], s56 offen offset:1024 lds\n"
  "ds_read_b64_tr_b16 a[38:39], v5 offset:1024\n"
  "ds_read_b64_tr_b16 a[40:41], v6 offset:0\n"
  "v_add_u32 v7, 0xffff4000, v7\n"
  "v_accvgpr_write_b32 a81, 0\n"
  "v_mfma_f32_32x32x16_bf16 a[160:175], a[64:67], a[72:75], a[160:175]\n"
  "ds_read_b64_tr_b16 a[42:43], v6 offset:1024\n"
  "ds_read_b64_tr_b16 a[44:45], v7 offset:0\n"
  "v_accvgpr_write_b32 a80, 0\n"
  "v_mfma_f32_32x32x16_bf16 a[224:239], a[64:67], a[76:79], a[224:239]\n"
  "buffer_load_dwordx4 v28, s[44:47], s56 offen offset:2048 lds\n"
  "ds_read_b64_tr_b16 a[46:47], v7 offset:1024\n"
  "v_add_u32 v1, 0xffff4000, v1\n"
  "v_accvgpr_write_b32 a31, 0\n"
  "v_mfma_f32_32x32x16_bf16 a[176:191], a[68:71], a[72:75], a[176:191]\n"
  "v_add_u32 v2, 0xffff4000, v2\n"
  "v_accvgpr_write_b32 a30, 0\n"
  "v_mfma_f32_32x32x16_bf16 a[240:255], a[68:71], a[76:79], a[240:255]\n"
  "buffer_load_dwordx4 v29, s[44:47], s56 offen offset:3072 lds\n"
  "s_waitcnt lgkmcnt(4)\n"
  "v_add_u32 v3, 0xffff4000, v3\n"
  "v_accvgpr_write_b32 a29, 0\n"
  "v_mfma_f32_32x32x16_bf16 a[128:143], a[32:35], a[48:51], a[128:143]\n"
  "ds_read_b128 a[72:75], v1 offset:0\n"
  "ds_read_b64_tr_b16 a[56:57], v4 offset:4096\n"
  "v_mfma_f32_32x32x16_bf16 a[192:207], a[32:35], a[52:55], a[192:207]\n"
  "s_add_u32 m0, s52, 0x1c000\n"
  "s_nop 0\n"
  "buffer_load_dwordx4 v18, s[40:43], s55 offen offset:0 lds\n"
  "ds_read_b64_tr_b16 a[58:59], v4 offset:5120\n"
  "ds_read_b128 a[76:79], v1 offset:4096\n"
  "v_mfma_f32_32x32x16_bf16 a[144:159], a[36:39], a[48:51], a[144:159]\n"
  "ds_read_b64_tr_b16 a[60:61], v5 offset:4096\n"
  "v_accvgpr_write_b32 a28, 0\n"
  "v_mfma_f32_32x32x16_bf16 a[208:223], a[36:39], a[52:55], a[208:223]\n"
  "s_waitcnt lgkmcnt(5)\n"
  "buffer_load_dwordx4 v19, s[40:43], s55 offen offset:1024 lds\n"
  "ds_read_b64_tr_b16 a[62:63], v5 offset:5120\n"
  "ds_read_b64_tr_b16 a[64:65], v6 offset:4096\n"
  "v_accvgpr_write_b32 a27, 0\n"
  "v_mfma_f32_32x32x16_bf16 a[160:175], a[40:43], a[48:51], a[160:175]\n"
  "ds_read_b64_tr_b16 a[66:67], v6 offset:5120\n"
  "ds_read_b64_tr_b16 a[68:69], v7 offset:4096\n"
  "v_accvgpr_write_b32 a26, 0\n"
  "v_mfma_f32_32x32x16_bf16 a[224:239], a[40:43], a[52:55], a[224:239]\n"
  "buffer_load_dwordx4 v20, s[40:43], s55 offen offset:2048 lds\n"
  "ds_read_b64_tr_b16 a[70:71], v7 offset:5120\n"
  "v_accvgpr_write_b32 a25, 0\n"
  "v_mfma_f32_32x32x16_bf16 a[176:191], a[44:47], a[48:51], a[176:191]\n"
  "v_accvgpr_write_b32 a24, 0\n"
  "v_mfma_f32_32x32x16_bf16 a[240:255], a[44:47], a[52:55], a[240:255]\n"
  "buffer_load_dwordx4 v21, s[40:43], s55 offen offset:3072 lds\n"
  "s_waitcnt lgkmcnt(4)\n"
  "v_accvgpr_write_b32 a23, 0\n"
  "v_mfma_f32_32x32x16_bf16 a[128:143], a[56:59], a[72:75], a[128:143]\n"
  "ds_read_b128 a[48:51], v2 offset:0\n"
  "ds_read_b64_tr_b16 a[32:33], v4 offset:8192\n"
  "v_mfma_f32_32x32x16_bf16 a[192:207], a[56:59], a[76:79], a[192:207]\n"
  "s_add_u32 m0, s52, 0x1d000\n"
  "s_nop 0\n"
  "buffer_load_dwordx4 v22, s[40:43], s55 offen offset:0 lds\n"
  "ds_read_b64_tr_b16 a[34:35], v4 offset:9216\n"
  "ds_read_b128 a[52:55], v2 offset:4096\n"
  "v_mfma_f32_32x32x16_bf16 a[144:159], a[60:63], a[72:75], a[144:159]\n"
  "ds_read_b64_tr_b16 a[36:37], v5 offset:8192\n"
  "v_accvgpr_write_b32 a22, 0\n"
  "v_mfma_f32_32x32x16_bf16 a[208:223], a[60:63], a[76:79], a[208:223]\n"
  "s_waitcnt lgkmcnt(5)\n"
  "buffer_load_dwordx4 v23, s[40:43], s55 offen offset:1024 lds\n"
  "ds_read_b64_tr_b16 a[38:39], v5 offset:9216\n"
  "ds_read_b64_tr_b16 a[40:41], v6 offset:8192\n"
  "v_accvgpr_write_b32 a21, 0\n"
  "v_mfma_f32_32x32x16_bf16 a[160:175], a[64:67], a[72:75], a[160:175]\n"
  "ds_read_b64_tr_b16 a[42:43], v6 offset:9216\n"
  "ds_read_b64_tr_b16 a[44:45], v7 offset:8192\n"
  "v_accvgpr_write_b32 a20, 0\n"
  "v_mfma_f32_32x32x16_bf16 a[224:239], a[64:67], a[76:79], a[224:239]\n"
  "buffer_load_dwordx4 v24, s[40:43], s55 offen offset:2048 lds\n"
  "ds_read_b64_tr_b16 a[46:47], v7 offset:9216\n"
  "v_accvgpr_write_b32 a19, 0\n"
  "v_mfma_f32_32x32x16_bf16 a[176:191], a[68:71], a[72:75], a[176:191]\n"
  "v_accvgpr_write_b32 a18, 0\n"
  "v_mfma_f32_32x32x16_bf16 a[240:255], a[68:71], a[76:79], a[240:255]\n"
  "buffer_load_dwordx4 v25, s[40:43], s55 offen offset:3072 lds\n"
  "s_waitcnt lgkmcnt(4)\n"
  "v_accvgpr_write_b32 a17, 0\n"
  "v_mfma_f32_32x32x16_bf16 a[128:143], a[32:35], a[48:51], a[128:143]\n"
  "ds_read_b128 a[72:75], v3 offset:0\n"
  "ds_read_b64_tr_b16 a[56:57], v4 offset:12288\n"
  "v_mfma_f32_32x32x16_bf16 a[192:207], a[32:35], a[52:55], a[192:207]\n"
  "ds_read_b64_tr_b16 a[58:59], v4 offset:13312\n"
  "ds_read_b128 a[76:79], v3 offset:4096\n"
  "v_mfma_f32_32x32x16_bf16 a[144:159], a[36:39], a[48:51], a[144:159]\n"
  "ds_read_b64_tr_b16 a[60:61], v5 offset:12288\n"
  "v_accvgpr_write_b32 a16, 0\n"
  "v_mfma_f32_32x32x16_bf16 a[208:223], a[36:39], a[52:55], a[208:223]\n"
  "s_waitcnt lgkmcnt(5)\n"
  "ds_read_b64_tr_b16 a[62:63], v5 offset:13312\n"
  "ds_read_b64_tr_b16 a[64:65], v6 offset:12288\n"
  "v_accvgpr_write_b32 a15, 0\n"
  "v_mfma_f32_32x32x16_bf16 a[160:175], a[40:43], a[48:51], a[160:175]\n"
  "ds_read_b64_tr_b16 a[66:67], v6 offset:13312\n"
  "ds_read_b64_tr_b16 a[68:69], v7 offset:12288\n"
  "v_accvgpr_write_b32 a14, 0\n"
  "v_mfma_f32_32x32x16_bf16 a[224:239], a[40:43], a[52:55], a[224:239]\n"
  "ds_read_b64_tr_b16 a[70:71], v7 offset:13312\n"
  "v_accvgpr_write_b32 a13, 0\n"
  "v_mfma_f32_32x32x16_bf16 a[176:191], a[44:47], a[48:51], a[176:191]\n"
  "v_accvgpr_write_b32 a12, 0\n"
  "v_mfma_f32_32x32x16_bf16 a[240:255], a[44:47], a[52:55], a[240:255]\n"
  "s_waitcnt lgkmcnt(4)\n"
  "v_accvgpr_write_b32 a11, 0\n"
  "v_mfma_f32_32x32x16_bf16 a[128:143], a[56:59], a[72:75], a[128:143]\n"
  "v_add_u32 v0, 0xffff0000, v0\n"
  "v_add_u32 v4, 0xffff0000, v4\n"
  "s_waitcnt vmcnt(14) lgkmcnt(0)\n"
  "s_waitcnt vmcnt(11)\n"
  "s_barrier\n"
  "ds_read_b128 a[48:51], v0 offset:0\n"
  "ds_read_b64_tr_b16 a[32:33], v4 offset:0\n"
  "v_mov_b32 v16, s70\n"
  "v_mov_b32 v17, s71\n"
  "s_mov_b64 exec, 1\n"
  "buffer_store_dwordx2 v[16:17], off, s[48:51], s72 sc1\n"
  "buffer_store_dwordx2 v[16:17], off, s[48:51], s22 sc1\n"
  "s_mov_b64 exec, -1\n"
  "v_mfma_f32_32x32x16_bf16 a[192:207], a[56:59], a[76:79], a[192:207]\n"
  "ds_read_b64_tr_b16 a[34:35], v4 offset:1024\n"
  "ds_read_b128 a[52:55], v0 offset:4096\n"
  "v_add_u32 v5, 0xffff0000, v5\n"
  "v_mfma_f32_32x32x16_bf16 a[144:159], a[60:63], a[72:75], a[144:159]\n"
  "ds_read_b64_tr_b16 a[36:37], v5 offset:0\n"
  "v_add_u32 v6, 0xffff0000, v6\n"
  "v_accvgpr_write_b32 a10, 0\n"
  "v_mfma_f32_32x32x16_bf16 a[208:223], a[60:63], a[76:79], a[208:223]\n"
  "ds_read_b64_tr_b16 a[38:39], v5 offset:1024\n"
  "ds_read_b64_tr_b16 a[40:41], v6 offset:0\n"
  "v_add_u32 v7, 0xffff0000, v7\n"
  "v_accvgpr_write_b32 a9, 0\n"
  "v_mfma_f32_32x32x16_bf16 a[160:175], a[64:67], a[72:75], a[160:175]\n"
  "ds_read_b64_tr_b16 a[42:43], v6 offset:1024\n"
  "ds_read_b64_tr_b16 a[44:45], v7 offset:0\n"
  "v_accvgpr_write_b32 a8, 0\n"
  "v_mfma_f32_32x32x16_bf16 a[224:239], a[64:67], a[76:79], a[224:239]\n"
  "ds_read_b64_tr_b16 a[46:47], v7 offset:1024\n"
  "v_add_u32 v1, 0xffff0000, v1\n"
  "v_accvgpr_write_b32 a7, 0\n"
  "v_mfma_f32_32x32x16_bf16 a[176:191], a[68:71], a[72:75], a[176:191]\n"
  "v_add_u32 v2, 0xffff0000, v2\n"
  "v_accvgpr_write_b32 a6, 0\n"
  "v_mfma_f32_32x32x16_bf16 a[240:255], a[68:71], a[76:79], a[240:255]\n"
  "s_waitcnt lgkmcnt(4)\n"
  "v_add_u32 v3, 0xffff0000, v3\n"
  "v_accvgpr_write_b32 a5, 0\n"
  "v_mfma_f32_32x32x16_bf16 a[128:143], a[32:35], a[48:51], a[128:143]\n"
  "ds_read_b128 a[72:75], v1 offset:0\n"
  "ds_read_b64_tr_b16 a[56:57], v4 offset:4096\n"
  "v_mfma_f32_32x32x16_bf16 a[192:207], a[32:35], a[52:55], a[192:207]\n"
  "s_add_u32 s24, s36, s74\n"
  "s_addc_u32 s25, s37, 0\n"
  "s_sub_u32 s26, s64, 0x10000\n"
  "s_add_u32 s28, s24, 0xffc000\n"
  "s_addc_u32 s29, s25, 0\n"
  "s_sub_u32 s30, s64, 0xc000\n"
  "ds_read_b64_tr_b16 a[58:59], v4 offset:5120\n"
  "ds_read_b128 a[76:79], v1 offset:4096\n"
  "v_lshrrev_b32 v31, 1, v30\n"
  "v_min_u32 v31, 56, v31\n"
  "v_mfma_f32_32x32x16_bf16 a[144:159], a[36:39], a[48:51], a[144:159]\n"
  "ds_read_b64_tr_b16 a[60:61], v5 offset:4096\n"
  "v_accvgpr_write_b32 a4, 0\n"
  "v_mfma_f32_32x32x16_bf16 a[208:223], a[36:39], a[52:55], a[208:223]\n"
  "s_waitcnt lgkmcnt(5)\n"
  "ds_read_b64_tr_b16 a[62:63], v5 offset:5120\n"
  "ds_read_b64_tr_b16 a[64:65], v6 offset:4096\n"
  "v_accvgpr_write_b32 a3, 0\n"
  "v_mfma_f32_32x32x16_bf16 a[160:175], a[40:43], a[48:51], a[160:175]\n"
  "ds_read_b64_tr_b16 a[66:67], v6 offset:5120\n"
  "ds_read_b64_tr_b16 a[68:69], v7 offset:4096\n"
  "v_accvgpr_write_b32 a2, 0\n"
  "v_mfma_f32_32x32x16_bf16 a[224:239], a[40:43], a[52:55], a[224:239]\n"
  "ds_read_b64_tr_b16 a[70:71], v7 offset:5120\n"
  "v_accvgpr_write_b32 a1, 0\n"
  "v_mfma_f32_32x32x16_bf16 a[176:191], a[44:47], a[48:51], a[176:191]\n"
  "v_accvgpr_write_b32 a0, 0\n"
  "v_mfma_f32_32x32x16_bf16 a[240:255], a[44:47], a[52:55], a[240:255]\n"
  "s_waitcnt lgkmcnt(4)\n"
  "v_mfma_f32_32x32x16_bf16 a[128:143], a[56:59], a[72:75], a[128:143]\n"
  "ds_read_b128 a[48:51], v2 offset:0\n"
  "ds_read_b64_tr_b16 a[32:33], v4 offset:8192\n"
  "v_mfma_f32_32x32x16_bf16 a[192:207], a[56:59], a[76:79], a[192:207]\n"
  "ds_read_b64_tr_b16 a[34:35], v4 offset:9216\n"
  "ds_read_b128 a[52:55], v2 offset:4096\n"
  "v_mfma_f32_32x32x16_bf16 a[144:159], a[60:63], a[72:75], a[144:159]\n"
  "ds_read_b64_tr_b16 a[36:37], v5 offset:8192\n"
  "v_mfma_f32_32x32x16_bf16 a[208:223], a[60:63], a[76:79], a[208:223]\n"
  "s_waitcnt lgkmcnt(5)\n"
  "ds_read_b64_tr_b16 a[38:39], v5 offset:9216\n"
  "ds_read_b64_tr_b16 a[40:41], v6 offset:8192\n"
  "v_mfma_f32_32x32x16_bf16 a[160:175], a[64:67], a[72:75], a[160:175]\n"
  "ds_read_b64_tr_b16 a[42:43], v6 offset:9216\n"
  "ds_read_b64_tr_b16 a[44:45], v7 offset:8192\n"
  "v_mfma_f32_32x32x16_bf16 a[224:239], a[64:67], a[76:79], a[224:239]\n"
  "ds_read_b64_tr_b16 a[46:47], v7 offset:9216\n"
  "v_mfma_f32_32x32x16_bf16 a[176:191], a[68:71], a[72:75], a[176:191]\n"
  "v_mfma_f32_32x32x16_bf16 a[240:255], a[68:71], a[76:79], a[240:255]\n"
  "s_waitcnt lgkmcnt(4)\n"
  "v_mfma_f32_32x32x16_bf16 a[128:143], a[32:35], a[48:51], a[128:143]\n"
  "ds_read_b128 a[72:75], v3 offset:0\n"
  "ds_read_b64_tr_b16 a[56:57], v4 offset:12288\n"
  "v_mfma_f32_32x32x16_bf16 a[192:207], a[32:35], a[52:55], a[192:207]\n"
  "ds_read_b64_tr_b16 a[58:59], v4 offset:13312\n"
  "ds_read_b128 a[76:79], v3 offset:4096\n"
  "v_mfma_f32_32x32x16_bf16 a[144:159], a[36:39], a[48:51], a[144:159]\n"
  "ds_read_b64_tr_b16 a[60:61], v5 offset:12288\n"
  "v_mfma_f32_32x32x16_bf16 a[208:223], a[36:39], a[52:55], a[208:223]\n"
  "s_waitcnt lgkmcnt(5)\n"
  "ds_read_b64_tr_b16 a[62:63], v5 offset:13312\n"
  "ds_read_b64_tr_b16 a[64:65], v6 offset:12288\n"
  "v_mfma_f32_32x32x16_bf16 a[160:175], a[40:43], a[48:51], a[160:175]\n"
  "ds_read_b64_tr_b16 a[66:67], v6 offset:13312\n"
  "ds_read_b64_tr_b16 a[68:69], v7 offset:12288\n"
  "v_mfma_f32_32x32x16_bf16 a[224:239], a[40:43], a[52:55], a[224:239]\n"
  "ds_read_b64_tr_b16 a[70:71], v7 offset:13312\n"
  "v_mfma_f32_32x32x16_bf16 a[176:191], a[44:47], a[48:51], a[176:191]\n"
  "v_mfma_f32_32x32x16_bf16 a[240:255], a[44:47], a[52:55], a[240:255]\n"
  "s_waitcnt lgkmcnt(4)\n"
  "v_mfma_f32_32x32x16_bf16 a[128:143], a[56:59], a[72:75], a[128:143]\n"
  "s_waitcnt vmcnt(2) lgkmcnt(0)\n"
  "s_barrier\n"
  "v_add_u32 v0, 0x1c000, v0\n"
  "ds_read_b128 a[48:51], v0 offset:0\n"
  "v_add_u32 v4, 0x1c000, v4\n"
  "ds_read_b64_tr_b16 a[32:33], v4 offset:0\n"
  "v_mfma_f32_32x32x16_bf16 a[192:207], a[56:59], a[76:79], a[192:207]\n"
  "ds_read_b64_tr_b16 a[34:35], v4 offset:1024\n"
  "ds_read_b128 a[52:55], v0 offset:4096\n"
  "v_add_u32 v5, 0x1c000, v5\n"
  "v_mfma_f32_32x32x16_bf16 a[144:159], a[60:63], a[72:75], a[144:159]\n"
  "ds_read_b64_tr_b16 a[36:37], v5 offset:0\n"
  "v_add_u32 v6, 0x1c000, v6\n"
  "v_mfma_f32_32x32x16_bf16 a[208:223], a[60:63], a[76:79], a[208:223]\n"
  "ds_read_b64_tr_b16 a[38:39], v5 offset:1024\n"
  "ds_read_b64_tr_b16 a[40:41], v6 offset:0\n"
  "v_add_u32 v7, 0x1c000, v7\n"
  "v_mfma_f32_32x32x16_bf16 a[160:175], a[64:67], a[72:75], a[160:175]\n"
  "ds_read_b64_tr_b16 a[42:43], v6 offset:1024\n"
  "ds_read_b64_tr_b16 a[44:45], v7 offset:0\n"
  "v_mfma_f32_32x32x16_bf16 a[224:239], a[64:67], a[76:79], a[224:239]\n"
  "ds_read_b64_tr_b16 a[46:47], v7 offset:1024\n"
  "v_add_u32 v1, 0x1c000, v1\n"
  "v_mfma_f32_32x32x16_bf16 a[176:191], a[68:71], a[72:75], a[176:191]\n"
  "v_add_u32 v2, 0x1c000, v2\n"
  "v_mfma_f32_32x32x16_bf16 a[240:255], a[68:71], a[76:79], a[240:255]\n"
  "s_waitcnt lgkmcnt(4)\n"
  "v_add_u32 v3, 0x1c000, v3\n"
  "v_mfma_f32_32x32x16_bf16 a[128:143], a[32:35], a[48:51], a[128:143]\n"
  "ds_read_b128 a[72:75], v1 offset:0\n"
  "ds_read_b64_tr_b16 a[56:57], v4 offset:4096\n"
  "v_mfma_f32_32x32x16_bf16 a[192:207], a[32:35], a[52:55], a[192:207]\n"
  "ds_read_b64_tr_b16 a[58:59], v4 offset:5120\n"
  "ds_read_b128 a[76:79], v1 offset:4096\n"
  "v_mfma_f32_32x32x16_bf16 a[144:159], a[36:39], a[48:51], a[144:159]\n"
  "ds_read_b64_tr_b16 a[60:61], v5 offset:4096\n"
  "v_mfma_f32_32x32x16_bf16 a[208:223], a[36:39], a[52:55], a[208:223]\n"
  "s_waitcnt lgkmcnt(5)\n"
  "ds_read_b64_tr_b16 a[62:63], v5 offset:5120\n"
  "ds_read_b64_tr_b16 a[64:65], v6 offset:4096\n"
  "v_mfma_f32_32x32x16_bf16 a[160:175], a[40:43], a[48:51], a[160:175]\n"
  "ds_read_b64_tr_b16 a[66:67], v6 offset:5120\n"
  "ds_read_b64_tr_b16 a[68:69], v7 offset:4096\n"
  "v_mfma_f32_32x32x16_bf16 a[224:239], a[40:43], a[52:55], a[224:239]\n"
  "ds_read_b64_tr_b16 a[70:71], v7 offset:5120\n"
  "v_mfma_f32_32x32x16_bf16 a[176:191], a[44:47], a[48:51], a[176:191]\n"
  "v_mfma_f32_32x32x16_bf16 a[240:255], a[44:47], a[52:55], a[240:255]\n"
  "buffer_load_dwordx2 v[16:17], v31, s[48:51], s73 offen sc1\n"
  "s_waitcnt lgkmcnt(4)\n"
  "v_mov_b32 v20, v30\n"
  "v_mfma_f32_32x32x16_bf16 a[128:143], a[56:59], a[72:75], a[128:143]\n"
  "ds_read_b128 a[48:51], v2 offset:0\n"
  "ds_read_b64_tr_b16 a[32:33], v4 offset:8192\n"
  "v_mfma_f32_32x32x16_bf16 a[192:207], a[56:59], a[76:79], a[192:207]\n"
  "ds_read_b64_tr_b16 a[34:35], v4 offset:9216\n"
  "ds_read_b128 a[52:55], v2 offset:4096\n"
  "v_mfma_f32_32x32x16_bf16 a[144:159], a[60:63], a[72:75], a[144:159]\n"
  "ds_read_b64_tr_b16 a[36:37], v5 offset:8192\n"
  "v_mfma_f32_32x32x16_bf16 a[208:223], a[60:63], a[76:79], a[208:223]\n"
  "s_waitcnt lgkmcnt(5)\n"
  "ds_read_b64_tr_b16 a[38:39], v5 offset:9216\n"
  "ds_read_b64_tr_b16 a[40:41], v6 offset:8192\n"
  "v_mfma_f32_32x32x16_bf16 a[160:175], a[64:67], a[72:75], a[160:175]\n"
  "ds_read_b64_tr_b16 a[42:43], v6 offset:9216\n"
  "ds_read_b64_tr_b16 a[44:45], v7 offset:8192\n"
  "v_mfma_f32_32x32x16_bf16 a[224:239], a[64:67], a[76:79], a[224:239]\n"
  "ds_read_b64_tr_b16 a[46:47], v7 offset:9216\n"
  "v_mfma_f32_32x32x16_bf16 a[176:191], a[68:71], a[72:75], a[176:191]\n"
  "v_mfma_f32_32x32x16_bf16 a[240:255], a[68:71], a[76:79], a[240:255]\n"
  "s_waitcnt lgkmcnt(4)\n"
  "v_mfma_f32_32x32x16_bf16 a[128:143], a[32:35], a[48:51], a[128:143]\n"
  "ds_read_b128 a[72:75], v3 offset:0\n"
  "ds_read_b64_tr_b16 a[56:57], v4 offset:12288\n"
  "v_mfma_f32_32x32x16_bf16 a[192:207], a[32:35], a[52:55], a[192:207]\n"
  "ds_read_b64_tr_b16 a[58:59], v4 offset:13312\n"
  "ds_read_b128 a[76:79], v3 offset:4096\n"
  "v_mfma_f32_32x32x16_bf16 a[144:159], a[36:39], a[48:51], a[144:159]\n"
  "ds_read_b64_tr_b16 a[60:61], v5 offset:12288\n"
  "v_mfma_f32_32x32x16_bf16 a[208:223], a[36:39], a[52:55], a[208:223]\n"
  "s_waitcnt lgkmcnt(5)\n"
  "ds_read_b64_tr_b16 a[62:63], v5 offset:13312\n"
  "ds_read_b64_tr_b16 a[64:65], v6 offset:12288\n"
  "v_mfma_f32_32x32x16_bf16 a[160:175], a[40:43], a[48:51], a[160:175]\n"
  "ds_read_b64_tr_b16 a[66:67], v6 offset:13312\n"
  "ds_read_b64_tr_b16 a[68:69], v7 offset:12288\n"
  "v_mfma_f32_32x32x16_bf16 a[224:239], a[40:43], a[52:55], a[224:239]\n"
  "ds_read_b64_tr_b16 a[70:71], v7 offset:13312\n"
  "v_mfma_f32_32x32x16_bf16 a[176:191], a[44:47], a[48:51], a[176:191]\n"
  "v_mfma_f32_32x32x16_bf16 a[240:255], a[44:47], a[52:55], a[240:255]\n"
  "s_waitcnt lgkmcnt(4)\n"
  "v_mfma_f32_32x32x16_bf16 a[128:143], a[56:59], a[72:75], a[128:143]\n"
  "v_mfma_f32_32x32x16_bf16 a[192:207], a[56:59], a[76:79], a[192:207]\n"
  "v_mfma_f32_32x32x16_bf16 a[144:159], a[60:63], a[72:75], a[144:159]\n"
  "v_mfma_f32_32x32x16_bf16 a[208:223], a[60:63], a[76:79], a[208:223]\n"
  "s_waitcnt lgkmcnt(0)\n"
  "v_mfma_f32_32x32x16_bf16 a[160:175], a[64:67], a[72:75], a[160:175]\n"
  "v_mfma_f32_32x32x16_bf16 a[224:239], a[64:67], a[76:79], a[224:239]\n"
  "v_mfma_f32_32x32x16_bf16 a[176:191], a[68:71], a[72:75], a[176:191]\n"
  "v_mfma_f32_32x32x16_bf16 a[240:255], a[68:71], a[76:79], a[240:255]\n"
  "v_accvgpr_read_b32 v21, a104\n"
  "v_accvgpr_read_b32 v22, a105\n"
  "v_add_u32 v0, 0x10000, v20\n"
  "v_add_u32 v1, 0x4000, v20\n"
  "v_accvgpr_write_b32 a104, 0\n"
  "v_accvgpr_write_b32 a105, 0\n"
  "v_accvgpr_write_b32 a32, 0\n"
  "v_accvgpr_write_b32 a33, 0\n"
  "v_accvgpr_write_b32 a34, 0\n"
  "v_accvgpr_write_b32 a35, 0\n"
  "v_accvgpr_write_b32 a36, 0\n"
  "v_accvgpr_write_b32 a37, 0\n"
  "s_waitcnt vmcnt(0)\n"
  "v_cmp_ne_u64 vcc, s[70:71], v[16:17]\n"
  "s_nop 0\n"
  "s_cbranch_vccnz PKV_%=\n"
  "PKV_%=_r:\n"
  "s_add_u32 s55, s53, 0x0\n"
  "s_add_u32 m0, s54, 0x0\n"
  "s_nop 0\n"
  "buffer_load_dwordx4 v20, s[24:27], s55 offen offset:0 sc1 lds\n"
  "buffer_load_dwordx4 v20, s[24:27], s55 offen offset:1024 sc1 lds\n"
  "buffer_load_dwordx4 v20, s[24:27], s55 offen offset:2048 sc1 lds\n"
  "buffer_load_dwordx4 v20, s[24:27], s55 offen offset:3072 sc1 lds\n"
  "v_accvgpr_read_b32 v128, a128\n"
  "v_accvgpr_read_b32 v129, a129\n"
  "v_accvgpr_read_b32 v130, a130\n"
  "v_accvgpr_read_b32 v131, a131\n"
  "v_accvgpr_read_b32 v132, a132\n"
  "v_accvgpr_read_b32 v133, a133\n"
  "s_add_u32 s55, s53, 0x4000\n"
  "s_add_u32 m0, s53, 0x4000\n"
  "s_nop 0\n"
  "buffer_load_dwordx4 v20, s[28:31], s55 offen offset:0 sc1 lds\n"
  "v_accvgpr_read_b32 v134, a134\n"
  "v_accvgpr_read_b32 v135, a135\n"
  "v_accvgpr_read_b32 v136, a136\n"
  "v_accvgpr_read_b32 v137, a137\n"
  "v_accvgpr_read_b32 v138, a138\n"
  "v_accvgpr_read_b32 v139, a139\n"
  "buffer_load_dwordx4 v20, s[28:31], s55 offen offset:1024 sc1 lds\n"
  "v_accvgpr_read_b32 v140, a140\n"
  "v_accvgpr_read_b32 v141, a141\n"
  "v_accvgpr_read_b32 v142, a142\n"
  "v_accvgpr_read_b32 v143, a143\n"
  "v_accvgpr_read_b32 v144, a144\n"
  "v_accvgpr_read_b32 v145, a145\n"
  "buffer_load_dwordx4 v20, s[28:31], s55 offen offset:2048 sc1 lds\n"
  "v_accvgpr_read_b32 v146, a146\n"
  "v_accvgpr_read_b32 v147, a147\n"
  "v_accvgpr_read_b32 v148, a148\n"
  "v_accvgpr_read_b32 v149, a149\n"
  "v_accvgpr_read_b32 v150, a150\n"
  "v_accvgpr_read_b32 v151, a151\n"
  "buffer_load_dwordx4 v20, s[28:31], s55 offen offset:3072 sc1 lds\n"
  "v_accvgpr_read_b32 v152, a152\n"
  "v_accvgpr_read_b32 v153, a153\n"
  "v_accvgpr_read_b32 v154, a154\n"
  "v_accvgpr_read_b32 v155, a155\n"
  "v_accvgpr_read_b32 v156, a156\n"
  "v_accvgpr_read_b32 v157, a157\n"
  "s_add_u32 s55, s53, 0x4000\n"
  "s_add_u32 m0, s54, 0x4000\n"
  "s_nop 0\n"
  "buffer_load_dwordx4 v20, s[24:27], s55 offen offset:0 sc1 lds\n"
  "v_accvgpr_read_b32 v158, a158\n"
  "v_accvgpr_read_b32 v159, a159\n"
  "v_accvgpr_read_b32 v160, a160\n"
  "v_accvgpr_read_b32 v161, a161\n"
  "v_accvgpr_read_b32 v162, a162\n"
  "v_accvgpr_read_b32 v163, a163\n"
  "buffer_load_dwordx4 v20, s[24:27], s55 offen offset:1024 sc1 lds\n"
  "v_accvgpr_read_b32 v164, a164\n"
  "v_accvgpr_read_b32 v165, a165\n"
  "v_accvgpr_read_b32 v166, a166\n"
  "v_accvgpr_read_b32 v167, a167\n"
  "v_accvgpr_read_b32 v168, a168\n"
  "v_accvgpr_read_b32 v169, a169\n"
  "buffer_load_dwordx4 v20, s[24:27], s55 offen offset:2048 sc1 lds\n"
  "v_accvgpr_read_b32 v170, a170\n"
  "v_accvgpr_read_b32 v171, a171\n"
  "v_accvgpr_read_b32 v172, a172\n"
  "v_accvgpr_read_b32 v173, a173\n"
  "v_accvgpr_read_b32 v174, a174\n"
  "v_accvgpr_read_b32 v175, a175\n"
  "buffer_load_dwordx4 v20, s[24:27], s55 offen offset:3072 sc1 lds\n"
  "v_accvgpr_read_b32 v176, a176\n"
  "v_accvgpr_read_b32 v177, a177\n"
  "v_accvgpr_read_b32 v178, a178\n"
  "v_accvgpr_read_b32 v179, a179\n"
  "v_accvgpr_read_b32 v180, a180\n"
  "v_accvgpr_read_b32 v181, a181\n"
  "s_add_u32 s55, s53, 0x8000\n"
  "s_add_u32 m0, s53, 0x8000\n"
  "s_nop 0\n"
  "buffer_load_dwordx4 v20, s[28:31], s55 offen offset:0 sc1 lds\n"
  "v_accvgpr_read_b32 v182, a182\n"
  "v_accvgpr_read_b32 v183, a183\n"
  "v_accvgpr_read_b32 v184, a184\n"
  "v_accvgpr_read_b32 v185, a185\n"
  "v_accvgpr_read_b32 v186, a186\n"
  "v_accvgpr_read_b32 v187, a187\n"
  "buffer_load_dwordx4 v20, s[28:31], s55 offen offset:1024 sc1 lds\n"
  "v_accvgpr_read_b32 v188, a188\n"
  "v_accvgpr_read_b32 v189, a189\n"
  "v_accvgpr_read_b32 v190, a190\n"
  "v_accvgpr_read_b32 v191, a191\n"
  "v_accvgpr_read_b32 v192, a192\n"
  "v_accvgpr_read_b32 v193, a193\n"
  "buffer_load_dwordx4 v20, s[28:31], s55 offen offset:2048 sc1 lds\n"
  "v_accvgpr_read_b32 v194, a194\n"
  "v_accvgpr_read_b32 v195, a195\n"
  "v_accvgpr_read_b32 v196, a196\n"
  "v_accvgpr_read_b32 v197, a197\n"
  "v_accvgpr_read_b32 v198, a198\n"
  "v_accvgpr_read_b32 v199, a199\n"
  "buffer_load_dwordx4 v20, s[28:31], s55 offen offset:3072 sc1 lds\n"
  "v_accvgpr_read_b32 v200, a200\n"
  "v_accvgpr_read_b32 v201, a201\n"
  "v_accvgpr_read_b32 v202, a202\n"
  "v_accvgpr_read_b32 v203, a203\n"
  "v_accvgpr_read_b32 v204, a204\n"
  "v_accvgpr_read_b32 v205, a205\n"
  "s_add_u32 s55, s53, 0x8000\n"
  "s_add_u32 m0, s54, 0x8000\n"
  "s_nop 0\n"
  "buffer_load_dwordx4 v20, s[24:27], s55 offen offset:0 sc1 lds\n"
  "v_accvgpr_read_b32 v206, a206\n"
  "v_accvgpr_read_b32 v207, a207\n"
  "v_accvgpr_read_b32 v208, a208\n"
  "v_accvgpr_read_b32 v209, a209\n"
  "v_accvgpr_read_b32 v210, a210\n"
  "v_accvgpr_read_b32 v211, a211\n"
  "buffer_load_dwordx4 v20, s[24:27], s55 offen offset:1024 sc1 lds\n"
  "v_accvgpr_read_b32 v212, a212\n"
  "v_accvgpr_read_b32 v213, a213\n"
  "v_accvgpr_read_b32 v214, a214\n"
  "v_accvgpr_read_b32 v215, a215\n"
  "v_accvgpr_read_b32 v216, a216\n"
  "v_accvgpr_read_b32 v217, a217\n"
  "buffer_load_dwordx4 v20, s[24:27], s55 offen offset:2048 sc1 lds\n"
  "v_accvgpr_read_b32 v218, a218\n"
  "v_accvgpr_read_b32 v219, a219\n"
  "v_accvgpr_read_b32 v220, a220\n"
  "v_accvgpr_read_b32 v221, a221\n"
  "v_accvgpr_read_b32 v222, a222\n"
  "v_accvgpr_read_b32 v223, a223\n"
  "buffer_load_dwordx4 v20, s[24:27], s55 offen offset:3072 sc1 lds\n"
  "v_accvgpr_read_b32 v224, a224\n"
  "v_accvgpr_read_b32 v225, a225\n"
  "v_accvgpr_read_b32 v226, a226\n"
  "v_accvgpr_read_b32 v227, a227\n"
  "v_accvgpr_read_b32 v228, a228\n"
  "v_accvgpr_read_b32 v229, a229\n"
  "s_add_u32 s55, s53, 0xc000\n"
  "s_add_u32 m0, s53, 0xc000\n"
  "s_nop 0\n"
  "buffer_load_dwordx4 v20, s[28:31], s55 offen offset:0 sc1 lds\n"
  "v_accvgpr_read_b32 v230, a230\n"
  "v_accvgpr_read_b32 v231, a231\n"
  "v_accvgpr_read_b32 v232, a232\n"
  "v_accvgpr_read_b32 v233, a233\n"
  "v_accvgpr_read_b32 v234, a234\n"
  "v_accvgpr_read_b32 v235, a235\n"
  "buffer_load_dwordx4 v20, s[28:31], s55 offen offset:1024 sc1 lds\n"
  "v_accvgpr_read_b32 v236, a236\n"
  "v_accvgpr_read_b32 v237, a237\n"
  "v_accvgpr_read_b32 v238, a238\n"
  "v_accvgpr_read_b32 v239, a239\n"
  "v_accvgpr_read_b32 v240, a240\n"
  "v_accvgpr_read_b32 v241, a241\n"
  "buffer_load_dwordx4 v20, s[28:31], s55 offen offset:2048 sc1 lds\n"
  "v_accvgpr_read_b32 v242, a242\n"
  "v_accvgpr_read_b32 v243, a243\n"
  "v_accvgpr_read_b32 v244, a244\n"
  "v_accvgpr_read_b32 v245, a245\n"
  "v_accvgpr_read_b32 v246, a246\n"
  "v_accvgpr_read_b32 v247, a247\n"
  "buffer_load_dwordx4 v20, s[28:31], s55 offen offset:3072 sc1 lds\n"
  "v_accvgpr_read_b32 v248, a248\n"
  "v_accvgpr_read_b32 v249, a249\n"
  "v_accvgpr_read_b32 v250, a250\n"
  "v_accvgpr_read_b32 v251, a251\n"
  "v_accvgpr_read_b32 v252, a252\n"
  "v_accvgpr_read_b32 v253, a253\n"
  "v_accvgpr_read_b32 v254, a254\n"
  "v_accvgpr_read_b32 v255, a255\n"
  "v_pk_mul_f32 v[128:129], v[128:129], s[68:69]\n"
  "v_pk_mul_f32 v[130:131], v[130:131], s[68:69]\n"
  "v_pk_mul_f32 v[132:133], v[132:133], s[68:69]\n"
  "v_pk_mul_f32 v[134:135], v[134:135], s[68:69]\n"
  "v_pk_mul_f32 v[136:137], v[136:137], s[68:69]\n"
  "v_pk_mul_f32 v[138:139], v[138:139], s[68:69]\n"
  "v_pk_mul_f32 v[140:141], v[140:141], s[68:69]\n"
  "v_pk_mul_f32 v[142:143], v[142:143], s[68:69]\n"
  "v_cvt_pk_bf16_f32 v128, v128, v129\n"
  "v_cvt_pk_bf16_f32 v129, v130, v131\n"
  "v_cvt_pk_bf16_f32 v130, v132, v133\n"
  "v_cvt_pk_bf16_f32 v131, v134, v135\n"
  "v_cvt_pk_bf16_f32 v132, v136, v137\n"
  "v_cvt_pk_bf16_f32 v133, v138, v139\n"
  "v_cvt_pk_bf16_f32 v134, v140, v141\n"
  "v_cvt_pk_bf16_f32 v135, v142, v143\n"
  "v_accvgpr_write_b32 a128, v128\n"
  "v_accvgpr_write_b32 a129, v129\n"
  "v_accvgpr_write_b32 a130, v130\n"
  "v_accvgpr_write_b32 a131, v131\n"
  "v_accvgpr_write_b32 a132, v132\n"
  "v_accvgpr_write_b32 a133, v133\n"
  "v_accvgpr_write_b32 a134, v134\n"
  "v_accvgpr_write_b32 a135, v135\n"
  "v_pk_mul_f32 v[144:145], v[144:145], s[68:69]\n"
  "v_pk_mul_f32 v[146:147], v[146:147], s[68:69]\n"
  "v_pk_mul_f32 v[148:149], v[148:149], s[68:69]\n"
  "v_pk_mul_f32 v[150:151], v[150:151], s[68:69]\n"
  "v_pk_mul_f32 v[152:153], v[152:153], s[68:69]\n"
  "v_pk_mul_f32 v[154:155], v[154:155], s[68:69]\n"
  "v_pk_mul_f32 v[156:157], v[156:157], s[68:69]\n"
  "v_pk_mul_f32 v[158:159], v[158:159], s[68:69]\n"
  "v_cvt_pk_bf16_f32 v144, v144, v145\n"
  "v_cvt_pk_bf16_f32 v145, v146, v147\n"
  "v_cvt_pk_bf16_f32 v146, v148, v149\n"
  "v_cvt_pk_bf16_f32 v147, v150, v151\n"
  "v_cvt_pk_bf16_f32 v148, v152, v153\n"
  "v_cvt_pk_bf16_f32 v149, v154, v155\n"
  "v_cvt_pk_bf16_f32 v150, v156, v157\n"
  "v_cvt_pk_bf16_f32 v151, v158, v159\n"
  "v_accvgpr_write_b32 a136, v144\n"
  "v_accvgpr_write_b32 a137, v145\n"
  "v_accvgpr_write_b32 a138, v146\n"
  "v_accvgpr_write_b32 a139, v147\n"
  "v_accvgpr_write_b32 a140, v148\n"
  "v_accvgpr_write_b32 a141, v149\n"
  "v_accvgpr_write_b32 a142, v150\n"
  "v_accvgpr_write_b32 a143, v151\n"
  "v_pk_mul_f32 v[160:161], v[160:161], s[68:69]\n"
  "v_pk_mul_f32 v[162:163], v[162:163], s[68:69]\n"
  "v_pk_mul_f32 v[164:165], v[164:165], s[68:69]\n"
  "v_pk_mul_f32 v[166:167], v[166:167], s[68:69]\n"
  "v_pk_mul_f32 v[168:169], v[168:169], s[68:69]\n"
  "v_pk_mul_f32 v[170:171], v[170:171], s[68:69]\n"
  "v_pk_mul_f32 v[172:173], v[172:173], s[68:69]\n"
  "v_pk_mul_f32 v[174:175], v[174:175], s[68:69]\n"
  "v_cvt_pk_bf16_f32 v160, v160, v161\n"
  "v_cvt_pk_bf16_f32 v161, v162, v163\n"
  "v_cvt_pk_bf16_f32 v162, v164, v165\n"
  "v_cvt_pk_bf16_f32 v163, v166, v167\n"
  "v_cvt_pk_bf16_f32 v164, v168, v169\n"
  "v_cvt_pk_bf16_f32 v165, v170, v171\n"
  "v_cvt_pk_bf16_f32 v166, v172, v173\n"
  "v_cvt_pk_bf16_f32 v167, v174, v175\n"
  "v_accvgpr_write_b32 a144, v160\n"
  "v_accvgpr_write_b32 a145, v161\n"
  "v_accvgpr_write_b32 a146, v162\n"
  "v_accvgpr_write_b32 a147, v163\n"
  "v_accvgpr_write_b32 a148, v164\n"
  "v_accvgpr_write_b32 a149, v165\n"
  "v_accvgpr_write_b32 a150, v166\n"
  "v_accvgpr_write_b32 a151, v167\n"
  "v_pk_mul_f32 v[176:177], v[176:177], s[68:69]\n"
  "v_pk_mul_f32 v[178:179], v[178:179], s[68:69]\n"
  "v_pk_mul_f32 v[180:181], v[180:181], s[68:69]\n"
  "v_pk_mul_f32 v[182:183], v[182:183], s[68:69]\n"
  "v_pk_mul_f32 v[184:185], v[184:185], s[68:69]\n"
  "v_pk_mul_f32 v[186:187], v[186:187], s[68:69]\n"
  "v_pk_mul_f32 v[188:189], v[188:189], s[68:69]\n"
  "v_pk_mul_f32 v[190:191], v[190:191], s[68:69]\n"
  "v_cvt_pk_bf16_f32 v176, v176, v177\n"
  "v_cvt_pk_bf16_f32 v177, v178, v179\n"
  "v_cvt_pk_bf16_f32 v178, v180, v181\n"
  "v_cvt_pk_bf16_f32 v179, v182, v183\n"
  "v_cvt_pk_bf16_f32 v180, v184, v185\n"
  "v_cvt_pk_bf16_f32 v181, v186, v187\n"
  "v_cvt_pk_bf16_f32 v182, v188, v189\n"
  "v_cvt_pk_bf16_f32 v183, v190, v191\n"
  "v_accvgpr_write_b32 a152, v176\n"
  "v_accvgpr_write_b32 a153, v177\n"
  "v_accvgpr_write_b32 a154, v178\n"
  "v_accvgpr_write_b32 a155, v179\n"
  "v_accvgpr_write_b32 a156, v180\n"
  "v_accvgpr_write_b32 a157, v181\n"
  "v_accvgpr_write_b32 a158, v182\n"
  "v_accvgpr_write_b32 a159, v183\n"
  "v_pk_mul_f32 v[192:193], v[192:193], s[68:69]\n"
  "v_pk_mul_f32 v[194:195], v[194:195], s[68:69]\n"
  "v_pk_mul_f32 v[196:197], v[196:197], s[68:69]\n"
  "v_pk_mul_f32 v[198:199], v[198:199], s[68:69]\n"
  "v_pk_mul_f32 v[200:201], v[200:201], s[68:69]\n"
  "v_pk_mul_f32 v[202:203], v[202:203], s[68:69]\n"
  "v_pk_mul_f32 v[204:205], v[204:205], s[68:69]\n"
  "v_pk_mul_f32 v[206:207], v[206:207], s[68:69]\n"
  "v_cvt_pk_bf16_f32 v192, v192, v193\n"
  "v_cvt_pk_bf16_f32 v193, v194, v195\n"
  "v_cvt_pk_bf16_f32 v194, v196, v197\n"
  "v_cvt_pk_bf16_f32 v195, v198, v199\n"
  "v_cvt_pk_bf16_f32 v196, v200, v201\n"
  "v_cvt_pk_bf16_f32 v197, v202, v203\n"
  "v_cvt_pk_bf16_f32 v198, v204, v205\n"
  "v_cvt_pk_bf16_f32 v199, v206, v207\n"
  "v_accvgpr_write_b32 a160, v192\n"
  "v_accvgpr_write_b32 a161, v193\n"
  "v_accvgpr_write_b32 a162, v194\n"
  "v_accvgpr_write_b32 a163, v195\n"
  "v_accvgpr_write_b32 a164, v196\n"
  "v_accvgpr_write_b32 a165, v197\n"
  "v_accvgpr_write_b32 a166, v198\n"
  "v_accvgpr_write_b32 a167, v199\n"
  "v_pk_mul_f32 v[208:209], v[208:209], s[68:69]\n"
  "v_pk_mul_f32 v[210:211], v[210:211], s[68:69]\n"
  "v_pk_mul_f32 v[212:213], v[212:213], s[68:69]\n"
  "v_pk_mul_f32 v[214:215], v[214:215], s[68:69]\n"
  "v_pk_mul_f32 v[216:217], v[216:217], s[68:69]\n"
  "v_pk_mul_f32 v[218:219], v[218:219], s[68:69]\n"
  "v_pk_mul_f32 v[220:221], v[220:221], s[68:69]\n"
  "v_pk_mul_f32 v[222:223], v[222:223], s[68:69]\n"
  "v_cvt_pk_bf16_f32 v208, v208, v209\n"
  "v_cvt_pk_bf16_f32 v209, v210, v211\n"
  "v_cvt_pk_bf16_f32 v210, v212, v213\n"
  "v_cvt_pk_bf16_f32 v211, v214, v215\n"
  "v_cvt_pk_bf16_f32 v212, v216, v217\n"
  "v_cvt_pk_bf16_f32 v213, v218, v219\n"
  "v_cvt_pk_bf16_f32 v214, v220, v221\n"
  "v_cvt_pk_bf16_f32 v215, v222, v223\n"
  "v_accvgpr_write_b32 a168, v208\n"
  "v_accvgpr_write_b32 a169, v209\n"
  "v_accvgpr_write_b32 a170, v210\n"
  "v_accvgpr_write_b32 a171, v211\n"
  "v_accvgpr_write_b32 a172, v212\n"
  "v_accvgpr_write_b32 a173, v213\n"
  "v_accvgpr_write_b32 a174, v214\n"
  "v_accvgpr_write_b32 a175, v215\n"
  "v_pk_mul_f32 v[224:225], v[224:225], s[68:69]\n"
  "v_pk_mul_f32 v[226:227], v[226:227], s[68:69]\n"
  "v_pk_mul_f32 v[228:229], v[228:229], s[68:69]\n"
  "v_pk_mul_f32 v[230:231], v[230:231], s[68:69]\n"
  "v_pk_mul_f32 v[232:233], v[232:233], s[68:69]\n"
  "v_pk_mul_f32 v[234:235], v[234:235], s[68:69]\n"
  "v_pk_mul_f32 v[236:237], v[236:237], s[68:69]\n"
  "v_pk_mul_f32 v[238:239], v[238:239], s[68:69]\n"
  "v_cvt_pk_bf16_f32 v224, v224, v225\n"
  "v_cvt_pk_bf16_f32 v225, v226, v227\n"
  "v_cvt_pk_bf16_f32 v226, v228, v229\n"
  "v_cvt_pk_bf16_f32 v227, v230, v231\n"
  "v_cvt_pk_bf16_f32 v228, v232, v233\n"
  "v_cvt_pk_bf16_f32 v229, v234, v235\n"
  "v_cvt_pk_bf16_f32 v230, v236, v237\n"
  "v_cvt_pk_bf16_f32 v231, v238, v239\n"
  "v_accvgpr_write_b32 a176, v224\n"
  "v_accvgpr_write_b32 a177, v225\n"
  "v_accvgpr_write_b32 a178, v226\n"
  "v_accvgpr_write_b32 a179, v227\n"
  "v_accvgpr_write_b32 a180, v228\n"
  "v_accvgpr_write_b32 a181, v229\n"
  "v_accvgpr_write_b32 a182, v230\n"
  "v_accvgpr_write_b32 a183, v231\n"
  "v_pk_mul_f32 v[240:241], v[240:241], s[68:69]\n"
  "v_pk_mul_f32 v[242:243], v[242:243], s[68:69]\n"
  "v_pk_mul_f32 v[244:245], v[244:245], s[68:69]\n"
  "v_pk_mul_f32 v[246:247], v[246:247], s[68:69]\n"
  "v_pk_mul_f32 v[248:249], v[248:249], s[68:69]\n"
  "v_pk_mul_f32 v[250:251], v[250:251], s[68:69]\n"
  "v_pk_mul_f32 v[252:253], v[252:253], s[68:69]\n"
  "v_pk_mul_f32 v[254:255], v[254:255], s[68:69]\n"
  "v_cvt_pk_bf16_f32 v240, v240, v241\n"
  "v_cvt_pk_bf16_f32 v241, v242, v243\n"
  "v_cvt_pk_bf16_f32 v242, v244, v245\n"
  "v_cvt_pk_bf16_f32 v243, v246, v247\n"
  "v_cvt_pk_bf16_f32 v244, v248, v249\n"
  "v_cvt_pk_bf16_f32 v245, v250, v251\n"
  "v_cvt_pk_bf16_f32 v246, v252, v253\n"
  "v_cvt_pk_bf16_f32 v247, v254, v255\n"
  "v_accvgpr_write_b32 a184, v240\n"
  "v_accvgpr_write_b32 a185, v241\n"
  "v_accvgpr_write_b32 a186, v242\n"
  "v_accvgpr_write_b32 a187, v243\n"
  "v_accvgpr_write_b32 a188, v244\n"
  "v_accvgpr_write_b32 a189, v245\n"
  "v_accvgpr_write_b32 a190, v246\n"
  "v_accvgpr_write_b32 a191, v247\n"
  "s_waitcnt vmcnt(20) lgkmcnt(0)\n"
  "s_barrier\n"
  "ds_read_b128 a[192:195], v0 offset:0\n"
  "ds_read_b128 a[196:199], v0 offset:1024\n"
  "ds_read_b128 a[200:203], v0 offset:2048\n"
  "ds_read_b128 a[204:207], v0 offset:3072\n"
  "ds_read_b128 a[208:211], v0 offset:4096\n"
  "ds_read_b128 a[212:215], v0 offset:5120\n"
  "ds_read_b128 a[216:219], v0 offset:6144\n"
  "ds_read_b128 a[220:223], v0 offset:7168\n"
  "ds_read_b128 a[224:227], v0 offset:8192\n"
  "ds_read_b128 a[228:231], v0 offset:9216\n"
  "ds_read_b128 a[232:235], v0 offset:10240\n"
  "ds_read_b128 a[236:239], v0 offset:11264\n"
  "ds_read_b128 a[240:243], v0 offset:12288\n"
  "ds_read_b128 a[244:247], v0 offset:13312\n"
  "ds_read_b128 a[248:251], v0 offset:14336\n"
  "ds_read_b128 a[252:255], v0 offset:15360\n"
  "s_add_u32 s55, s53, 0xc000\n"
  "s_add_u32 m0, s54, 0xc000\n"
  "s_nop 0\n"
  "buffer_load_dwordx4 v20, s[24:27], s55 offen offset:0 sc1 lds\n"
  "buffer_load_dwordx4 v20, s[24:27], s55 offen offset:1024 sc1 lds\n"
  "buffer_load_dwordx4 v20, s[24:27], s55 offen offset:2048 sc1 lds\n"
  "buffer_load_dwordx4 v20, s[24:27], s55 offen offset:3072 sc1 lds\n"
  "s_waitcnt lgkmcnt(15)\n"
  "v_mfma_f32_32x32x16_bf16 v[32:47], a[192:195], a[128:131], 0\n"
  "s_waitcnt lgkmcnt(14)\n"
  "v_mfma_f32_32x32x16_bf16 v[32:47], a[196:199], a[132:135], v[32:47]\n"
  "s_waitcnt lgkmcnt(13)\n"
  "v_mfma_f32_32x32x16_bf16 v[32:47], a[200:203], a[136:139], v[32:47]\n"
  "s_waitcnt lgkmcnt(12)\n"
  "v_mfma_f32_32x32x16_bf16 v[32:47], a[204:207], a[140:143], v[32:47]\n"
  "s_waitcnt lgkmcnt(11)\n"
  "v_mfma_f32_32x32x16_bf16 v[32:47], a[208:211], a[144:147], v[32:47]\n"
  "s_waitcnt lgkmcnt(10)\n"
  "v_mfma_f32_32x32x16_bf16 v[32:47], a[212:215], a[148:151], v[32:47]\n"
  "s_waitcnt lgkmcnt(9)\n"
  "v_mfma_f32_32x32x16_bf16 v[32:47], a[216:219], a[152:155], v[32:47]\n"
  "s_waitcnt lgkmcnt(8)\n"
  "v_mfma_f32_32x32x16_bf16 v[32:47], a[220:223], a[156:159], v[32:47]\n"
  "s_waitcnt lgkmcnt(7)\n"
  "v_mfma_f32_32x32x16_bf16 v[48:63], a[224:227], a[128:131], 0\n"
  "s_waitcnt lgkmcnt(6)\n"
  "v_mfma_f32_32x32x16_bf16 v[48:63], a[228:231], a[132:135], v[48:63]\n"
  "s_waitcnt lgkmcnt(5)\n"
  "v_mfma_f32_32x32x16_bf16 v[48:63], a[232:235], a[136:139], v[48:63]\n"
  "s_waitcnt lgkmcnt(4)\n"
  "v_mfma_f32_32x32x16_bf16 v[48:63], a[236:239], a[140:143], v[48:63]\n"
  "s_waitcnt lgkmcnt(3)\n"
  "v_mfma_f32_32x32x16_bf16 v[48:63], a[240:243], a[144:147], v[48:63]\n"
  "s_waitcnt lgkmcnt(2)\n"
  "v_mfma_f32_32x32x16_bf16 v[48:63], a[244:247], a[148:151], v[48:63]\n"
  "s_waitcnt lgkmcnt(1)\n"
  "v_mfma_f32_32x32x16_bf16 v[48:63], a[248:251], a[152:155], v[48:63]\n"
  "s_waitcnt lgkmcnt(0)\n"
  "v_mfma_f32_32x32x16_bf16 v[48:63], a[252:255], a[156:159], v[48:63]\n"
  "v_mov_b32 v224, 0\n"
  "v_mov_b32 v225, 0\n"
  "v_mov_b32 v226, 0\n"
  "v_mov_b32 v227, 0\n"
  "v_mov_b32 v228, 0\n"
  "v_mov_b32 v229, 0\n"
  "v_mov_b32 v230, 0\n"
  "v_mov_b32 v231, 0\n"
  "v_mov_b32 v232, 0\n"
  "v_mov_b32 v233, 0\n"
  "v_mov_b32 v234, 0\n"
  "v_mov_b32 v235, 0\n"
  "v_mov_b32 v236, 0\n"
  "v_mov_b32 v237, 0\n"
  "v_mov_b32 v238, 0\n"
  "v_mov_b32 v239, 0\n"
  "v_mov_b32 v240, 0\n"
  "v_mov_b32 v241, 0\n"
  "v_mov_b32 v242, 0\n"
  "v_mov_b32 v243, 0\n"
  "v_mov_b32 v244, 0\n"
  "v_mov_b32 v245, 0\n"
  "v_mov_b32 v246, 0\n"
  "v_mov_b32 v247, 0\n"
  "v_mov_b32 v248, 0\n"
  "v_mov_b32 v249, 0\n"
  "v_mov_b32 v250, 0\n"
  "v_mov_b32 v251, 0\n"
  "v_mov_b32 v252, 0\n"
  "v_mov_b32 v253, 0\n"
  "v_mov_b32 v254, 0\n"
  "v_mov_b32 v255, 0\n"
  "v_mov_b32 v14, 0\n"
  "v_mov_b32 v15, 0\n"
  "s_cmp_ge_u32 s60, s65\n"
  "s_cbranch_scc1 MP_%=\n"
  "MP_%=_r:\n"
  "v_mfma_f32_32x32x16_bf16 v[64:79], a[192:195], a[160:163], v[224:239]\n"
  "v_max3_f32 v16, v32, v33, v34\n"
  "v_max3_f32 v17, v35, v36, v37\n"
  "v_max3_f32 v16, v16, v38, v39\n"
  "v_max3_f32 v17, v17, v40, v41\n"
  "v_max3_f32 v16, v16, v42, v43\n"
  "v_max3_f32 v17, v17, v44, v45\n"
  "v_mfma_f32_32x32x16_bf16 v[64:79], a[196:199], a[164:167], v[64:79]\n"
  "v_max3_f32 v16, v16, v46, v47\n"
  "v_max3_f32 v17, v17, v48, v49\n"
  "v_max3_f32 v16, v16, v50, v51\n"
  "v_max3_f32 v17, v17, v52, v53\n"
  "v_max3_f32 v16, v16, v54, v55\n"
  "v_mfma_f32_32x32x16_bf16 v[64:79], a[200:203], a[168:171], v[64:79]\n"
  "v_max3_f32 v17, v17, v56, v57\n"
  "v_max3_f32 v16, v16, v58, v59\n"
  "v_max3_f32 v17, v17, v60, v61\n"
  "v_max3_f32 v16, v16, v62, v63\n"
  "v_max_f32 v16, v16, v17\n"
  "v_mfma_f32_32x32x16_bf16 v[64:79], a[204:207], a[172:175], v[64:79]\n"
  "v_mov_b32 v17, v16\n"
  "s_nop 1\n"
  "v_permlane32_swap_b32 v16, v17\n"
  "v_max_f32 v16, v16, v17\n"
  "v_sub_f32 v32, v32, v16\n"
  "v_sub_f32 v33, v33, v16\n"
  "v_mfma_f32_32x32x16_bf16 v[64:79], a[208:211], a[176:179], v[64:79]\n"
  "v_sub_f32 v34, v34, v16\n"
  "v_sub_f32 v35, v35, v16\n"
  "v_sub_f32 v36, v36, v16\n"
  "v_sub_f32 v37, v37, v16\n"
  "v_sub_f32 v38, v38, v16\n"
  "v_mfma_f32_32x32x16_bf16 v[64:79], a[212:215], a[180:183], v[64:79]\n"
  "v_sub_f32 v39, v39, v16\n"
  "v_sub_f32 v40, v40, v16\n"
  "v_sub_f32 v41, v41, v16\n"
  "v_sub_f32 v42, v42, v16\n"
  "v_sub_f32 v43, v43, v16\n"
  "v_sub_f32 v44, v44, v16\n"
  "v_mfma_f32_32x32x16_bf16 v[64:79], a[216:219], a[184:187], v[64:79]\n"
  "v_sub_f32 v45, v45, v16\n"
  "v_sub_f32 v46, v46, v16\n"
  "v_sub_f32 v47, v47, v16\n"
  "v_sub_f32 v48, v48, v16\n"
  "v_sub_f32 v49, v49, v16\n"
  "v_mfma_f32_32x32x16_bf16 v[64:79], a[220:223], a[188:191], v[64:79]\n"
  "v_sub_f32 v50, v50, v16\n"
  "v_sub_f32 v51, v51, v16\n"
  "v_sub_f32 v52, v52, v16\n"
  "v_sub_f32 v53, v53, v16\n"
  "v_sub_f32 v54, v54, v16\n"
  "v_mfma_f32_32x32x16_bf16 v[80:95], a[224:227], a[160:163], v[240:255]\n"
  "v_sub_f32 v55, v55, v16\n"
  "v_sub_f32 v56, v56, v16\n"
  "v_sub_f32 v57, v57, v16\n"
  "v_sub_f32 v58, v58, v16\n"
  "v_sub_f32 v59, v59, v16\n"
  "v_mfma_f32_32x32x16_bf16 v[80:95], a[228:231], a[164:167], v[80:95]\n"
  "v_sub_f32 v60, v60, v16\n"
  "v_sub_f32 v61, v61, v16\n"
  "v_sub_f32 v62, v62, v16\n"
  "v_sub_f32 v63, v63, v16\n"
  "v_sub_f32 v192, 0, v16\n"
  "v_mfma_f32_32x32x16_bf16 v[80:95], a[232:235], a[168:171], v[80:95]\n"
  "v_sub_f32 v193, 0, v16\n"
  "v_sub_f32 v194, 0, v16\n"
  "v_sub_f32 v195, 0, v16\n"
  "v_sub_f32 v196, 0, v16\n"
  "v_sub_f32 v197, 0, v16\n"
  "v_sub_f32 v198, 0, v16\n"
  "v_mfma_f32_32x32x16_bf16 v[80:95], a[236:239], a[172:175], v[80:95]\n"
  "v_sub_f32 v199, 0, v16\n"
  "v_sub_f32 v200, 0, v16\n"
  "v_sub_f32 v201, 0, v16\n"
  "v_sub_f32 v202, 0, v16\n"
  "v_sub_f32 v203, 0, v16\n"
  "v_mfma_f32_32x32x16_bf16 v[80:95], a[240:243], a[176:179], v[80:95]\n"
  "v_sub_f32 v204, 0, v16\n"
  "v_sub_f32 v205, 0, v16\n"
  "v_sub_f32 v206, 0, v16\n"
  "v_sub_f32 v207, 0, v16\n"
  "v_sub_f32 v208, 0, v16\n"
  "v_mfma_f32_32x32x16_bf16 v[80:95], a[244:247], a[180:183], v[80:95]\n"
  "v_sub_f32 v209, 0, v16\n"
  "v_sub_f32 v210, 0, v16\n"
  "v_sub_f32 v211, 0, v16\n"
  "v_sub_f32 v212, 0, v16\n"
  "v_sub_f32 v213, 0, v16\n"
  "v_mfma_f32_32x32x16_bf16 v[80:95], a[248:251], a[184:187], v[80:95]\n"
  "v_sub_f32 v214, 0, v16\n"
  "v_sub_f32 v215, 0, v16\n"
  "v_sub_f32 v216, 0, v16\n"
  "v_sub_f32 v217, 0, v16\n"
  "v_sub_f32 v218, 0, v16\n"
  "v_mfma_f32_32x32x16_bf16 v[80:95], a[252:255], a[188:191], v[80:95]\n"
  "v_sub_f32 v219, 0, v16\n"
  "v_sub_f32 v220, 0, v16\n"
  "v_sub_f32 v221, 0, v16\n"
  "v_sub_f32 v222, 0, v16\n"
  "v_sub_f32 v223, 0, v16\n"
  "v_exp_f32 v2, v32\n"
  "v_accvgpr_write_b32 a79, 0\n"
  "v_accvgpr_write_b32 a78, 0\n"
  "v_exp_f32 v3, v33\n"
  "v_accvgpr_write_b32 a77, 0\n"
  "v_accvgpr_write_b32 a76, 0\n"
  "v_cvt_pk_bf16_f32 v96, v2, v3\n"
  "v_exp_f32 v4, v34\n"
  "v_accvgpr_write_b32 a75, 0\n"
  "v_accvgpr_write_b32 a74, 0\n"
  "v_add_f32 v10, v2, v4\n"
  "v_exp_f32 v5, v35\n"
  "v_accvgpr_write_b32 a73, 0\n"
  "v_accvgpr_write_b32 a72, 0\n"
  "v_add_f32 v11, v3, v5\n"
  "v_cvt_pk_bf16_f32 v97, v4, v5\n"
  "v_exp_f32 v6, v36\n"
  "v_accvgpr_write_b32 a71, 0\n"
  "v_accvgpr_write_b32 a70, 0\n"
  "v_add_f32 v10, v10, v6\n"
  "v_exp_f32 v7, v37\n"
  "v_accvgpr_write_b32 a69, 0\n"
  "v_accvgpr_write_b32 a68, 0\n"
  "v_add_f32 v11, v11, v7\n"
  "v_cvt_pk_bf16_f32 v98, v6, v7\n"
  "v_exp_f32 v8, v38\n"
  "v_accvgpr_write_b32 a67, 0\n"
  "v_accvgpr_write_b32 a66, 0\n"
  "v_add_f32 v10, v10, v8\n"
  "v_exp_f32 v9, v39\n"
  "v_accvgpr_write_b32 a65, 0\n"
  "v_accvgpr_write_b32 a64, 0\n"
  "v_add_f32 v11, v11, v9\n"
  "v_cvt_pk_bf16_f32 v99, v8, v9\n"
  "v_exp_f32 v2, v40\n"
  "v_accvgpr_write_b32 a63, 0\n"
  "v_accvgpr_write_b32 a62, 0\n"
  "v_add_f32 v10, v10, v2\n"
  "v_exp_f32 v3, v41\n"
  "v_accvgpr_write_b32 a61, 0\n"
  "v_accvgpr_write_b32 a60, 0\n"
  "v_add_f32 v11, v11, v3\n"
  "v_cvt_pk_bf16_f32 v100, v2, v3\n"
  "v_exp_f32 v4, v42\n"
  "v_accvgpr_write_b32 a59, 0\n"
  "v_accvgpr_write_b32 a58, 0\n"
  "v_add_f32 v10, v10, v4\n"
  "v_exp_f32 v5, v43\n"
  "v_accvgpr_write_b32 a57, 0\n"
  "v_accvgpr_write_b32 a56, 0\n"
  "v_add_f32 v11, v11, v5\n"
  "v_cvt_pk_bf16_f32 v101, v4, v5\n"
  "v_exp_f32 v6, v44\n"
  "v_accvgpr_write_b32 a55, 0\n"
  "v_accvgpr_write_b32 a54, 0\n"
  "v_add_f32 v10, v10, v6\n"
  "v_exp_f32 v7, v45\n"
  "v_accvgpr_write_b32 a53, 0\n"
  "v_accvgpr_write_b32 a52, 0\n"
  "v_add_f32 v11, v11, v7\n"
  "v_cvt_pk_bf16_f32 v102, v6, v7\n"
  "v_exp_f32 v8, v46\n"
  "v_accvgpr_write_b32 a51, 0\n"
  "v_accvgpr_write_b32 a50, 0\n"
  "v_add_f32 v10, v10, v8\n"
  "v_exp_f32 v9, v47\n"
  "v_accvgpr_write_b32 a49, 0\n"
  "v_accvgpr_write_b32 a48, 0\n"
  "v_add_f32 v11, v11, v9\n"
  "v_cvt_pk_bf16_f32 v103, v8, v9\n"
  "v_exp_f32 v2, v48\n"
  "v_accvgpr_write_b32 a47, 0\n"
  "v_accvgpr_write_b32 a46, 0\n"
  "v_add_f32 v10, v10, v2\n"
  "v_exp_f32 v3, v49\n"
  "v_accvgpr_write_b32 a45, 0\n"
  "v_accvgpr_write_b32 a44, 0\n"
  "v_add_f32 v11, v11, v3\n"
  "v_cvt_pk_bf16_f32 v104, v2, v3\n"
  "v_exp_f32 v4, v50\n"
  "v_accvgpr_write_b32 a43, 0\n"
  "v_accvgpr_write_b32 a42, 0\n"
  "v_accvgpr_write_b32 a38, 0\n"
  "v_accvgpr_write_b32 a39, 0\n"
  "v_accvgpr_write_b32 a40, 0\n"
  "v_accvgpr_write_b32 a41, 0\n"
  "v_max3_f32 v16, v64, v65, v66\n"
  "v_max3_f32 v17, v67, v68, v69\n"
  "v_max3_f32 v16, v16, v70, v71\n"
  "v_max3_f32 v17, v17, v72, v73\n"
  "v_max3_f32 v16, v16, v74, v75\n"
  "v_max3_f32 v17, v17, v76, v77\n"
  "v_max3_f32 v16, v16, v78, v79\n"
  "v_max3_f32 v17, v17, v80, v81\n"
  "v_max3_f32 v16, v16, v82, v83\n"
  "v_max3_f32 v17, v17, v84, v85\n"
  "v_max3_f32 v16, v16, v86, v87\n"
  "v_max3_f32 v17, v17, v88, v89\n"
  "v_max3_f32 v16, v16, v90, v91\n"
  "v_max3_f32 v17, v17, v92, v93\n"
  "v_max3_f32 v16, v16, v94, v95\n"
  "v_max_f32 v16, v16, v17\n"
  "v_mov_b32 v17, v16\n"
  "s_nop 1\n"
  "v_permlane32_swap_b32 v16, v17\n"
  "v_max_f32 v16, v16, v17\n"
  "v_sub_f32 v224, v224, v16\n"
  "v_sub_f32 v225, v225, v16\n"
  "v_sub_f32 v226, v226, v16\n"
  "v_sub_f32 v227, v227, v16\n"
  "v_sub_f32 v228, v228, v16\n"
  "v_sub_f32 v229, v229, v16\n"
  "v_sub_f32 v230, v230, v16\n"
  "v_sub_f32 v231, v231, v16\n"
  "v_sub_f32 v232, v232, v16\n"
  "v_sub_f32 v233, v233, v16\n"
  "v_sub_f32 v234, v234, v16\n"
  "v_sub_f32 v235, v235, v16\n"
  "v_sub_f32 v236, v236, v16\n"
  "v_sub_f32 v237, v237, v16\n"
  "v_sub_f32 v238, v238, v16\n"
  "v_sub_f32 v239, v239, v16\n"
  "v_sub_f32 v240, v240, v16\n"
  "v_sub_f32 v241, v241, v16\n"
  "v_sub_f32 v242, v242, v16\n"
  "v_sub_f32 v243, v243, v16\n"
  "v_sub_f32 v244, v244, v16\n"
  "v_sub_f32 v245, v245, v16\n"
  "v_sub_f32 v246, v246, v16\n"
  "v_sub_f32 v247, v247, v16\n"
  "v_sub_f32 v248, v248, v16\n"
  "v_sub_f32 v249, v249, v16\n"
  "v_sub_f32 v250, v250, v16\n"
  "v_sub_f32 v251, v251, v16\n"
  "v_sub_f32 v252, v252, v16\n"
  "v_sub_f32 v253, v253, v16\n"
  "v_sub_f32 v254, v254, v16\n"
  "v_sub_f32 v255, v255, v16\n"
  "s_waitcnt vmcnt(20)\n"
  "v_accvgpr_write_b32 a127, 0\n"
  "v_lshlrev_b32 v31, 3, v20\n"
  "s_lshl_b32 s76, s79, 1\n"
  "s_mov_b64 s[32:33], s[12:13]\n"
  "s_mov_b32 s34, 0x500000\n"
  "buffer_load_dword v18, v31, s[32:35], s76 offen\n"
  "s_mov_b64 s[32:33], s[18:19]\n"
  "s_mov_b32 s34, 0xc8000\n"
  "buffer_load_dword v18, v31, s[32:35], s76 offen\n"
  "s_mov_b64 s[32:33], s[14:15]\n"
  "s_mov_b32 s34, 0x280000\n"
  "buffer_load_dword v18, v31, s[32:35], s76 offen\n"
  ".p2align 6\n"
  "LD_%=:\n"
  "v_mfma_f32_32x32x16_bf16 v[64:79], a[192:195], a[160:163], v[224:239]\n"
  "s_waitcnt vmcnt(16)\n"
  "s_barrier\n"
  "ds_read_b128 v[128:131], v1 offset:0\n"
  "v_add_f32 v10, v10, v4\n"
  "v_exp_f32 v5, v51\n"
  "v_mfma_f32_32x32x16_bf16 v[64:79], a[196:199], a[164:167], v[64:79]\n"
  "ds_read_b128 v[132:135], v1 offset:1024\n"
  "v_add_f32 v11, v11, v5\n"
  "v_cvt_pk_bf16_f32 v105, v4, v5\n"
  "v_exp_f32 v6, v52\n"
  "v_mfma_f32_32x32x16_bf16 v[64:79], a[200:203], a[168:171], v[64:79]\n"
  "ds_read_b128 v[136:139], v1 offset:2048\n"
  "v_add_f32 v10, v10, v6\n"
  "v_exp_f32 v7, v53\n"
  "v_pk_add_u16 v0, v0, s61\n"
  "v_mfma_f32_32x32x16_bf16 v[64:79], a[204:207], a[172:175], v[64:79]\n"
  "ds_read_b128 v[140:143], v1 offset:3072\n"
  "v_add_f32 v11, v11, v7\n"
  "v_cvt_pk_bf16_f32 v106, v6, v7\n"
  "v_exp_f32 v8, v54\n"
  "v_mfma_f32_32x32x16_bf16 v[64:79], a[208:211], a[176:179], v[64:79]\n"
  "ds_read_b128 v[144:147], v1 offset:4096\n"
  "v_add_f32 v10, v10, v8\n"
  "v_exp_f32 v9, v55\n"
  "v_mfma_f32_32x32x16_bf16 v[64:79], a[212:215], a[180:183], v[64:79]\n"
  "ds_read_b128 v[148:151], v1 offset:5120\n"
  "v_add_f32 v11, v11, v9\n"
  "v_cvt_pk_bf16_f32 v107, v8, v9\n"
  "v_exp_f32 v2, v56\n"
  "v_mfma_f32_32x32x16_bf16 v[64:79], a[216:219], a[184:187], v[64:79]\n"
  "ds_read_b128 v[152:155], v1 offset:6144\n"
  "v_add_f32 v10, v10, v2\n"
  "v_exp_f32 v3, v57\n"
  "v_mfma_f32_32x32x16_bf16 v[64:79], a[220:223], a[188:191], v[64:79]\n"
  "ds_read_b128 v[156:159], v1 offset:7168\n"
  "v_add_f32 v11, v11, v3\n"
  "v_cvt_pk_bf16_f32 v108, v2, v3\n"
  "v_exp_f32 v4, v58\n"
  "v_mfma_f32_32x32x16_bf16 v[80:95], a[224:227], a[160:163], v[240:255]\n"
  "ds_read_b128 v[160:163], v1 offset:8192\n"
  "v_add_f32 v10, v10, v4\n"
  "v_exp_f32 v5, v59\n"
  "v_mfma_f32_32x32x16_bf16 v[80:95], a[228:231], a[164:167], v[80:95]\n"
  "ds_read_b128 v[164:167], v1 offset:9216\n"
  "v_add_f32 v11, v11, v5\n"
  "v_cvt_pk_bf16_f32 v109, v4, v5\n"
  "v_exp_f32 v6, v60\n"
  "v_mfma_f32_32x32x16_bf16 v[80:95], a[232:235], a[168:171], v[80:95]\n"
  "ds_read_b128 v[168:171], v1 offset:10240\n"
  "v_add_f32 v10, v10, v6\n"
  "v_exp_f32 v7, v61\n"
  "v_mfma_f32_32x32x16_bf16 v[80:95], a[236:239], a[172:175], v[80:95]\n"
  "ds_read_b128 v[172:175], v1 offset:11264\n"
  "v_add_f32 v11, v11, v7\n"
  "v_cvt_pk_bf16_f32 v110, v6, v7\n"
  "v_exp_f32 v8, v62\n"
  "v_mfma_f32_32x32x16_bf16 v[80:95], a[240:243], a[176:179], v[80:95]\n"
  "ds_read_b128 v[176:179], v1 offset:12288\n"
  "v_add_f32 v10, v10, v8\n"
  "v_exp_f32 v9, v63\n"
  "v_mfma_f32_32x32x16_bf16 v[80:95], a[244:247], a[180:183], v[80:95]\n"
  "ds_read_b128 v[180:183], v1 offset:13312\n"
  "v_add_f32 v11, v11, v9\n"
  "v_cvt_pk_bf16_f32 v111, v8, v9\n"
  "v_exp_f32 v2, v64\n"
  "v_mfma_f32_32x32x16_bf16 v[80:95], a[248:251], a[184:187], v[80:95]\n"
  "ds_read_b128 v[184:187], v1 offset:14336\n"
  "v_add_f32 v10, v10, v11\n"
  "v_exp_f32 v3, v65\n"
  "v_cmp_nge_f32 vcc, s67, v10\n"
  "v_mfma_f32_32x32x16_bf16 v[80:95], a[252:255], a[188:191], v[80:95]\n"
  "s_cbranch_vccnz FBD0_%=\n"
  "FBD0_%=_r:\n"
  "ds_read_b128 v[188:191], v1 offset:15360\n"
  "s_waitcnt lgkmcnt(8)\n"
  "v_cvt_pk_bf16_f32 v112, v2, v3\n"
  "v_exp_f32 v4, v66\n"
  "v_mfma_f32_32x32x16_bf16 a[0:15], v[128:131], v[96:99], a[0:15]\n"
  "ds_read_b128 a[192:195], v0 offset:0\n"
  "v_add_f32 v12, v2, v4\n"
  "v_exp_f32 v5, v67\n"
  "v_add_f32 v14, v14, v10\n"
  "v_mfma_f32_32x32x16_bf16 a[16:31], v[132:135], v[96:99], a[16:31]\n"
  "ds_read_b128 a[196:199], v0 offset:1024\n"
  "v_add_f32 v13, v3, v5\n"
  "v_cvt_pk_bf16_f32 v113, v4, v5\n"
  "v_exp_f32 v6, v68\n"
  "v_mfma_f32_32x32x16_bf16 a[32:47], v[136:139], v[96:99], a[32:47]\n"
  "ds_read_b128 a[200:203], v0 offset:2048\n"
  "v_add_f32 v12, v12, v6\n"
  "v_exp_f32 v7, v69\n"
  "v_pk_add_u16 v1, v1, s61\n"
  "v_mfma_f32_32x32x16_bf16 a[48:63], v[140:143], v[96:99], a[48:63]\n"
  "ds_read_b128 a[204:207], v0 offset:3072\n"
  "v_add_f32 v13, v13, v7\n"
  "v_cvt_pk_bf16_f32 v114, v6, v7\n"
  "v_exp_f32 v8, v70\n"
  "v_mfma_f32_32x32x16_bf16 a[0:15], v[144:147], v[100:103], a[0:15]\n"
  "ds_read_b128 a[208:211], v0 offset:4096\n"
  "v_add_f32 v12, v12, v8\n"
  "v_exp_f32 v9, v71\n"
  "v_mfma_f32_32x32x16_bf16 a[16:31], v[148:151], v[100:103], a[16:31]\n"
  "ds_read_b128 a[212:215], v0 offset:5120\n"
  "v_add_f32 v13, v13, v9\n"
  "v_cvt_pk_bf16_f32 v115, v8, v9\n"
  "v_exp_f32 v2, v72\n"
  "v_mfma_f32_32x32x16_bf16 a[32:47], v[152:155], v[100:103], a[32:47]\n"
  "ds_read_b128 a[216:219], v0 offset:6144\n"
  "v_add_f32 v12, v12, v2\n"
  "v_exp_f32 v3, v73\n"
  "v_mfma_f32_32x32x16_bf16 a[48:63], v[156:159], v[100:103], a[48:63]\n"
  "ds_read_b128 a[220:223], v0 offset:7168\n"
  "s_waitcnt lgkmcnt(8)\n"
  "v_add_f32 v13, v13, v3\n"
  "v_cvt_pk_bf16_f32 v116, v2, v3\n"
  "v_exp_f32 v4, v74\n"
  "v_mfma_f32_32x32x16_bf16 a[0:15], v[160:163], v[104:107], a[0:15]\n"
  "ds_read_b128 a[224:227], v0 offset:8192\n"
  "v_add_f32 v12, v12, v4\n"
  "v_exp_f32 v5, v75\n"
  "v_mfma_f32_32x32x16_bf16 a[16:31], v[164:167], v[104:107], a[16:31]\n"
  "ds_read_b128 a[228:231], v0 offset:9216\n"
  "v_add_f32 v13, v13, v5\n"
  "v_cvt_pk_bf16_f32 v117, v4, v5\n"
  "v_exp_f32 v6, v76\n"
  "v_mfma_f32_32x32x16_bf16 a[32:47], v[168:171], v[104:107], a[32:47]\n"
  "ds_read_b128 a[232:235], v0 offset:10240\n"
  "v_add_f32 v12, v12, v6\n"
  "v_exp_f32 v7, v77\n"
  "v_mfma_f32_32x32x16_bf16 a[48:63], v[172:175], v[104:107], a[48:63]\n"
  "ds_read_b128 a[236:239], v0 offset:11264\n"
  "v_add_f32 v13, v13, v7\n"
  "v_cvt_pk_bf16_f32 v118, v6, v7\n"
  "v_exp_f32 v8, v78\n"
  "v_mfma_f32_32x32x16_bf16 a[0:15], v[176:179], v[108:111], a[0:15]\n"
  "s_cmp_eq_u32 s60, s66\n"
  "ds_read_b128 a[240:243], v0 offset:12288\n"
  "s_cbranch_scc1 MD0_%=\n"
  "MD0_%=_r:\n"
  "v_add_f32 v12, v12, v8\n"
  "v_exp_f32 v9, v79\n"
  "v_mfma_f32_32x32x16_bf16 a[16:31], v[180:183], v[108:111], a[16:31]\n"
  "ds_read_b128 a[244:247], v0 offset:13312\n"
  "v_add_f32 v13, v13, v9\n"
  "v_cvt_pk_bf16_f32 v119, v8, v9\n"
  "v_exp_f32 v2, v80\n"
  "v_mfma_f32_32x32x16_bf16 a[32:47], v[184:187], v[108:111], a[32:47]\n"
  "ds_read_b128 a[248:251], v0 offset:14336\n"
  "v_add_f32 v12, v12, v2\n"
  "v_exp_f32 v3, v81\n"
  "v_mfma_f32_32x32x16_bf16 a[48:63], v[188:191], v[108:111], a[48:63]\n"
  "ds_read_b128 a[252:255], v0 offset:15360\n"
  "s_waitcnt lgkmcnt(8)\n"
  "v_add_f32 v13, v13, v3\n"
  "v_cvt_pk_bf16_f32 v120, v2, v3\n"
  "v_exp_f32 v4, v82\n"
  "v_mfma_f32_32x32x16_bf16 v[32:47], a[192:195], a[128:131], v[192:207]\n"
  "v_add_f32 v12, v12, v4\n"
  "v_exp_f32 v5, v83\n"
  "v_mfma_f32_32x32x16_bf16 v[32:47], a[196:199], a[132:135], v[32:47]\n"
  "s_pack_ll_b32_b16 m0, s60, 1\n"
  "v_add_f32 v13, v13, v5\n"
  "v_cvt_pk_bf16_f32 v121, v4, v5\n"
  "v_exp_f32 v6, v84\n"
  "v_mfma_f32_32x32x16_bf16 v[32:47], a[200:203], a[136:139], v[32:47]\n"
  "buffer_load_dwordx4 v20, s[24:27], s60 offen offset:0 sc1 lds\n"
  "v_add_f32 v12, v12, v6\n"
  "v_exp_f32 v7, v85\n"
  "v_mfma_f32_32x32x16_bf16 v[32:47], a[204:207], a[140:143], v[32:47]\n"
  "v_add_f32 v13, v13, v7\n"
  "v_cvt_pk_bf16_f32 v122, v6, v7\n"
  "v_exp_f32 v8, v86\n"
  "v_mfma_f32_32x32x16_bf16 v[32:47], a[208:211], a[144:147], v[32:47]\n"
  "v_add_f32 v12, v12, v8\n"
  "v_exp_f32 v9, v87\n"
  "v_mfma_f32_32x32x16_bf16 v[32:47], a[212:215], a[148:151], v[32:47]\n"
  "v_add_f32 v13, v13, v9\n"
  "v_cvt_pk_bf16_f32 v123, v8, v9\n"
  "v_exp_f32 v2, v88\n"
  "v_mfma_f32_32x32x16_bf16 v[32:47], a[216:219], a[152:155], v[32:47]\n"
  "buffer_load_dwordx4 v20, s[24:27], s60 offen offset:1024 sc1 lds\n"
  "v_add_f32 v12, v12, v2\n"
  "v_exp_f32 v3, v89\n"
  "v_mfma_f32_32x32x16_bf16 v[32:47], a[220:223], a[156:159], v[32:47]\n"
  "s_waitcnt lgkmcnt(0)\n"
  "v_add_f32 v13, v13, v3\n"
  "v_cvt_pk_bf16_f32 v124, v2, v3\n"
  "v_exp_f32 v4, v90\n"
  "v_mfma_f32_32x32x16_bf16 v[48:63], a[224:227], a[128:131], v[208:223]\n"
  "v_add_f32 v12, v12, v4\n"
  "v_exp_f32 v5, v91\n"
  "v_mfma_f32_32x32x16_bf16 v[48:63], a[228:231], a[132:135], v[48:63]\n"
  "v_add_f32 v13, v13, v5\n"
  "v_cvt_pk_bf16_f32 v125, v4, v5\n"
  "v_exp_f32 v6, v92\n"
  "v_mfma_f32_32x32x16_bf16 v[48:63], a[232:235], a[136:139], v[48:63]\n"
  "buffer_load_dwordx4 v20, s[24:27], s60 offen offset:2048 sc1 lds\n"
  "v_add_f32 v12, v12, v6\n"
  "v_exp_f32 v7, v93\n"
  "v_mfma_f32_32x32x16_bf16 v[48:63], a[236:239], a[140:143], v[48:63]\n"
  "v_add_f32 v13, v13, v7\n"
  "v_cvt_pk_bf16_f32 v126, v6, v7\n"
  "v_exp_f32 v8, v94\n"
  "v_mfma_f32_32x32x16_bf16 v[48:63], a[240:243], a[144:147], v[48:63]\n"
  "buffer_load_dwordx4 v20, s[24:27], s60 offen offset:3072 sc1 lds\n"
  "v_add_f32 v12, v12, v8\n"
  "v_exp_f32 v9, v95\n"
  "v_mfma_f32_32x32x16_bf16 v[48:63], a[244:247], a[148:151], v[48:63]\n"
  "v_add_f32 v13, v13, v9\n"
  "v_cvt_pk_bf16_f32 v127, v8, v9\n"
  "v_exp_f32 v2, v32\n"
  "v_mfma_f32_32x32x16_bf16 v[48:63], a[248:251], a[152:155], v[48:63]\n"
  "v_add_f32 v12, v12, v13\n"
  "v_exp_f32 v3, v33\n"
  "v_cmp_nge_f32 vcc, s67, v12\n"
  "v_mfma_f32_32x32x16_bf16 v[48:63], a[252:255], a[156:159], v[48:63]\n"
  "s_cbranch_vccnz FBD1_%=\n"
  "FBD1_%=_r:\n"
  "v_cvt_pk_bf16_f32 v96, v2, v3\n"
  "v_exp_f32 v4, v34\n"
  "v_mfma_f32_32x32x16_bf16 a[64:79], v[128:131], v[112:115], a[64:79]\n"
  "v_add_f32 v10, v2, v4\n"
  "v_exp_f32 v5, v35\n"
  "v_add_f32 v15, v15, v12\n"
  "v_mfma_f32_32x32x16_bf16 a[80:95], v[132:135], v[112:115], a[80:95]\n"
  "s_pack_ll_b32_b16 m0, s60, 0\n"
  "v_add_f32 v11, v3, v5\n"
  "v_cvt_pk_bf16_f32 v97, v4, v5\n"
  "v_exp_f32 v6, v36\n"
  "v_mfma_f32_32x32x16_bf16 a[96:111], v[136:139], v[112:115], a[96:111]\n"
  "buffer_load_dwordx4 v20, s[28:31], s60 offen offset:0 sc1 lds\n"
  "v_add_f32 v10, v10, v6\n"
  "v_exp_f32 v7, v37\n"
  "v_mfma_f32_32x32x16_bf16 a[112:127], v[140:143], v[112:115], a[112:127]\n"
  "v_add_f32 v11, v11, v7\n"
  "v_cvt_pk_bf16_f32 v98, v6, v7\n"
  "v_exp_f32 v8, v38\n"
  "v_mfma_f32_32x32x16_bf16 a[64:79], v[144:147], v[116:119], a[64:79]\n"
  "v_add_f32 v10, v10, v8\n"
  "v_exp_f32 v9, v39\n"
  "v_mfma_f32_32x32x16_bf16 a[80:95], v[148:151], v[116:119], a[80:95]\n"
  "v_add_f32 v11, v11, v9\n"
  "v_cvt_pk_bf16_f32 v99, v8, v9\n"
  "v_exp_f32 v2, v40\n"
  "v_mfma_f32_32x32x16_bf16 a[96:111], v[152:155], v[116:119], a[96:111]\n"
  "buffer_load_dwordx4 v20, s[28:31], s60 offen offset:1024 sc1 lds\n"
  "v_add_f32 v10, v10, v2\n"
  "v_exp_f32 v3, v41\n"
  "v_mfma_f32_32x32x16_bf16 a[112:127], v[156:159], v[116:119], a[112:127]\n"
  "v_add_f32 v11, v11, v3\n"
  "v_cvt_pk_bf16_f32 v100, v2, v3\n"
  "v_exp_f32 v4, v42\n"
  "v_mfma_f32_32x32x16_bf16 a[64:79], v[160:163], v[120:123], a[64:79]\n"
  "v_add_f32 v10, v10, v4\n"
  "v_exp_f32 v5, v43\n"
  "v_mfma_f32_32x32x16_bf16 a[80:95], v[164:167], v[120:123], a[80:95]\n"
  "v_add_f32 v11, v11, v5\n"
  "v_cvt_pk_bf16_f32 v101, v4, v5\n"
  "v_exp_f32 v6, v44\n"
  "v_mfma_f32_32x32x16_bf16 a[96:111], v[168:171], v[120:123], a[96:111]\n"
  "buffer_load_dwordx4 v20, s[28:31], s60 offen offset:2048 sc1 lds\n"
  "v_add_f32 v10, v10, v6\n"
  "v_exp_f32 v7, v45\n"
  "v_mfma_f32_32x32x16_bf16 a[112:127], v[172:175], v[120:123], a[112:127]\n"
  "v_add_f32 v11, v11, v7\n"
  "v_cvt_pk_bf16_f32 v102, v6, v7\n"
  "v_exp_f32 v8, v46\n"
  "v_mfma_f32_32x32x16_bf16 a[64:79], v[176:179], v[124:127], a[64:79]\n"
  "buffer_load_dwordx4 v20, s[28:31], s60 offen offset:3072 sc1 lds\n"
  "v_add_f32 v10, v10, v8\n"
  "v_exp_f32 v9, v47\n"
  "v_mfma_f32_32x32x16_bf16 a[80:95], v[180:183], v[124:127], a[80:95]\n"
  "s_add_u32 s60, s60, s61\n"
  "v_add_f32 v11, v11, v9\n"
  "v_cvt_pk_bf16_f32 v103, v8, v9\n"
  "v_exp_f32 v2, v48\n"
  "v_mfma_f32_32x32x16_bf16 a[96:111], v[184:187], v[124:127], a[96:111]\n"
  "s_cmp_ge_u32 s60, s65\n"
  "s_cbranch_scc1 MD1_%=\n"
  "MD1_%=_r:\n"
  "v_add_f32 v10, v10, v2\n"
  "v_exp_f32 v3, v49\n"
  "v_mfma_f32_32x32x16_bf16 a[112:127], v[188:191], v[124:127], a[112:127]\n"
  "s_cmp_lt_u32 s60, s63\n"
  "v_add_f32 v11, v11, v3\n"
  "v_cvt_pk_bf16_f32 v104, v2, v3\n"
  "v_exp_f32 v4, v50\n"
  "s_cbranch_scc1 LD_%=\n"
  "v_pk_add_u16 v0, v0, s61\n"
  "s_waitcnt vmcnt(12)\n"
  "s_barrier\n"
  "ds_read_b128 a[192:195], v0 offset:0\n"
  "ds_read_b128 a[196:199], v0 offset:1024\n"
  "ds_read_b128 a[200:203], v0 offset:2048\n"
  "ds_read_b128 a[204:207], v0 offset:3072\n"
  "ds_read_b128 a[208:211], v0 offset:4096\n"
  "ds_read_b128 a[212:215], v0 offset:5120\n"
  "ds_read_b128 a[216:219], v0 offset:6144\n"
  "ds_read_b128 a[220:223], v0 offset:7168\n"
  "ds_read_b128 a[224:227], v0 offset:8192\n"
  "ds_read_b128 a[228:231], v0 offset:9216\n"
  "ds_read_b128 a[232:235], v0 offset:10240\n"
  "ds_read_b128 a[236:239], v0 offset:11264\n"
  "ds_read_b128 a[240:243], v0 offset:12288\n"
  "ds_read_b128 a[244:247], v0 offset:13312\n"
  "ds_read_b128 a[248:251], v0 offset:14336\n"
  "ds_read_b128 a[252:255], v0 offset:15360\n"
  "v_lshrrev_b32 v24, 1, v20\n"
  "v_and_b32 v24, 0x78, v24\n"
  "buffer_load_dwordx2 v[18:19], v24, s[48:51], s23 offen sc1 \n"
  "v_mov_b32 v16, v15\n"
  "s_nop 1\n"
  "v_permlane32_swap_b32 v16, v15\n"
  "v_add_f32 v16, v16, v15\n"
  "v_rcp_f32 v16, v16\n"
  "v_accvgpr_read_b32 v224, a64\n"
  "v_accvgpr_read_b32 v225, a65\n"
  "v_accvgpr_read_b32 v226, a66\n"
  "v_accvgpr_read_b32 v227, a67\n"
  "v_accvgpr_read_b32 v228, a68\n"
  "v_accvgpr_read_b32 v229, a69\n"
  "v_accvgpr_read_b32 v230, a70\n"
  "v_accvgpr_read_b32 v231, a71\n"
  "v_pk_mul_f32 v[224:225], v[224:225], v[16:17] op_sel_hi:[1,0]\n"
  "v_pk_mul_f32 v[226:227], v[226:227], v[16:17] op_sel_hi:[1,0]\n"
  "v_pk_mul_f32 v[228:229], v[228:229], v[16:17] op_sel_hi:[1,0]\n"
  "v_pk_mul_f32 v[230:231], v[230:231], v[16:17] op_sel_hi:[1,0]\n"
  "v_cvt_pk_bf16_f32 v224, v224, v225\n"
  "v_cvt_pk_bf16_f32 v225, v226, v227\n"
  "v_cvt_pk_bf16_f32 v226, v228, v229\n"
  "v_cvt_pk_bf16_f32 v227, v230, v231\n"
  "buffer_store_dwordx4 v[224:227], v20, s[36:39], s20 offen offset:0 sc1 \n"
  "v_accvgpr_read_b32 v232, a72\n"
  "v_accvgpr_read_b32 v233, a73\n"
  "v_accvgpr_read_b32 v234, a74\n"
  "v_accvgpr_read_b32 v235, a75\n"
  "v_accvgpr_read_b32 v236, a76\n"
  "v_accvgpr_read_b32 v237, a77\n"
  "v_accvgpr_read_b32 v238, a78\n"
  "v_accvgpr_read_b32 v239, a79\n"
  "v_pk_mul_f32 v[232:233], v[232:233], v[16:17] op_sel_hi:[1,0]\n"
  "v_pk_mul_f32 v[234:235], v[234:235], v[16:17] op_sel_hi:[1,0]\n"
  "v_pk_mul_f32 v[236:237], v[236:237], v[16:17] op_sel_hi:[1,0]\n"
  "v_pk_mul_f32 v[238:239], v[238:239], v[16:17] op_sel_hi:[1,0]\n"
  "v_cvt_pk_bf16_f32 v232, v232, v233\n"
  "v_cvt_pk_bf16_f32 v233, v234, v235\n"
  "v_cvt_pk_bf16_f32 v234, v236, v237\n"
  "v_cvt_pk_bf16_f32 v235, v238, v239\n"
  "buffer_store_dwordx4 v[232:235], v20, s[36:39], s20 offen offset:1024 sc1 \n"
  "v_accvgpr_read_b32 v240, a80\n"
  "v_accvgpr_read_b32 v241, a81\n"
  "v_accvgpr_read_b32 v242, a82\n"
  "v_accvgpr_read_b32 v243, a83\n"
  "v_accvgpr_read_b32 v244, a84\n"
  "v_accvgpr_read_b32 v245, a85\n"
  "v_accvgpr_read_b32 v246, a86\n"
  "v_accvgpr_read_b32 v247, a87\n"
  "v_pk_mul_f32 v[240:241], v[240:241], v[16:17] op_sel_hi:[1,0]\n"
  "v_pk_mul_f32 v[242:243], v[242:243], v[16:17] op_sel_hi:[1,0]\n"
  "v_pk_mul_f32 v[244:245], v[244:245], v[16:17] op_sel_hi:[1,0]\n"
  "v_pk_mul_f32 v[246:247], v[246:247], v[16:17] op_sel_hi:[1,0]\n"
  "v_cvt_pk_bf16_f32 v240, v240, v241\n"
  "v_cvt_pk_bf16_f32 v241, v242, v243\n"
  "v_cvt_pk_bf16_f32 v242, v244, v245\n"
  "v_cvt_pk_bf16_f32 v243, v246, v247\n"
  "buffer_store_dwordx4 v[240:243], v20, s[36:39], s20 offen offset:2048 sc1 \n"
  "v_accvgpr_read_b32 v248, a88\n"
  "v_accvgpr_read_b32 v249, a89\n"
  "v_accvgpr_read_b32 v250, a90\n"
  "v_accvgpr_read_b32 v251, a91\n"
  "v_accvgpr_read_b32 v252, a92\n"
  "v_accvgpr_read_b32 v253, a93\n"
  "v_accvgpr_read_b32 v254, a94\n"
  "v_accvgpr_read_b32 v255, a95\n"
  "v_pk_mul_f32 v[248:249], v[248:249], v[16:17] op_sel_hi:[1,0]\n"
  "v_pk_mul_f32 v[250:251], v[250:251], v[16:17] op_sel_hi:[1,0]\n"
  "v_pk_mul_f32 v[252:253], v[252:253], v[16:17] op_sel_hi:[1,0]\n"
  "v_pk_mul_f32 v[254:255], v[254:255], v[16:17] op_sel_hi:[1,0]\n"
  "v_cvt_pk_bf16_f32 v248, v248, v249\n"
  "v_cvt_pk_bf16_f32 v249, v250, v251\n"
  "v_cvt_pk_bf16_f32 v250, v252, v253\n"
  "v_cvt_pk_bf16_f32 v251, v254, v255\n"
  "buffer_store_dwordx4 v[248:251], v20, s[36:39], s20 offen offset:3072 sc1 \n"
  "v_accvgpr_read_b32 v224, a96\n"
  "v_accvgpr_read_b32 v225, a97\n"
  "v_accvgpr_read_b32 v226, a98\n"
  "v_accvgpr_read_b32 v227, a99\n"
  "v_accvgpr_read_b32 v228, a100\n"
  "v_accvgpr_read_b32 v229, a101\n"
  "v_accvgpr_read_b32 v230, a102\n"
  "v_accvgpr_read_b32 v231, a103\n"
  "v_pk_mul_f32 v[224:225], v[224:225], v[16:17] op_sel_hi:[1,0]\n"
  "v_pk_mul_f32 v[226:227], v[226:227], v[16:17] op_sel_hi:[1,0]\n"
  "v_pk_mul_f32 v[228:229], v[228:229], v[16:17] op_sel_hi:[1,0]\n"
  "v_pk_mul_f32 v[230:231], v[230:231], v[16:17] op_sel_hi:[1,0]\n"
  "v_cvt_pk_bf16_f32 v224, v224, v225\n"
  "v_cvt_pk_bf16_f32 v225, v226, v227\n"
  "v_cvt_pk_bf16_f32 v226, v228, v229\n"
  "v_cvt_pk_bf16_f32 v227, v230, v231\n"
  "s_add_u32 s20, s20, 0x1000\n"
  "buffer_store_dwordx4 v[224:227], v20, s[36:39], s20 offen offset:0 sc1 \n"
  "v_accvgpr_read_b32 v232, a104\n"
  "v_accvgpr_read_b32 v233, a105\n"
  "v_accvgpr_read_b32 v234, a106\n"
  "v_accvgpr_read_b32 v235, a107\n"
  "v_accvgpr_read_b32 v236, a108\n"
  "v_accvgpr_read_b32 v237, a109\n"
  "v_accvgpr_read_b32 v238, a110\n"
  "v_accvgpr_read_b32 v239, a111\n"
  "v_pk_mul_f32 v[232:233], v[232:233], v[16:17] op_sel_hi:[1,0]\n"
  "v_pk_mul_f32 v[234:235], v[234:235], v[16:17] op_sel_hi:[1,0]\n"
  "v_pk_mul_f32 v[236:237], v[236:237], v[16:17] op_sel_hi:[1,0]\n"
  "v_pk_mul_f32 v[238:239], v[238:239], v[16:17] op_sel_hi:[1,0]\n"
  "v_cvt_pk_bf16_f32 v232, v232, v233\n"
  "v_cvt_pk_bf16_f32 v233, v234, v235\n"
  "v_cvt_pk_bf16_f32 v234, v236, v237\n"
  "v_cvt_pk_bf16_f32 v235, v238, v239\n"
  "buffer_store_dwordx4 v[232:235], v20, s[36:39], s20 offen offset:1024 sc1 \n"
  "v_accvgpr_read_b32 v240, a112\n"
  "v_accvgpr_read_b32 v241, a113\n"
  "v_accvgpr_read_b32 v242, a114\n"
  "v_accvgpr_read_b32 v243, a115\n"
  "v_accvgpr_read_b32 v244, a116\n"
  "v_accvgpr_read_b32 v245, a117\n"
  "v_accvgpr_read_b32 v246, a118\n"
  "v_accvgpr_read_b32 v247, a119\n"
  "v_pk_mul_f32 v[240:241], v[240:241], v[16:17] op_sel_hi:[1,0]\n"
  "v_pk_mul_f32 v[242:243], v[242:243], v[16:17] op_sel_hi:[1,0]\n"
  "v_pk_mul_f32 v[244:245], v[244:245], v[16:17] op_sel_hi:[1,0]\n"
  "v_pk_mul_f32 v[246:247], v[246:247], v[16:17] op_sel_hi:[1,0]\n"
  "v_cvt_pk_bf16_f32 v240, v240, v241\n"
  "v_cvt_pk_bf16_f32 v241, v242, v243\n"
  "v_cvt_pk_bf16_f32 v242, v244, v245\n"
  "v_cvt_pk_bf16_f32 v243, v246, v247\n"
  "buffer_store_dwordx4 v[240:243], v20, s[36:39], s20 offen offset:2048 sc1 \n"
  "v_accvgpr_read_b32 v248, a120\n"
  "v_accvgpr_read_b32 v249, a121\n"
  "v_accvgpr_read_b32 v250, a122\n"
  "v_accvgpr_read_b32 v251, a123\n"
  "v_accvgpr_read_b32 v252, a124\n"
  "v_accvgpr_read_b32 v253, a125\n"
  "v_accvgpr_read_b32 v254, a126\n"
  "v_accvgpr_read_b32 v255, a127\n"
  "v_pk_mul_f32 v[248:249], v[248:249], v[16:17] op_sel_hi:[1,0]\n"
  "v_pk_mul_f32 v[250:251], v[250:251], v[16:17] op_sel_hi:[1,0]\n"
  "v_pk_mul_f32 v[252:253], v[252:253], v[16:17] op_sel_hi:[1,0]\n"
  "v_pk_mul_f32 v[254:255], v[254:255], v[16:17] op_sel_hi:[1,0]\n"
  "v_cvt_pk_bf16_f32 v248, v248, v249\n"
  "v_cvt_pk_bf16_f32 v249, v250, v251\n"
  "v_cvt_pk_bf16_f32 v250, v252, v253\n"
  "v_cvt_pk_bf16_f32 v251, v254, v255\n"
  "buffer_store_dwordx4 v[248:251], v20, s[36:39], s20 offen offset:3072 sc1 \n"
  "s_waitcnt vmcnt(8)\n"
  "v_cmp_ne_u64 vcc, s[70:71], v[18:19]\n"
  "s_nop 0\n"
  "s_cbranch_vccnz PW_%=\n"
  "PW_%=_r:\n"
  "v_mov_b32 v24, -1\n"
  "v_mov_b32 v25, -1\n"
  "v_mov_b32 v26, -1\n"
  "v_mov_b32 v27, -1\n"
  "buffer_store_dwordx4 v[24:27], v20, s[36:39], s95 offen offset:0 sc1\n"
  "buffer_store_dwordx4 v[24:27], v20, s[36:39], s95 offen offset:1024 sc1\n"
  "buffer_store_dwordx4 v[24:27], v20, s[36:39], s95 offen offset:2048 sc1\n"
  "buffer_store_dwordx4 v[24:27], v20, s[36:39], s95 offen offset:3072 sc1\n"
  "s_add_u32 s95, s95, 0x1000\n"
  "buffer_store_dwordx4 v[24:27], v20, s[36:39], s95 offen offset:0 sc1\n"
  "buffer_store_dwordx4 v[24:27], v20, s[36:39], s95 offen offset:1024 sc1\n"
  "buffer_store_dwordx4 v[24:27], v20, s[36:39], s95 offen offset:2048 sc1\n"
  "buffer_store_dwordx4 v[24:27], v20, s[36:39], s95 offen offset:3072 sc1\n"
  "s_add_u32 s95, s95, 0x1000\n"
  "buffer_store_dwordx4 v[24:27], v20, s[36:39], s95 offen offset:0 sc1\n"
  "s_cmp_eq_u32 s60, s62\n"
  "s_cbranch_scc1 MT_%=\n"
  "MT_%=_r:\n"
  "s_waitcnt lgkmcnt(8)\n"
  ".p2align 6\n"
  "LS_%=:\n"
  "v_mfma_f32_32x32x16_bf16 v[64:79], a[192:195], a[128:131], v[192:207]\n"
  "s_waitcnt vmcnt(12)\n"
  "s_barrier\n"
  "ds_read_b128 v[128:131], v1 offset:0\n"
  "v_add_f32 v10, v10, v4\n"
  "v_exp_f32 v5, v51\n"
  "v_mfma_f32_32x32x16_bf16 v[64:79], a[196:199], a[132:135], v[64:79]\n"
  "s_pack_ll_b32_b16 m0, s60, 1\n"
  "ds_read_b128 v[132:135], v1 offset:1024\n"
  "v_add_f32 v11, v11, v5\n"
  "v_cvt_pk_bf16_f32 v105, v4, v5\n"
  "v_exp_f32 v6, v52\n"
  "v_mfma_f32_32x32x16_bf16 v[64:79], a[200:203], a[136:139], v[64:79]\n"
  "buffer_load_dwordx4 v20, s[24:27], s60 offen offset:0 sc1 lds\n"
  "v_add_f32 v10, v10, v6\n"
  "v_exp_f32 v7, v53\n"
  "v_mfma_f32_32x32x16_bf16 v[64:79], a[204:207], a[140:143], v[64:79]\n"
  "ds_read_b128 v[136:139], v1 offset:2048\n"
  "ds_read_b128 v[140:143], v1 offset:3072\n"
  "v_add_f32 v11, v11, v7\n"
  "v_cvt_pk_bf16_f32 v106, v6, v7\n"
  "v_exp_f32 v8, v54\n"
  "v_mfma_f32_32x32x16_bf16 v[64:79], a[208:211], a[144:147], v[64:79]\n"
  "buffer_load_dwordx4 v20, s[24:27], s60 offen offset:1024 sc1 lds\n"
  "v_add_f32 v10, v10, v8\n"
  "v_exp_f32 v9, v55\n"
  "v_mfma_f32_32x32x16_bf16 v[64:79], a[212:215], a[148:151], v[64:79]\n"
  "ds_read_b128 v[144:147], v1 offset:4096\n"
  "ds_read_b128 v[148:151], v1 offset:5120\n"
  "v_add_f32 v11, v11, v9\n"
  "v_cvt_pk_bf16_f32 v107, v8, v9\n"
  "v_exp_f32 v2, v56\n"
  "v_mfma_f32_32x32x16_bf16 v[64:79], a[216:219], a[152:155], v[64:79]\n"
  "ds_read_b128 v[152:155], v1 offset:6144\n"
  "v_add_f32 v10, v10, v2\n"
  "v_exp_f32 v3, v57\n"
  "v_mfma_f32_32x32x16_bf16 v[64:79], a[220:223], a[156:159], v[64:79]\n"
  "ds_read_b128 v[156:159], v1 offset:7168\n"
  "s_waitcnt lgkmcnt(8)\n"
  "v_add_f32 v11, v11, v3\n"
  "v_cvt_pk_bf16_f32 v108, v2, v3\n"
  "v_exp_f32 v4, v58\n"
  "v_mfma_f32_32x32x16_bf16 v[80:95], a[224:227], a[128:131], v[208:223]\n"
  "buffer_load_dwordx4 v20, s[24:27], s60 offen offset:2048 sc1 lds\n"
  "v_add_f32 v10, v10, v4\n"
  "v_exp_f32 v5, v59\n"
  "v_pk_add_u16 v0, v0, s61\n"
  "v_mfma_f32_32x32x16_bf16 v[80:95], a[228:231], a[132:135], v[80:95]\n"
  "ds_read_b128 v[160:163], v1 offset:8192\n"
  "ds_read_b128 v[164:167], v1 offset:9216\n"
  "v_add_f32 v11, v11, v5\n"
  "v_cvt_pk_bf16_f32 v109, v4, v5\n"
  "v_exp_f32 v6, v60\n"
  "v_mfma_f32_32x32x16_bf16 v[80:95], a[232:235], a[136:139], v[80:95]\n"
  "buffer_load_dwordx4 v20, s[24:27], s60 offen offset:3072 sc1 lds\n"
  "v_add_f32 v10, v10, v6\n"
  "v_exp_f32 v7, v61\n"
  "v_mfma_f32_32x32x16_bf16 v[80:95], a[236:239], a[140:143], v[80:95]\n"
  "ds_read_b128 v[168:171], v1 offset:10240\n"
  "ds_read_b128 v[172:175], v1 offset:11264\n"
  "v_add_f32 v11, v11, v7\n"
  "v_cvt_pk_bf16_f32 v110, v6, v7\n"
  "v_exp_f32 v8, v62\n"
  "v_mfma_f32_32x32x16_bf16 v[80:95], a[240:243], a[144:147], v[80:95]\n"
  "ds_read_b128 v[176:179], v1 offset:12288\n"
  "v_add_f32 v10, v10, v8\n"
  "v_exp_f32 v9, v63\n"
  "v_mfma_f32_32x32x16_bf16 v[80:95], a[244:247], a[148:151], v[80:95]\n"
  "ds_read_b128 v[180:183], v1 offset:13312\n"
  "v_add_f32 v11, v11, v9\n"
  "v_cvt_pk_bf16_f32 v111, v8, v9\n"
  "v_exp_f32 v2, v64\n"
  "v_mfma_f32_32x32x16_bf16 v[80:95], a[248:251], a[152:155], v[80:95]\n"
  "ds_read_b128 v[184:187], v1 offset:14336\n"
  "v_add_f32 v10, v10, v11\n"
  "v_exp_f32 v3, v65\n"
  "v_cmp_nge_f32 vcc, s67, v10\n"
  "v_mfma_f32_32x32x16_bf16 v[80:95], a[252:255], a[156:159], v[80:95]\n"
  "s_cbranch_vccnz FBS0_%=\n"
  "FBS0_%=_r:\n"
  "ds_read_b128 v[188:191], v1 offset:15360\n"
  "s_waitcnt lgkmcnt(8)\n"
  "v_cvt_pk_bf16_f32 v112, v2, v3\n"
  "v_exp_f32 v4, v66\n"
  "v_mfma_f32_32x32x16_bf16 a[0:15], v[128:131], v[96:99], a[0:15]\n"
  "ds_read_b128 a[192:195], v0 offset:0\n"
  "v_add_f32 v12, v2, v4\n"
  "v_exp_f32 v5, v67\n"
  "v_add_f32 v14, v14, v10\n"
  "v_mfma_f32_32x32x16_bf16 a[16:31], v[132:135], v[96:99], a[16:31]\n"
  "s_pack_ll_b32_b16 m0, s60, 0\n"
  "ds_read_b128 a[196:199], v0 offset:1024\n"
  "v_add_f32 v13, v3, v5\n"
  "v_cvt_pk_bf16_f32 v113, v4, v5\n"
  "v_exp_f32 v6, v68\n"
  "v_mfma_f32_32x32x16_bf16 a[32:47], v[136:139], v[96:99], a[32:47]\n"
  "buffer_load_dwordx4 v20, s[28:31], s60 offen offset:0 sc1 lds\n"
  "v_add_f32 v12, v12, v6\n"
  "v_exp_f32 v7, v69\n"
  "v_mfma_f32_32x32x16_bf16 a[48:63], v[140:143], v[96:99], a[48:63]\n"
  "ds_read_b128 a[200:203], v0 offset:2048\n"
  "ds_read_b128 a[204:207], v0 offset:3072\n"
  "v_add_f32 v13, v13, v7\n"
  "v_cvt_pk_bf16_f32 v114, v6, v7\n"
  "v_exp_f32 v8, v70\n"
  "v_mfma_f32_32x32x16_bf16 a[0:15], v[144:147], v[100:103], a[0:15]\n"
  "buffer_load_dwordx4 v20, s[28:31], s60 offen offset:1024 sc1 lds\n"
  "v_add_f32 v12, v12, v8\n"
  "v_exp_f32 v9, v71\n"
  "v_mfma_f32_32x32x16_bf16 a[16:31], v[148:151], v[100:103], a[16:31]\n"
  "ds_read_b128 a[208:211], v0 offset:4096\n"
  "ds_read_b128 a[212:215], v0 offset:5120\n"
  "v_add_f32 v13, v13, v9\n"
  "v_cvt_pk_bf16_f32 v115, v8, v9\n"
  "v_exp_f32 v2, v72\n"
  "v_mfma_f32_32x32x16_bf16 a[32:47], v[152:155], v[100:103], a[32:47]\n"
  "ds_read_b128 a[216:219], v0 offset:6144\n"
  "v_add_f32 v12, v12, v2\n"
  "v_exp_f32 v3, v73\n"
  "v_mfma_f32_32x32x16_bf16 a[48:63], v[156:159], v[100:103], a[48:63]\n"
  "ds_read_b128 a[220:223], v0 offset:7168\n"
  "s_waitcnt lgkmcnt(8)\n"
  "v_add_f32 v13, v13, v3\n"
  "v_cvt_pk_bf16_f32 v116, v2, v3\n"
  "v_exp_f32 v4, v74\n"
  "v_mfma_f32_32x32x16_bf16 a[0:15], v[160:163], v[104:107], a[0:15]\n"
  "buffer_load_dwordx4 v20, s[28:31], s60 offen offset:2048 sc1 lds\n"
  "v_add_f32 v12, v12, v4\n"
  "v_exp_f32 v5, v75\n"
  "v_mfma_f32_32x32x16_bf16 a[16:31], v[164:167], v[104:107], a[16:31]\n"
  "ds_read_b128 a[224:227], v0 offset:8192\n"
  "ds_read_b128 a[228:231], v0 offset:9216\n"
  "v_add_f32 v13, v13, v5\n"
  "v_cvt_pk_bf16_f32 v117, v4, v5\n"
  "v_exp_f32 v6, v76\n"
  "v_mfma_f32_32x32x16_bf16 a[32:47], v[168:171], v[104:107], a[32:47]\n"
  "buffer_load_dwordx4 v20, s[28:31], s60 offen offset:3072 sc1 lds\n"
  "v_add_f32 v12, v12, v6\n"
  "v_exp_f32 v7, v77\n"
  "v_pk_add_u16 v1, v1, s61\n"
  "v_mfma_f32_32x32x16_bf16 a[48:63], v[172:175], v[104:107], a[48:63]\n"
  "ds_read_b128 a[232:235], v0 offset:10240\n"
  "ds_read_b128 a[236:239], v0 offset:11264\n"
  "v_add_f32 v13, v13, v7\n"
  "v_cvt_pk_bf16_f32 v118, v6, v7\n"
  "v_exp_f32 v8, v78\n"
  "v_mfma_f32_32x32x16_bf16 a[0:15], v[176:179], v[108:111], a[0:15]\n"
  "s_add_u32 s60, s60, s61\n"
  "ds_read_b128 a[240:243], v0 offset:12288\n"
  "v_add_f32 v12, v12, v8\n"
  "v_exp_f32 v9, v79\n"
  "v_mfma_f32_32x32x16_bf16 a[16:31], v[180:183], v[108:111], a[16:31]\n"
  "ds_read_b128 a[244:247], v0 offset:13312\n"
  "v_add_f32 v13, v13, v9\n"
  "v_cvt_pk_bf16_f32 v119, v8, v9\n"
  "v_exp_f32 v2, v80\n"
  "v_mfma_f32_32x32x16_bf16 a[32:47], v[184:187], v[108:111], a[32:47]\n"
  "s_cmp_eq_u32 s60, s66\n"
  "ds_read_b128 a[248:251], v0 offset:14336\n"
  "s_cbranch_scc1 MS0_%=\n"
  "MS0_%=_r:\n"
  "v_add_f32 v12, v12, v2\n"
  "v_exp_f32 v3, v81\n"
  "v_mfma_f32_32x32x16_bf16 a[48:63], v[188:191], v[108:111], a[48:63]\n"
  "ds_read_b128 a[252:255], v0 offset:15360\n"
  "s_waitcnt lgkmcnt(8)\n"
  "v_add_f32 v13, v13, v3\n"
  "v_cvt_pk_bf16_f32 v120, v2, v3\n"
  "v_exp_f32 v4, v82\n"
  "v_mfma_f32_32x32x16_bf16 v[32:47], a[192:195], a[128:131], v[192:207]\n"
  "s_waitcnt vmcnt(12)\n"
  "s_barrier\n"
  "ds_read_b128 v[128:131], v1 offset:0\n"
  "v_add_f32 v12, v12, v4\n"
  "v_exp_f32 v5, v83\n"
  "v_mfma_f32_32x32x16_bf16 v[32:47], a[196:199], a[132:135], v[32:47]\n"
  "s_pack_ll_b32_b16 m0, s60, 1\n"
  "ds_read_b128 v[132:135], v1 offset:1024\n"
  "v_add_f32 v13, v13, v5\n"
  "v_cvt_pk_bf16_f32 v121, v4, v5\n"
  "v_exp_f32 v6, v84\n"
  "v_mfma_f32_32x32x16_bf16 v[32:47], a[200:203], a[136:139], v[32:47]\n"
  "buffer_load_dwordx4 v20, s[24:27], s60 offen offset:0 sc1 lds\n"
  "v_add_f32 v12, v12, v6\n"
  "v_exp_f32 v7, v85\n"
  "v_mfma_f32_32x32x16_bf16 v[32:47], a[204:207], a[140:143], v[32:47]\n"
  "ds_read_b128 v[136:139], v1 offset:2048\n"
  "ds_read_b128 v[140:143], v1 offset:3072\n"
  "v_add_f32 v13, v13, v7\n"
  "v_cvt_pk_bf16_f32 v122, v6, v7\n"
  "v_exp_f32 v8, v86\n"
  "v_mfma_f32_32x32x16_bf16 v[32:47], a[208:211], a[144:147], v[32:47]\n"
  "buffer_load_dwordx4 v20, s[24:27], s60 offen offset:1024 sc1 lds\n"
  "v_add_f32 v12, v12, v8\n"
  "v_exp_f32 v9, v87\n"
  "v_mfma_f32_32x32x16_bf16 v[32:47], a[212:215], a[148:151], v[32:47]\n"
  "ds_read_b128 v[144:147], v1 offset:4096\n"
  "ds_read_b128 v[148:151], v1 offset:5120\n"
  "v_add_f32 v13, v13, v9\n"
  "v_cvt_pk_bf16_f32 v123, v8, v9\n"
  "v_exp_f32 v2, v88\n"
  "v_mfma_f32_32x32x16_bf16 v[32:47], a[216:219], a[152:155], v[32:47]\n"
  "ds_read_b128 v[152:155], v1 offset:6144\n"
  "v_add_f32 v12, v12, v2\n"
  "v_exp_f32 v3, v89\n"
  "v_mfma_f32_32x32x16_bf16 v[32:47], a[220:223], a[156:159], v[32:47]\n"
  "ds_read_b128 v[156:159], v1 offset:7168\n"
  "s_waitcnt lgkmcnt(8)\n"
  "v_add_f32 v13, v13, v3\n"
  "v_cvt_pk_bf16_f32 v124, v2, v3\n"
  "v_exp_f32 v4, v90\n"
  "v_mfma_f32_32x32x16_bf16 v[48:63], a[224:227], a[128:131], v[208:223]\n"
  "buffer_load_dwordx4 v20, s[24:27], s60 offen offset:2048 sc1 lds\n"
  "v_add_f32 v12, v12, v4\n"
  "v_exp_f32 v5, v91\n"
  "v_pk_add_u16 v0, v0, s61\n"
  "v_mfma_f32_32x32x16_bf16 v[48:63], a[228:231], a[132:135], v[48:63]\n"
  "ds_read_b128 v[160:163], v1 offset:8192\n"
  "ds_read_b128 v[164:167], v1 offset:9216\n"
  "v_add_f32 v13, v13, v5\n"
  "v_cvt_pk_bf16_f32 v125, v4, v5\n"
  "v_exp_f32 v6, v92\n"
  "v_mfma_f32_32x32x16_bf16 v[48:63], a[232:235], a[136:139], v[48:63]\n"
  "buffer_load_dwordx4 v20, s[24:27], s60 offen offset:3072 sc1 lds\n"
  "v_add_f32 v12, v12, v6\n"
  "v_exp_f32 v7, v93\n"
  "v_mfma_f32_32x32x16_bf16 v[48:63], a[236:239], a[140:143], v[48:63]\n"
  "ds_read_b128 v[168:171], v1 offset:10240\n"
  "ds_read_b128 v[172:175], v1 offset:11264\n"
  "v_add_f32 v13, v13, v7\n"
  "v_cvt_pk_bf16_f32 v126, v6, v7\n"
  "v_exp_f32 v8, v94\n"
  "v_mfma_f32_32x32x16_bf16 v[48:63], a[240:243], a[144:147], v[48:63]\n"
  "ds_read_b128 v[176:179], v1 offset:12288\n"
  "v_add_f32 v12, v12, v8\n"
  "v_exp_f32 v9, v95\n"
  "v_mfma_f32_32x32x16_bf16 v[48:63], a[244:247], a[148:151], v[48:63]\n"
  "ds_read_b128 v[180:183], v1 offset:13312\n"
  "v_add_f32 v13, v13, v9\n"
  "v_cvt_pk_bf16_f32 v127, v8, v9\n"
  "v_exp_f32 v2, v32\n"
  "v_mfma_f32_32x32x16_bf16 v[48:63], a[248:251], a[152:155], v[48:63]\n"
  "ds_read_b128 v[184:187], v1 offset:14336\n"
  "v_add_f32 v12, v12, v13\n"
  "v_exp_f32 v3, v33\n"
  "v_cmp_nge_f32 vcc, s67, v12\n"
  "v_mfma_f32_32x32x16_bf16 v[48:63], a[252:255], a[156:159], v[48:63]\n"
  "s_cbranch_vccnz FBS1_%=\n"
  "FBS1_%=_r:\n"
  "ds_read_b128 v[188:191], v1 offset:15360\n"
  "s_waitcnt lgkmcnt(8)\n"
  "v_cvt_pk_bf16_f32 v96, v2, v3\n"
  "v_exp_f32 v4, v34\n"
  "v_mfma_f32_32x32x16_bf16 a[0:15], v[128:131], v[112:115], a[0:15]\n"
  "ds_read_b128 a[192:195], v0 offset:0\n"
  "v_add_f32 v10, v2, v4\n"
  "v_exp_f32 v5, v35\n"
  "v_add_f32 v14, v14, v12\n"
  "v_mfma_f32_32x32x16_bf16 a[16:31], v[132:135], v[112:115], a[16:31]\n"
  "s_pack_ll_b32_b16 m0, s60, 0\n"
  "ds_read_b128 a[196:199], v0 offset:1024\n"
  "v_add_f32 v11, v3, v5\n"
  "v_cvt_pk_bf16_f32 v97, v4, v5\n"
  "v_exp_f32 v6, v36\n"
  "v_mfma_f32_32x32x16_bf16 a[32:47], v[136:139], v[112:115], a[32:47]\n"
  "buffer_load_dwordx4 v20, s[28:31], s60 offen offset:0 sc1 lds\n"
  "v_add_f32 v10, v10, v6\n"
  "v_exp_f32 v7, v37\n"
  "v_mfma_f32_32x32x16_bf16 a[48:63], v[140:143], v[112:115], a[48:63]\n"
  "ds_read_b128 a[200:203], v0 offset:2048\n"
  "ds_read_b128 a[204:207], v0 offset:3072\n"
  "v_add_f32 v11, v11, v7\n"
  "v_cvt_pk_bf16_f32 v98, v6, v7\n"
  "v_exp_f32 v8, v38\n"
  "v_mfma_f32_32x32x16_bf16 a[0:15], v[144:147], v[116:119], a[0:15]\n"
  "buffer_load_dwordx4 v20, s[28:31], s60 offen offset:1024 sc1 lds\n"
  "v_add_f32 v10, v10, v8\n"
  "v_exp_f32 v9, v39\n"
  "v_mfma_f32_32x32x16_bf16 a[16:31], v[148:151], v[116:119], a[16:31]\n"
  "ds_read_b128 a[208:211], v0 offset:4096\n"
  "ds_read_b128 a[212:215], v0 offset:5120\n"
  "v_add_f32 v11, v11, v9\n"
  "v_cvt_pk_bf16_f32 v99, v8, v9\n"
  "v_exp_f32 v2, v40\n"
  "v_mfma_f32_32x32x16_bf16 a[32:47], v[152:155], v[116:119], a[32:47]\n"
  "ds_read_b128 a[216:219], v0 offset:6144\n"
  "v_add_f32 v10, v10, v2\n"
  "v_exp_f32 v3, v41\n"
  "v_mfma_f32_32x32x16_bf16 a[48:63], v[156:159], v[116:119], a[48:63]\n"
  "ds_read_b128 a[220:223], v0 offset:7168\n"
  "s_waitcnt lgkmcnt(8)\n"
  "v_add_f32 v11, v11, v3\n"
  "v_cvt_pk_bf16_f32 v100, v2, v3\n"
  "v_exp_f32 v4, v42\n"
  "v_mfma_f32_32x32x16_bf16 a[0:15], v[160:163], v[120:123], a[0:15]\n"
  "buffer_load_dwordx4 v20, s[28:31], s60 offen offset:2048 sc1 lds\n"
  "v_add_f32 v10, v10, v4\n"
  "v_exp_f32 v5, v43\n"
  "v_mfma_f32_32x32x16_bf16 a[16:31], v[164:167], v[120:123], a[16:31]\n"
  "ds_read_b128 a[224:227], v0 offset:8192\n"
  "ds_read_b128 a[228:231], v0 offset:9216\n"
  "v_add_f32 v11, v11, v5\n"
  "v_cvt_pk_bf16_f32 v101, v4, v5\n"
  "v_exp_f32 v6, v44\n"
  "v_mfma_f32_32x32x16_bf16 a[32:47], v[168:171], v[120:123], a[32:47]\n"
  "buffer_load_dwordx4 v20, s[28:31], s60 offen offset:3072 sc1 lds\n"
  "v_add_f32 v10, v10, v6\n"
  "v_exp_f32 v7, v45\n"
  "v_pk_add_u16 v1, v1, s61\n"
  "v_mfma_f32_32x32x16_bf16 a[48:63], v[172:175], v[120:123], a[48:63]\n"
  "ds_read_b128 a[232:235], v0 offset:10240\n"
  "ds_read_b128 a[236:239], v0 offset:11264\n"
  "v_add_f32 v11, v11, v7\n"
  "v_cvt_pk_bf16_f32 v102, v6, v7\n"
  "v_exp_f32 v8, v46\n"
  "v_mfma_f32_32x32x16_bf16 a[0:15], v[176:179], v[124:127], a[0:15]\n"
  "s_add_u32 s60, s60, s61\n"
  "ds_read_b128 a[240:243], v0 offset:12288\n"
  "s_waitcnt lgkmcnt(5)\n"
  "v_add_f32 v10, v10, v8\n"
  "v_exp_f32 v9, v47\n"
  "v_mfma_f32_32x32x16_bf16 a[16:31], v[180:183], v[124:127], a[16:31]\n"
  "ds_read_b128 a[244:247], v0 offset:13312\n"
  "v_add_f32 v11, v11, v9\n"
  "v_cvt_pk_bf16_f32 v103, v8, v9\n"
  "v_exp_f32 v2, v48\n"
  "v_mfma_f32_32x32x16_bf16 a[32:47], v[184:187], v[124:127], a[32:47]\n"
  "s_cmp_eq_u32 s60, s62\n"
  "ds_read_b128 a[248:251], v0 offset:14336\n"
  "s_cbranch_scc1 MS1_%=\n"
  "MS1_%=_r:\n"
  "v_add_f32 v10, v10, v2\n"
  "v_exp_f32 v3, v49\n"
  "v_mfma_f32_32x32x16_bf16 a[48:63], v[188:191], v[124:127], a[48:63]\n"
  "s_cmp_lt_u32 s60, s64\n"
  "ds_read_b128 a[252:255], v0 offset:15360\n"
  "v_add_f32 v11, v11, v3\n"
  "v_cvt_pk_bf16_f32 v104, v2, v3\n"
  "v_exp_f32 v4, v50\n"
  "s_cbranch_scc1 LS_%=\n"
  "s_waitcnt vmcnt(0) lgkmcnt(0)\n"
  "s_barrier\n"
  "s_nop 7\n"
  "s_branch LEND_%=\n"
  "PKV_%=:\n"
  "s_mov_b32 s57, 0x20000\n"
  "PKV_%=_l:\n"
  "s_sleep 2\n"
  "buffer_load_dwordx2 v[16:17], v31, s[48:51], s73 offen sc1 \n"
  "s_waitcnt vmcnt(0)\n"
  "v_cmp_ne_u64 vcc, s[70:71], v[16:17]\n"
  "s_sub_u32 s57, s57, 1\n"
  "s_cbranch_scc1 PKV_%=_r\n"
  "s_cbranch_vccnz PKV_%=_l\n"
  "s_branch PKV_%=_r\n"
  "MP_%=:\n"
  "s_nop 7\n"
  "s_andn2_b32 s55, s60, 0x3fff\n"
  "s_lshr_b32 s55, s55, 8\n"
  "s_add_i32 s55, s55, -256\n"
  "v_subrev_u32 v16, s55, v22\n"
  "v_mov_b32 v17, 0xff800000\n"
  "v_cmp_gt_i32 s[40:41], 0, v16\n"
  "v_cmp_gt_i32 s[42:43], 1, v16\n"
  "v_cmp_gt_i32 s[44:45], 2, v16\n"
  "v_cmp_gt_i32 s[46:47], 3, v16\n"
  "v_cndmask_b32 v224, v224, v17, s[40:41]\n"
  "v_cndmask_b32 v225, v225, v17, s[42:43]\n"
  "v_cndmask_b32 v226, v226, v17, s[44:45]\n"
  "v_cndmask_b32 v227, v227, v17, s[46:47]\n"
  "v_cmp_gt_i32 s[40:41], 8, v16\n"
  "v_cmp_gt_i32 s[42:43], 9, v16\n"
  "v_cmp_gt_i32 s[44:45], 10, v16\n"
  "v_cmp_gt_i32 s[46:47], 11, v16\n"
  "v_cndmask_b32 v228, v228, v17, s[40:41]\n"
  "v_cndmask_b32 v229, v229, v17, s[42:43]\n"
  "v_cndmask_b32 v230, v230, v17, s[44:45]\n"
  "v_cndmask_b32 v231, v231, v17, s[46:47]\n"
  "v_cmp_gt_i32 s[40:41], 16, v16\n"
  "v_cmp_gt_i32 s[42:43], 17, v16\n"
  "v_cmp_gt_i32 s[44:45], 18, v16\n"
  "v_cmp_gt_i32 s[46:47], 19, v16\n"
  "v_cndmask_b32 v232, v232, v17, s[40:41]\n"
  "v_cndmask_b32 v233, v233, v17, s[42:43]\n"
  "v_cndmask_b32 v234, v234, v17, s[44:45]\n"
  "v_cndmask_b32 v235, v235, v17, s[46:47]\n"
  "v_cmp_gt_i32 s[40:41], 24, v16\n"
  "v_cmp_gt_i32 s[42:43], 25, v16\n"
  "v_cmp_gt_i32 s[44:45], 26, v16\n"
  "v_cmp_gt_i32 s[46:47], 27, v16\n"
  "v_cndmask_b32 v236, v236, v17, s[40:41]\n"
  "v_cndmask_b32 v237, v237, v17, s[42:43]\n"
  "v_cndmask_b32 v238, v238, v17, s[44:45]\n"
  "v_cndmask_b32 v239, v239, v17, s[46:47]\n"
  "v_cmp_gt_i32 s[40:41], 32, v16\n"
  "v_cmp_gt_i32 s[42:43], 33, v16\n"
  "v_cmp_gt_i32 s[44:45], 34, v16\n"
  "v_cmp_gt_i32 s[46:47], 35, v16\n"
  "v_cndmask_b32 v240, v240, v17, s[40:41]\n"
  "v_cndmask_b32 v241, v241, v17, s[42:43]\n"
  "v_cndmask_b32 v242, v242, v17, s[44:45]\n"
  "v_cndmask_b32 v243, v243, v17, s[46:47]\n"
  "v_cmp_gt_i32 s[40:41], 40, v16\n"
  "v_cmp_gt_i32 s[42:43], 41, v16\n"
  "v_cmp_gt_i32 s[44:45], 42, v16\n"
  "v_cmp_gt_i32 s[46:47], 43, v16\n"
  "v_cndmask_b32 v244, v244, v17, s[40:41]\n"
  "v_cndmask_b32 v245, v245, v17, s[42:43]\n"
  "v_cndmask_b32 v246, v246, v17, s[44:45]\n"
  "v_cndmask_b32 v247, v247, v17, s[46:47]\n"
  "v_cmp_gt_i32 s[40:41], 48, v16\n"
  "v_cmp_gt_i32 s[42:43], 49, v16\n"
  "v_cmp_gt_i32 s[44:45], 50, v16\n"
  "v_cmp_gt_i32 s[46:47], 51, v16\n"
  "v_cndmask_b32 v248, v248, v17, s[40:41]\n"
  "v_cndmask_b32 v249, v249, v17, s[42:43]\n"
  "v_cndmask_b32 v250, v250, v17, s[44:45]\n"
  "v_cndmask_b32 v251, v251, v17, s[46:47]\n"
  "v_cmp_gt_i32 s[40:41], 56, v16\n"
  "v_cmp_gt_i32 s[42:43], 57, v16\n"
  "v_cmp_gt_i32 s[44:45], 58, v16\n"
  "v_cmp_gt_i32 s[46:47], 59, v16\n"
  "v_cndmask_b32 v252, v252, v17, s[40:41]\n"
  "v_cndmask_b32 v253, v253, v17, s[42:43]\n"
  "v_cndmask_b32 v254, v254, v17, s[44:45]\n"
  "v_cndmask_b32 v255, v255, v17, s[46:47]\n"
  "s_nop 1\n"
  "s_branch MP_%=_r\n"
  "FBD0_%=:\n"
  "s_nop 15\n"
  "v_max3_f32 v16, v32, v33, v34\n"
  "v_max3_f32 v17, v35, v36, v37\n"
  "v_max3_f32 v16, v16, v38, v39\n"
  "v_max3_f32 v17, v17, v40, v41\n"
  "v_max3_f32 v16, v16, v42, v43\n"
  "v_max3_f32 v17, v17, v44, v45\n"
  "v_max3_f32 v16, v16, v46, v47\n"
  "v_max3_f32 v17, v17, v48, v49\n"
  "v_max3_f32 v16, v16, v50, v51\n"
  "v_max3_f32 v17, v17, v52, v53\n"
  "v_max3_f32 v16, v16, v54, v55\n"
  "v_max3_f32 v17, v17, v56, v57\n"
  "v_max3_f32 v16, v16, v58, v59\n"
  "v_max3_f32 v17, v17, v60, v61\n"
  "v_max3_f32 v16, v16, v62, v63\n"
  "v_max_f32 v16, v16, v17\n"
  "v_mov_b32 v17, v16\n"
  "s_nop 1\n"
  "v_permlane32_swap_b32 v16, v17\n"
  "v_max_f32 v16, v16, v17\n"
  "v_max_f32 v16, 0, v16\n"
  "v_exp_f32 v17, -v16\n"
  "v_sub_f32 v32, v32, v16\n"
  "v_sub_f32 v33, v33, v16\n"
  "v_sub_f32 v34, v34, v16\n"
  "v_sub_f32 v35, v35, v16\n"
  "v_sub_f32 v36, v36, v16\n"
  "v_sub_f32 v37, v37, v16\n"
  "v_sub_f32 v38, v38, v16\n"
  "v_sub_f32 v39, v39, v16\n"
  "v_sub_f32 v40, v40, v16\n"
  "v_sub_f32 v41, v41, v16\n"
  "v_sub_f32 v42, v42, v16\n"
  "v_sub_f32 v43, v43, v16\n"
  "v_sub_f32 v44, v44, v16\n"
  "v_sub_f32 v45, v45, v16\n"
  "v_sub_f32 v46, v46, v16\n"
  "v_sub_f32 v47, v47, v16\n"
  "v_sub_f32 v48, v48, v16\n"
  "v_sub_f32 v49, v49, v16\n"
  "v_sub_f32 v50, v50, v16\n"
  "v_sub_f32 v51, v51, v16\n"
  "v_sub_f32 v52, v52, v16\n"
  "v_sub_f32 v53, v53, v16\n"
  "v_sub_f32 v54, v54, v16\n"
  "v_sub_f32 v55, v55, v16\n"
  "v_sub_f32 v56, v56, v16\n"
  "v_sub_f32 v57, v57, v16\n"
  "v_sub_f32 v58, v58, v16\n"
  "v_sub_f32 v59, v59, v16\n"
  "v_sub_f32 v60, v60, v16\n"
  "v_sub_f32 v61, v61, v16\n"
  "v_sub_f32 v62, v62, v16\n"
  "v_sub_f32 v63, v63, v16\n"
  "v_sub_f32 v192, v192, v16\n"
  "v_sub_f32 v193, v193, v16\n"
  "v_sub_f32 v194, v194, v16\n"
  "v_sub_f32 v195, v195, v16\n"
  "v_sub_f32 v196, v196, v16\n"
  "v_sub_f32 v197, v197, v16\n"
  "v_sub_f32 v198, v198, v16\n"
  "v_sub_f32 v199, v199, v16\n"
  "v_sub_f32 v200, v200, v16\n"
  "v_sub_f32 v201, v201, v16\n"
  "v_sub_f32 v202, v202, v16\n"
  "v_sub_f32 v203, v203, v16\n"
  "v_sub_f32 v204, v204, v16\n"
  "v_sub_f32 v205, v205, v16\n"
  "v_sub_f32 v206, v206, v16\n"
  "v_sub_f32 v207, v207, v16\n"
  "v_sub_f32 v208, v208, v16\n"
  "v_sub_f32 v209, v209, v16\n"
  "v_sub_f32 v210, v210, v16\n"
  "v_sub_f32 v211, v211, v16\n"
  "v_sub_f32 v212, v212, v16\n"
  "v_sub_f32 v213, v213, v16\n"
  "v_sub_f32 v214, v214, v16\n"
  "v_sub_f32 v215, v215, v16\n"
  "v_sub_f32 v216, v216, v16\n"
  "v_sub_f32 v217, v217, v16\n"
  "v_sub_f32 v218, v218, v16\n"
  "v_sub_f32 v219, v219, v16\n"
  "v_sub_f32 v220, v220, v16\n"
  "v_sub_f32 v221, v221, v16\n"
  "v_sub_f32 v222, v222, v16\n"
  "v_sub_f32 v223, v223, v16\n"
  "v_mul_f32 v14, v14, v17\n"
  "v_accvgpr_read_b32 v24, a0\n"
  "v_accvgpr_read_b32 v25, a1\n"
  "v_accvgpr_read_b32 v26, a2\n"
  "v_accvgpr_read_b32 v27, a3\n"
  "v_accvgpr_read_b32 v28, a4\n"
  "v_accvgpr_read_b32 v29, a5\n"
  "v_accvgpr_read_b32 v30, a6\n"
  "v_accvgpr_read_b32 v31, a7\n"
  "v_mul_f32 v24, v24, v17\n"
  "v_mul_f32 v25, v25, v17\n"
  "v_mul_f32 v26, v26, v17\n"
  "v_mul_f32 v27, v27, v17\n"
  "v_mul_f32 v28, v28, v17\n"
  "v_mul_f32 v29, v29, v17\n"
  "v_mul_f32 v30, v30, v17\n"
  "v_mul_f32 v31, v31, v17\n"
  "v_accvgpr_write_b32 a0, v24\n"
  "v_accvgpr_write_b32 a1, v25\n"
  "v_accvgpr_write_b32 a2, v26\n"
  "v_accvgpr_write_b32 a3, v27\n"
  "v_accvgpr_write_b32 a4, v28\n"
  "v_accvgpr_write_b32 a5, v29\n"
  "v_accvgpr_write_b32 a6, v30\n"
  "v_accvgpr_write_b32 a7, v31\n"
  "v_accvgpr_read_b32 v24, a8\n"
  "v_accvgpr_read_b32 v25, a9\n"
  "v_accvgpr_read_b32 v26, a10\n"
  "v_accvgpr_read_b32 v27, a11\n"
  "v_accvgpr_read_b32 v28, a12\n"
  "v_accvgpr_read_b32 v29, a13\n"
  "v_accvgpr_read_b32 v30, a14\n"
  "v_accvgpr_read_b32 v31, a15\n"
  "v_mul_f32 v24, v24, v17\n"
  "v_mul_f32 v25, v25, v17\n"
  "v_mul_f32 v26, v26, v17\n"
  "v_mul_f32 v27, v27, v17\n"
  "v_mul_f32 v28, v28, v17\n"
  "v_mul_f32 v29, v29, v17\n"
  "v_mul_f32 v30, v30, v17\n"
  "v_mul_f32 v31, v31, v17\n"
  "v_accvgpr_write_b32 a8, v24\n"
  "v_accvgpr_write_b32 a9, v25\n"
  "v_accvgpr_write_b32 a10, v26\n"
  "v_accvgpr_write_b32 a11, v27\n"
  "v_accvgpr_write_b32 a12, v28\n"
  "v_accvgpr_write_b32 a13, v29\n"
  "v_accvgpr_write_b32 a14, v30\n"
  "v_accvgpr_write_b32 a15, v31\n"
  "v_accvgpr_read_b32 v24, a16\n"
  "v_accvgpr_read_b32 v25, a17\n"
  "v_accvgpr_read_b32 v26, a18\n"
  "v_accvgpr_read_b32 v27, a19\n"
  "v_accvgpr_read_b32 v28, a20\n"
  "v_accvgpr_read_b32 v29, a21\n"
  "v_accvgpr_read_b32 v30, a22\n"
  "v_accvgpr_read_b32 v31, a23\n"
  "v_mul_f32 v24, v24, v17\n"
  "v_mul_f32 v25, v25, v17\n"
  "v_mul_f32 v26, v26, v17\n"
  "v_mul_f32 v27, v27, v17\n"
  "v_mul_f32 v28, v28, v17\n"
  "v_mul_f32 v29, v29, v17\n"
  "v_mul_f32 v30, v30, v17\n"
  "v_mul_f32 v31, v31, v17\n"
  "v_accvgpr_write_b32 a16, v24\n"
  "v_accvgpr_write_b32 a17, v25\n"
  "v_accvgpr_write_b32 a18, v26\n"
  "v_accvgpr_write_b32 a19, v27\n"
  "v_accvgpr_write_b32 a20, v28\n"
  "v_accvgpr_write_b32 a21, v29\n"
  "v_accvgpr_write_b32 a22, v30\n"
  "v_accvgpr_write_b32 a23, v31\n"
  "v_accvgpr_read_b32 v24, a24\n"
  "v_accvgpr_read_b32 v25, a25\n"
  "v_accvgpr_read_b32 v26, a26\n"
  "v_accvgpr_read_b32 v27, a27\n"
  "v_accvgpr_read_b32 v28, a28\n"
  "v_accvgpr_read_b32 v29, a29\n"
  "v_accvgpr_read_b32 v30, a30\n"
  "v_accvgpr_read_b32 v31, a31\n"
  "v_mul_f32 v24, v24, v17\n"
  "v_mul_f32 v25, v25, v17\n"
  "v_mul_f32 v26, v26, v17\n"
  "v_mul_f32 v27, v27, v17\n"
  "v_mul_f32 v28, v28, v17\n"
  "v_mul_f32 v29, v29, v17\n"
  "v_mul_f32 v30, v30, v17\n"
  "v_mul_f32 v31, v31, v17\n"
  "v_accvgpr_write_b32 a24, v24\n"
  "v_accvgpr_write_b32 a25, v25\n"
  "v_accvgpr_write_b32 a26, v26\n"
  "v_accvgpr_write_b32 a27, v27\n"
  "v_accvgpr_write_b32 a28, v28\n"
  "v_accvgpr_write_b32 a29, v29\n"
  "v_accvgpr_write_b32 a30, v30\n"
  "v_accvgpr_write_b32 a31, v31\n"
  "v_accvgpr_read_b32 v24, a32\n"
  "v_accvgpr_read_b32 v25, a33\n"
  "v_accvgpr_read_b32 v26, a34\n"
  "v_accvgpr_read_b32 v27, a35\n"
  "v_accvgpr_read_b32 v28, a36\n"
  "v_accvgpr_read_b32 v29, a37\n"
  "v_accvgpr_read_b32 v30, a38\n"
  "v_accvgpr_read_b32 v31, a39\n"
  "v_mul_f32 v24, v24, v17\n"
  "v_mul_f32 v25, v25, v17\n"
  "v_mul_f32 v26, v26, v17\n"
  "v_mul_f32 v27, v27, v17\n"
  "v_mul_f32 v28, v28, v17\n"
  "v_mul_f32 v29, v29, v17\n"
  "v_mul_f32 v30, v30, v17\n"
  "v_mul_f32 v31, v31, v17\n"
  "v_accvgpr_write_b32 a32, v24\n"
  "v_accvgpr_write_b32 a33, v25\n"
  "v_accvgpr_write_b32 a34, v26\n"
  "v_accvgpr_write_b32 a35, v27\n"
  "v_accvgpr_write_b32 a36, v28\n"
  "v_accvgpr_write_b32 a37, v29\n"
  "v_accvgpr_write_b32 a38, v30\n"
  "v_accvgpr_write_b32 a39, v31\n"
  "v_accvgpr_read_b32 v24, a40\n"
  "v_accvgpr_read_b32 v25, a41\n"
  "v_accvgpr_read_b32 v26, a42\n"
  "v_accvgpr_read_b32 v27, a43\n"
  "v_accvgpr_read_b32 v28, a44\n"
  "v_accvgpr_read_b32 v29, a45\n"
  "v_accvgpr_read_b32 v30, a46\n"
  "v_accvgpr_read_b32 v31, a47\n"
  "v_mul_f32 v24, v24, v17\n"
  "v_mul_f32 v25, v25, v17\n"
  "v_mul_f32 v26, v26, v17\n"
  "v_mul_f32 v27, v27, v17\n"
  "v_mul_f32 v28, v28, v17\n"
  "v_mul_f32 v29, v29, v17\n"
  "v_mul_f32 v30, v30, v17\n"
  "v_mul_f32 v31, v31, v17\n"
  "v_accvgpr_write_b32 a40, v24\n"
  "v_accvgpr_write_b32 a41, v25\n"
  "v_accvgpr_write_b32 a42, v26\n"
  "v_accvgpr_write_b32 a43, v27\n"
  "v_accvgpr_write_b32 a44, v28\n"
  "v_accvgpr_write_b32 a45, v29\n"
  "v_accvgpr_write_b32 a46, v30\n"
  "v_accvgpr_write_b32 a47, v31\n"
  "v_accvgpr_read_b32 v24, a48\n"
  "v_accvgpr_read_b32 v25, a49\n"
  "v_accvgpr_read_b32 v26, a50\n"
  "v_accvgpr_read_b32 v27, a51\n"
  "v_accvgpr_read_b32 v28, a52\n"
  "v_accvgpr_read_b32 v29, a53\n"
  "v_accvgpr_read_b32 v30, a54\n"
  "v_accvgpr_read_b32 v31, a55\n"
  "v_mul_f32 v24, v24, v17\n"
  "v_mul_f32 v25, v25, v17\n"
  "v_mul_f32 v26, v26, v17\n"
  "v_mul_f32 v27, v27, v17\n"
  "v_mul_f32 v28, v28, v17\n"
  "v_mul_f32 v29, v29, v17\n"
  "v_mul_f32 v30, v30, v17\n"
  "v_mul_f32 v31, v31, v17\n"
  "v_accvgpr_write_b32 a48, v24\n"
  "v_accvgpr_write_b32 a49, v25\n"
  "v_accvgpr_write_b32 a50, v26\n"
  "v_accvgpr_write_b32 a51, v27\n"
  "v_accvgpr_write_b32 a52, v28\n"
  "v_accvgpr_write_b32 a53, v29\n"
  "v_accvgpr_write_b32 a54, v30\n"
  "v_accvgpr_write_b32 a55, v31\n"
  "v_accvgpr_read_b32 v24, a56\n"
  "v_accvgpr_read_b32 v25, a57\n"
  "v_accvgpr_read_b32 v26, a58\n"
  "v_accvgpr_read_b32 v27, a59\n"
  "v_accvgpr_read_b32 v28, a60\n"
  "v_accvgpr_read_b32 v29, a61\n"
  "v_accvgpr_read_b32 v30, a62\n"
  "v_accvgpr_read_b32 v31, a63\n"
  "v_mul_f32 v24, v24, v17\n"
  "v_mul_f32 v25, v25, v17\n"
  "v_mul_f32 v26, v26, v17\n"
  "v_mul_f32 v27, v27, v17\n"
  "v_mul_f32 v28, v28, v17\n"
  "v_mul_f32 v29, v29, v17\n"
  "v_mul_f32 v30, v30, v17\n"
  "v_mul_f32 v31, v31, v17\n"
  "v_accvgpr_write_b32 a56, v24\n"
  "v_accvgpr_write_b32 a57, v25\n"
  "v_accvgpr_write_b32 a58, v26\n"
  "v_accvgpr_write_b32 a59, v27\n"
  "v_accvgpr_write_b32 a60, v28\n"
  "v_accvgpr_write_b32 a61, v29\n"
  "v_accvgpr_write_b32 a62, v30\n"
  "v_accvgpr_write_b32 a63, v31\n"
  "v_exp_f32 v2, v32\n"
  "v_exp_f32 v3, v33\n"
  "s_nop 0\n"
  "v_cvt_pk_bf16_f32 v96, v2, v3\n"
  "v_exp_f32 v4, v34\n"
  "s_nop 0\n"
  "v_add_f32 v10, v2, v4\n"
  "v_exp_f32 v5, v35\n"
  "s_nop 0\n"
  "v_add_f32 v11, v3, v5\n"
  "v_cvt_pk_bf16_f32 v97, v4, v5\n"
  "v_exp_f32 v6, v36\n"
  "s_nop 0\n"
  "v_add_f32 v10, v10, v6\n"
  "v_exp_f32 v7, v37\n"
  "s_nop 0\n"
  "v_add_f32 v11, v11, v7\n"
  "v_cvt_pk_bf16_f32 v98, v6, v7\n"
  "v_exp_f32 v8, v38\n"
  "s_nop 0\n"
  "v_add_f32 v10, v10, v8\n"
  "v_exp_f32 v9, v39\n"
  "s_nop 0\n"
  "v_add_f32 v11, v11, v9\n"
  "v_cvt_pk_bf16_f32 v99, v8, v9\n"
  "v_exp_f32 v2, v40\n"
  "s_nop 0\n"
  "v_add_f32 v10, v10, v2\n"
  "v_exp_f32 v3, v41\n"
  "s_nop 0\n"
  "v_add_f32 v11, v11, v3\n"
  "v_cvt_pk_bf16_f32 v100, v2, v3\n"
  "v_exp_f32 v4, v42\n"
  "s_nop 0\n"
  "v_add_f32 v10, v10, v4\n"
  "v_exp_f32 v5, v43\n"
  "s_nop 0\n"
  "v_add_f32 v11, v11, v5\n"
  "v_cvt_pk_bf16_f32 v101, v4, v5\n"
  "v_exp_f32 v6, v44\n"
  "s_nop 0\n"
  "v_add_f32 v10, v10, v6\n"
  "v_exp_f32 v7, v45\n"
  "s_nop 0\n"
  "v_add_f32 v11, v11, v7\n"
  "v_cvt_pk_bf16_f32 v102, v6, v7\n"
  "v_exp_f32 v8, v46\n"
  "s_nop 0\n"
  "v_add_f32 v10, v10, v8\n"
  "v_exp_f32 v9, v47\n"
  "s_nop 0\n"
  "v_add_f32 v11, v11, v9\n"
  "v_cvt_pk_bf16_f32 v103, v8, v9\n"
  "v_exp_f32 v2, v48\n"
  "s_nop 0\n"
  "v_add_f32 v10, v10, v2\n"
  "v_exp_f32 v3, v49\n"
  "s_nop 0\n"
  "v_add_f32 v11, v11, v3\n"
  "v_cvt_pk_bf16_f32 v104, v2, v3\n"
  "v_exp_f32 v4, v50\n"
  "s_nop 0\n"
  "v_add_f32 v10, v10, v4\n"
  "v_exp_f32 v5, v51\n"
  "s_nop 0\n"
  "v_add_f32 v11, v11, v5\n"
  "v_cvt_pk_bf16_f32 v105, v4, v5\n"
  "v_exp_f32 v6, v52\n"
  "s_nop 0\n"
  "v_add_f32 v10, v10, v6\n"
  "v_exp_f32 v7, v53\n"
  "s_nop 0\n"
  "v_add_f32 v11, v11, v7\n"
  "v_cvt_pk_bf16_f32 v106, v6, v7\n"
  "v_exp_f32 v8, v54\n"
  "s_nop 0\n"
  "v_add_f32 v10, v10, v8\n"
  "v_exp_f32 v9, v55\n"
  "s_nop 0\n"
  "v_add_f32 v11, v11, v9\n"
  "v_cvt_pk_bf16_f32 v107, v8, v9\n"
  "v_exp_f32 v2, v56\n"
  "s_nop 0\n"
  "v_add_f32 v10, v10, v2\n"
  "v_exp_f32 v3, v57\n"
  "s_nop 0\n"
  "v_add_f32 v11, v11, v3\n"
  "v_cvt_pk_bf16_f32 v108, v2, v3\n"
  "v_exp_f32 v4, v58\n"
  "s_nop 0\n"
  "v_add_f32 v10, v10, v4\n"
  "v_exp_f32 v5, v59\n"
  "s_nop 0\n"
  "v_add_f32 v11, v11, v5\n"
  "v_cvt_pk_bf16_f32 v109, v4, v5\n"
  "v_exp_f32 v6, v60\n"
  "s_nop 0\n"
  "v_add_f32 v10, v10, v6\n"
  "v_exp_f32 v7, v61\n"
  "s_nop 0\n"
  "v_add_f32 v11, v11, v7\n"
  "v_cvt_pk_bf16_f32 v110, v6, v7\n"
  "v_exp_f32 v8, v62\n"
  "s_nop 0\n"
  "v_add_f32 v10, v10, v8\n"
  "v_exp_f32 v9, v63\n"
  "s_nop 0\n"
  "v_add_f32 v11, v11, v9\n"
  "v_cvt_pk_bf16_f32 v111, v8, v9\n"
  "v_add_f32 v10, v10, v11\n"
  "v_exp_f32 v2, v64\n"
  "v_exp_f32 v3, v65\n"
  "s_nop 3\n"
  "s_branch FBD0_%=_r\n"
  "FBD1_%=:\n"
  "s_nop 15\n"
  "v_max3_f32 v16, v64, v65, v66\n"
  "v_max3_f32 v17, v67, v68, v69\n"
  "v_max3_f32 v16, v16, v70, v71\n"
  "v_max3_f32 v17, v17, v72, v73\n"
  "v_max3_f32 v16, v16, v74, v75\n"
  "v_max3_f32 v17, v17, v76, v77\n"
  "v_max3_f32 v16, v16, v78, v79\n"
  "v_max3_f32 v17, v17, v80, v81\n"
  "v_max3_f32 v16, v16, v82, v83\n"
  "v_max3_f32 v17, v17, v84, v85\n"
  "v_max3_f32 v16, v16, v86, v87\n"
  "v_max3_f32 v17, v17, v88, v89\n"
  "v_max3_f32 v16, v16, v90, v91\n"
  "v_max3_f32 v17, v17, v92, v93\n"
  "v_max3_f32 v16, v16, v94, v95\n"
  "v_max_f32 v16, v16, v17\n"
  "v_mov_b32 v17, v16\n"
  "s_nop 1\n"
  "v_permlane32_swap_b32 v16, v17\n"
  "v_max_f32 v16, v16, v17\n"
  "v_max_f32 v16, 0, v16\n"
  "v_exp_f32 v17, -v16\n"
  "v_sub_f32 v64, v64, v16\n"
  "v_sub_f32 v65, v65, v16\n"
  "v_sub_f32 v66, v66, v16\n"
  "v_sub_f32 v67, v67, v16\n"
  "v_sub_f32 v68, v68, v16\n"
  "v_sub_f32 v69, v69, v16\n"
  "v_sub_f32 v70, v70, v16\n"
  "v_sub_f32 v71, v71, v16\n"
  "v_sub_f32 v72, v72, v16\n"
  "v_sub_f32 v73, v73, v16\n"
  "v_sub_f32 v74, v74, v16\n"
  "v_sub_f32 v75, v75, v16\n"
  "v_sub_f32 v76, v76, v16\n"
  "v_sub_f32 v77, v77, v16\n"
  "v_sub_f32 v78, v78, v16\n"
  "v_sub_f32 v79, v79, v16\n"
  "v_sub_f32 v80, v80, v16\n"
  "v_sub_f32 v81, v81, v16\n"
  "v_sub_f32 v82, v82, v16\n"
  "v_sub_f32 v83, v83, v16\n"
  "v_sub_f32 v84, v84, v16\n"
  "v_sub_f32 v85, v85, v16\n"
  "v_sub_f32 v86, v86, v16\n"
  "v_sub_f32 v87, v87, v16\n"
  "v_sub_f32 v88, v88, v16\n"
  "v_sub_f32 v89, v89, v16\n"
  "v_sub_f32 v90, v90, v16\n"
  "v_sub_f32 v91, v91, v16\n"
  "v_sub_f32 v92, v92, v16\n"
  "v_sub_f32 v93, v93, v16\n"
  "v_sub_f32 v94, v94, v16\n"
  "v_sub_f32 v95, v95, v16\n"
  "v_sub_f32 v224, v224, v16\n"
  "v_sub_f32 v225, v225, v16\n"
  "v_sub_f32 v226, v226, v16\n"
  "v_sub_f32 v227, v227, v16\n"
  "v_sub_f32 v228, v228, v16\n"
  "v_sub_f32 v229, v229, v16\n"
  "v_sub_f32 v230, v230, v16\n"
  "v_sub_f32 v231, v231, v16\n"
  "v_sub_f32 v232, v232, v16\n"
  "v_sub_f32 v233, v233, v16\n"
  "v_sub_f32 v234, v234, v16\n"
  "v_sub_f32 v235, v235, v16\n"
  "v_sub_f32 v236, v236, v16\n"
  "v_sub_f32 v237, v237, v16\n"
  "v_sub_f32 v238, v238, v16\n"
  "v_sub_f32 v239, v239, v16\n"
  "v_sub_f32 v240, v240, v16\n"
  "v_sub_f32 v241, v241, v16\n"
  "v_sub_f32 v242, v242, v16\n"
  "v_sub_f32 v243, v243, v16\n"
  "v_sub_f32 v244, v244, v16\n"
  "v_sub_f32 v245, v245, v16\n"
  "v_sub_f32 v246, v246, v16\n"
  "v_sub_f32 v247, v247, v16\n"
  "v_sub_f32 v248, v248, v16\n"
  "v_sub_f32 v249, v249, v16\n"
  "v_sub_f32 v250, v250, v16\n"
  "v_sub_f32 v251, v251, v16\n"
  "v_sub_f32 v252, v252, v16\n"
  "v_sub_f32 v253, v253, v16\n"
  "v_sub_f32 v254, v254, v16\n"
  "v_sub_f32 v255, v255, v16\n"
  "v_mul_f32 v15, v15, v17\n"
  "v_accvgpr_read_b32 v24, a64\n"
  "v_accvgpr_read_b32 v25, a65\n"
  "v_accvgpr_read_b32 v26, a66\n"
  "v_accvgpr_read_b32 v27, a67\n"
  "v_accvgpr_read_b32 v28, a68\n"
  "v_accvgpr_read_b32 v29, a69\n"
  "v_accvgpr_read_b32 v30, a70\n"
  "v_accvgpr_read_b32 v31, a71\n"
  "v_mul_f32 v24, v24, v17\n"
  "v_mul_f32 v25, v25, v17\n"
  "v_mul_f32 v26, v26, v17\n"
  "v_mul_f32 v27, v27, v17\n"
  "v_mul_f32 v28, v28, v17\n"
  "v_mul_f32 v29, v29, v17\n"
  "v_mul_f32 v30, v30, v17\n"
  "v_mul_f32 v31, v31, v17\n"
  "v_accvgpr_write_b32 a64, v24\n"
  "v_accvgpr_write_b32 a65, v25\n"
  "v_accvgpr_write_b32 a66, v26\n"
  "v_accvgpr_write_b32 a67, v27\n"
  "v_accvgpr_write_b32 a68, v28\n"
  "v_accvgpr_write_b32 a69, v29\n"
  "v_accvgpr_write_b32 a70, v30\n"
  "v_accvgpr_write_b32 a71, v31\n"
  "v_accvgpr_read_b32 v24, a72\n"
  "v_accvgpr_read_b32 v25, a73\n"
  "v_accvgpr_read_b32 v26, a74\n"
  "v_accvgpr_read_b32 v27, a75\n"
  "v_accvgpr_read_b32 v28, a76\n"
  "v_accvgpr_read_b32 v29, a77\n"
  "v_accvgpr_read_b32 v30, a78\n"
  "v_accvgpr_read_b32 v31, a79\n"
  "v_mul_f32 v24, v24, v17\n"
  "v_mul_f32 v25, v25, v17\n"
  "v_mul_f32 v26, v26, v17\n"
  "v_mul_f32 v27, v27, v17\n"
  "v_mul_f32 v28, v28, v17\n"
  "v_mul_f32 v29, v29, v17\n"
  "v_mul_f32 v30, v30, v17\n"
  "v_mul_f32 v31, v31, v17\n"
  "v_accvgpr_write_b32 a72, v24\n"
  "v_accvgpr_write_b32 a73, v25\n"
  "v_accvgpr_write_b32 a74, v26\n"
  "v_accvgpr_write_b32 a75, v27\n"
  "v_accvgpr_write_b32 a76, v28\n"
  "v_accvgpr_write_b32 a77, v29\n"
  "v_accvgpr_write_b32 a78, v30\n"
  "v_accvgpr_write_b32 a79, v31\n"
  "v_accvgpr_read_b32 v24, a80\n"
  "v_accvgpr_read_b32 v25, a81\n"
  "v_accvgpr_read_b32 v26, a82\n"
  "v_accvgpr_read_b32 v27, a83\n"
  "v_accvgpr_read_b32 v28, a84\n"
  "v_accvgpr_read_b32 v29, a85\n"
  "v_accvgpr_read_b32 v30, a86\n"
  "v_accvgpr_read_b32 v31, a87\n"
  "v_mul_f32 v24, v24, v17\n"
  "v_mul_f32 v25, v25, v17\n"
  "v_mul_f32 v26, v26, v17\n"
  "v_mul_f32 v27, v27, v17\n"
  "v_mul_f32 v28, v28, v17\n"
  "v_mul_f32 v29, v29, v17\n"
  "v_mul_f32 v30, v30, v17\n"
  "v_mul_f32 v31, v31, v17\n"
  "v_accvgpr_write_b32 a80, v24\n"
  "v_accvgpr_write_b32 a81, v25\n"
  "v_accvgpr_write_b32 a82, v26\n"
  "v_accvgpr_write_b32 a83, v27\n"
  "v_accvgpr_write_b32 a84, v28\n"
  "v_accvgpr_write_b32 a85, v29\n"
  "v_accvgpr_write_b32 a86, v30\n"
  "v_accvgpr_write_b32 a87, v31\n"
  "v_accvgpr_read_b32 v24, a88\n"
  "v_accvgpr_read_b32 v25, a89\n"
  "v_accvgpr_read_b32 v26, a90\n"
  "v_accvgpr_read_b32 v27, a91\n"
  "v_accvgpr_read_b32 v28, a92\n"
  "v_accvgpr_read_b32 v29, a93\n"
  "v_accvgpr_read_b32 v30, a94\n"
  "v_accvgpr_read_b32 v31, a95\n"
  "v_mul_f32 v24, v24, v17\n"
  "v_mul_f32 v25, v25, v17\n"
  "v_mul_f32 v26, v26, v17\n"
  "v_mul_f32 v27, v27, v17\n"
  "v_mul_f32 v28, v28, v17\n"
  "v_mul_f32 v29, v29, v17\n"
  "v_mul_f32 v30, v30, v17\n"
  "v_mul_f32 v31, v31, v17\n"
  "v_accvgpr_write_b32 a88, v24\n"
  "v_accvgpr_write_b32 a89, v25\n"
  "v_accvgpr_write_b32 a90, v26\n"
  "v_accvgpr_write_b32 a91, v27\n"
  "v_accvgpr_write_b32 a92, v28\n"
  "v_accvgpr_write_b32 a93, v29\n"
  "v_accvgpr_write_b32 a94, v30\n"
  "v_accvgpr_write_b32 a95, v31\n"
  "v_accvgpr_read_b32 v24, a96\n"
  "v_accvgpr_read_b32 v25, a97\n"
  "v_accvgpr_read_b32 v26, a98\n"
  "v_accvgpr_read_b32 v27, a99\n"
  "v_accvgpr_read_b32 v28, a100\n"
  "v_accvgpr_read_b32 v29, a101\n"
  "v_accvgpr_read_b32 v30, a102\n"
  "v_accvgpr_read_b32 v31, a103\n"
  "v_mul_f32 v24, v24, v17\n"
  "v_mul_f32 v25, v25, v17\n"
  "v_mul_f32 v26, v26, v17\n"
  "v_mul_f32 v27, v27, v17\n"
  "v_mul_f32 v28, v28, v17\n"
  "v_mul_f32 v29, v29, v17\n"
  "v_mul_f32 v30, v30, v17\n"
  "v_mul_f32 v31, v31, v17\n"
  "v_accvgpr_write_b32 a96, v24\n"
  "v_accvgpr_write_b32 a97, v25\n"
  "v_accvgpr_write_b32 a98, v26\n"
  "v_accvgpr_write_b32 a99, v27\n"
  "v_accvgpr_write_b32 a100, v28\n"
  "v_accvgpr_write_b32 a101, v29\n"
  "v_accvgpr_write_b32 a102, v30\n"
  "v_accvgpr_write_b32 a103, v31\n"
  "v_accvgpr_read_b32 v24, a104\n"
  "v_accvgpr_read_b32 v25, a105\n"
  "v_accvgpr_read_b32 v26, a106\n"
  "v_accvgpr_read_b32 v27, a107\n"
  "v_accvgpr_read_b32 v28, a108\n"
  "v_accvgpr_read_b32 v29, a109\n"
  "v_accvgpr_read_b32 v30, a110\n"
  "v_accvgpr_read_b32 v31, a111\n"
  "v_mul_f32 v24, v24, v17\n"
  "v_mul_f32 v25, v25, v17\n"
  "v_mul_f32 v26, v26, v17\n"
  "v_mul_f32 v27, v27, v17\n"
  "v_mul_f32 v28, v28, v17\n"
  "v_mul_f32 v29, v29, v17\n"
  "v_mul_f32 v30, v30, v17\n"
  "v_mul_f32 v31, v31, v17\n"
  "v_accvgpr_write_b32 a104, v24\n"
  "v_accvgpr_write_b32 a105, v25\n"
  "v_accvgpr_write_b32 a106, v26\n"
  "v_accvgpr_write_b32 a107, v27\n"
  "v_accvgpr_write_b32 a108, v28\n"
  "v_accvgpr_write_b32 a109, v29\n"
  "v_accvgpr_write_b32 a110, v30\n"
  "v_accvgpr_write_b32 a111, v31\n"
  "v_accvgpr_read_b32 v24, a112\n"
  "v_accvgpr_read_b32 v25, a113\n"
  "v_accvgpr_read_b32 v26, a114\n"
  "v_accvgpr_read_b32 v27, a115\n"
  "v_accvgpr_read_b32 v28, a116\n"
  "v_accvgpr_read_b32 v29, a117\n"
  "v_accvgpr_read_b32 v30, a118\n"
  "v_accvgpr_read_b32 v31, a119\n"
  "v_mul_f32 v24, v24, v17\n"
  "v_mul_f32 v25, v25, v17\n"
  "v_mul_f32 v26, v26, v17\n"
  "v_mul_f32 v27, v27, v17\n"
  "v_mul_f32 v28, v28, v17\n"
  "v_mul_f32 v29, v29, v17\n"
  "v_mul_f32 v30, v30, v17\n"
  "v_mul_f32 v31, v31, v17\n"
  "v_accvgpr_write_b32 a112, v24\n"
  "v_accvgpr_write_b32 a113, v25\n"
  "v_accvgpr_write_b32 a114, v26\n"
  "v_accvgpr_write_b32 a115, v27\n"
  "v_accvgpr_write_b32 a116, v28\n"
  "v_accvgpr_write_b32 a117, v29\n"
  "v_accvgpr_write_b32 a118, v30\n"
  "v_accvgpr_write_b32 a119, v31\n"
  "v_accvgpr_read_b32 v24, a120\n"
  "v_accvgpr_read_b32 v25, a121\n"
  "v_accvgpr_read_b32 v26, a122\n"
  "v_accvgpr_read_b32 v27, a123\n"
  "v_accvgpr_read_b32 v28, a124\n"
  "v_accvgpr_read_b32 v29, a125\n"
  "v_accvgpr_read_b32 v30, a126\n"
  "v_accvgpr_read_b32 v31, a127\n"
  "v_mul_f32 v24, v24, v17\n"
  "v_mul_f32 v25, v25, v17\n"
  "v_mul_f32 v26, v26, v17\n"
  "v_mul_f32 v27, v27, v17\n"
  "v_mul_f32 v28, v28, v17\n"
  "v_mul_f32 v29, v29, v17\n"
  "v_mul_f32 v30, v30, v17\n"
  "v_mul_f32 v31, v31, v17\n"
  "v_accvgpr_write_b32 a120, v24\n"
  "v_accvgpr_write_b32 a121, v25\n"
  "v_accvgpr_write_b32 a122, v26\n"
  "v_accvgpr_write_b32 a123, v27\n"
  "v_accvgpr_write_b32 a124, v28\n"
  "v_accvgpr_write_b32 a125, v29\n"
  "v_accvgpr_write_b32 a126, v30\n"
  "v_accvgpr_write_b32 a127, v31\n"
  "v_exp_f32 v2, v64\n"
  "v_exp_f32 v3, v65\n"
  "s_nop 0\n"
  "v_cvt_pk_bf16_f32 v112, v2, v3\n"
  "v_exp_f32 v4, v66\n"
  "s_nop 0\n"
  "v_add_f32 v12, v2, v4\n"
  "v_exp_f32 v5, v67\n"
  "s_nop 0\n"
  "v_add_f32 v13, v3, v5\n"
  "v_cvt_pk_bf16_f32 v113, v4, v5\n"
  "v_exp_f32 v6, v68\n"
  "s_nop 0\n"
  "v_add_f32 v12, v12, v6\n"
  "v_exp_f32 v7, v69\n"
  "s_nop 0\n"
  "v_add_f32 v13, v13, v7\n"
  "v_cvt_pk_bf16_f32 v114, v6, v7\n"
  "v_exp_f32 v8, v70\n"
  "s_nop 0\n"
  "v_add_f32 v12, v12, v8\n"
  "v_exp_f32 v9, v71\n"
  "s_nop 0\n"
  "v_add_f32 v13, v13, v9\n"
  "v_cvt_pk_bf16_f32 v115, v8, v9\n"
  "v_exp_f32 v2, v72\n"
  "s_nop 0\n"
  "v_add_f32 v12, v12, v2\n"
  "v_exp_f32 v3, v73\n"
  "s_nop 0\n"
  "v_add_f32 v13, v13, v3\n"
  "v_cvt_pk_bf16_f32 v116, v2, v3\n"
  "v_exp_f32 v4, v74\n"
  "s_nop 0\n"
  "v_add_f32 v12, v12, v4\n"
  "v_exp_f32 v5, v75\n"
  "s_nop 0\n"
  "v_add_f32 v13, v13, v5\n"
  "v_cvt_pk_bf16_f32 v117, v4, v5\n"
  "v_exp_f32 v6, v76\n"
  "s_nop 0\n"
  "v_add_f32 v12, v12, v6\n"
  "v_exp_f32 v7, v77\n"
  "s_nop 0\n"
  "v_add_f32 v13, v13, v7\n"
  "v_cvt_pk_bf16_f32 v118, v6, v7\n"
  "v_exp_f32 v8, v78\n"
  "s_nop 0\n"
  "v_add_f32 v12, v12, v8\n"
  "v_exp_f32 v9, v79\n"
  "s_nop 0\n"
  "v_add_f32 v13, v13, v9\n"
  "v_cvt_pk_bf16_f32 v119, v8, v9\n"
  "v_exp_f32 v2, v80\n"
  "s_nop 0\n"
  "v_add_f32 v12, v12, v2\n"
  "v_exp_f32 v3, v81\n"
  "s_nop 0\n"
  "v_add_f32 v13, v13, v3\n"
  "v_cvt_pk_bf16_f32 v120, v2, v3\n"
  "v_exp_f32 v4, v82\n"
  "s_nop 0\n"
  "v_add_f32 v12, v12, v4\n"
  "v_exp_f32 v5, v83\n"
  "s_nop 0\n"
  "v_add_f32 v13, v13, v5\n"
  "v_cvt_pk_bf16_f32 v121, v4, v5\n"
  "v_exp_f32 v6, v84\n"
  "s_nop 0\n"
  "v_add_f32 v12, v12, v6\n"
  "v_exp_f32 v7, v85\n"
  "s_nop 0\n"
  "v_add_f32 v13, v13, v7\n"
  "v_cvt_pk_bf16_f32 v122, v6, v7\n"
  "v_exp_f32 v8, v86\n"
  "s_nop 0\n"
  "v_add_f32 v12, v12, v8\n"
  "v_exp_f32 v9, v87\n"
  "s_nop 0\n"
  "v_add_f32 v13, v13, v9\n"
  "v_cvt_pk_bf16_f32 v123, v8, v9\n"
  "v_exp_f32 v2, v88\n"
  "s_nop 0\n"
  "v_add_f32 v12, v12, v2\n"
  "v_exp_f32 v3, v89\n"
  "s_nop 0\n"
  "v_add_f32 v13, v13, v3\n"
  "v_cvt_pk_bf16_f32 v124, v2, v3\n"
  "v_exp_f32 v4, v90\n"
  "s_nop 0\n"
  "v_add_f32 v12, v12, v4\n"
  "v_exp_f32 v5, v91\n"
  "s_nop 0\n"
  "v_add_f32 v13, v13, v5\n"
  "v_cvt_pk_bf16_f32 v125, v4, v5\n"
  "v_exp_f32 v6, v92\n"
  "s_nop 0\n"
  "v_add_f32 v12, v12, v6\n"
  "v_exp_f32 v7, v93\n"
  "s_nop 0\n"
  "v_add_f32 v13, v13, v7\n"
  "v_cvt_pk_bf16_f32 v126, v6, v7\n"
  "v_exp_f32 v8, v94\n"
  "s_nop 0\n"
  "v_add_f32 v12, v12, v8\n"
  "v_exp_f32 v9, v95\n"
  "s_nop 0\n"
  "v_add_f32 v13, v13, v9\n"
  "v_cvt_pk_bf16_f32 v127, v8, v9\n"
  "v_add_f32 v12, v12, v13\n"
  "v_exp_f32 v2, v32\n"
  "v_exp_f32 v3, v33\n"
  "s_nop 3\n"
  "s_branch FBD1_%=_r\n"
  "MD0_%=:\n"
  "s_nop 7\n"
  "s_andn2_b32 s55, s60, 0x3fff\n"
  "s_lshr_b32 s55, s55, 8\n"
  "s_add_i32 s55, s55, -192\n"
  "v_subrev_u32 v16, s55, v21\n"
  "v_mov_b32 v17, 0xff800000\n"
  "v_cmp_gt_i32 s[40:41], 0, v16\n"
  "v_cmp_gt_i32 s[42:43], 1, v16\n"
  "v_cmp_gt_i32 s[44:45], 2, v16\n"
  "v_cmp_gt_i32 s[46:47], 3, v16\n"
  "v_cndmask_b32 v192, v192, v17, s[40:41]\n"
  "v_cndmask_b32 v193, v193, v17, s[42:43]\n"
  "v_cndmask_b32 v194, v194, v17, s[44:45]\n"
  "v_cndmask_b32 v195, v195, v17, s[46:47]\n"
  "v_cmp_gt_i32 s[40:41], 8, v16\n"
  "v_cmp_gt_i32 s[42:43], 9, v16\n"
  "v_cmp_gt_i32 s[44:45], 10, v16\n"
  "v_cmp_gt_i32 s[46:47], 11, v16\n"
  "v_cndmask_b32 v196, v196, v17, s[40:41]\n"
  "v_cndmask_b32 v197, v197, v17, s[42:43]\n"
  "v_cndmask_b32 v198, v198, v17, s[44:45]\n"
  "v_cndmask_b32 v199, v199, v17, s[46:47]\n"
  "v_cmp_gt_i32 s[40:41], 16, v16\n"
  "v_cmp_gt_i32 s[42:43], 17, v16\n"
  "v_cmp_gt_i32 s[44:45], 18, v16\n"
  "v_cmp_gt_i32 s[46:47], 19, v16\n"
  "v_cndmask_b32 v200, v200, v17, s[40:41]\n"
  "v_cndmask_b32 v201, v201, v17, s[42:43]\n"
  "v_cndmask_b32 v202, v202, v17, s[44:45]\n"
  "v_cndmask_b32 v203, v203, v17, s[46:47]\n"
  "v_cmp_gt_i32 s[40:41], 24, v16\n"
  "v_cmp_gt_i32 s[42:43], 25, v16\n"
  "v_cmp_gt_i32 s[44:45], 26, v16\n"
  "v_cmp_gt_i32 s[46:47], 27, v16\n"
  "v_cndmask_b32 v204, v204, v17, s[40:41]\n"
  "v_cndmask_b32 v205, v205, v17, s[42:43]\n"
  "v_cndmask_b32 v206, v206, v17, s[44:45]\n"
  "v_cndmask_b32 v207, v207, v17, s[46:47]\n"
  "v_cmp_gt_i32 s[40:41], 32, v16\n"
  "v_cmp_gt_i32 s[42:43], 33, v16\n"
  "v_cmp_gt_i32 s[44:45], 34, v16\n"
  "v_cmp_gt_i32 s[46:47], 35, v16\n"
  "v_cndmask_b32 v208, v208, v17, s[40:41]\n"
  "v_cndmask_b32 v209, v209, v17, s[42:43]\n"
  "v_cndmask_b32 v210, v210, v17, s[44:45]\n"
  "v_cndmask_b32 v211, v211, v17, s[46:47]\n"
  "v_cmp_gt_i32 s[40:41], 40, v16\n"
  "v_cmp_gt_i32 s[42:43], 41, v16\n"
  "v_cmp_gt_i32 s[44:45], 42, v16\n"
  "v_cmp_gt_i32 s[46:47], 43, v16\n"
  "v_cndmask_b32 v212, v212, v17, s[40:41]\n"
  "v_cndmask_b32 v213, v213, v17, s[42:43]\n"
  "v_cndmask_b32 v214, v214, v17, s[44:45]\n"
  "v_cndmask_b32 v215, v215, v17, s[46:47]\n"
  "v_cmp_gt_i32 s[40:41], 48, v16\n"
  "v_cmp_gt_i32 s[42:43], 49, v16\n"
  "v_cmp_gt_i32 s[44:45], 50, v16\n"
  "v_cmp_gt_i32 s[46:47], 51, v16\n"
  "v_cndmask_b32 v216, v216, v17, s[40:41]\n"
  "v_cndmask_b32 v217, v217, v17, s[42:43]\n"
  "v_cndmask_b32 v218, v218, v17, s[44:45]\n"
  "v_cndmask_b32 v219, v219, v17, s[46:47]\n"
  "v_cmp_gt_i32 s[40:41], 56, v16\n"
  "v_cmp_gt_i32 s[42:43], 57, v16\n"
  "v_cmp_gt_i32 s[44:45], 58, v16\n"
  "v_cmp_gt_i32 s[46:47], 59, v16\n"
  "v_cndmask_b32 v220, v220, v17, s[40:41]\n"
  "v_cndmask_b32 v221, v221, v17, s[42:43]\n"
  "v_cndmask_b32 v222, v222, v17, s[44:45]\n"
  "v_cndmask_b32 v223, v223, v17, s[46:47]\n"
  "s_nop 1\n"
  "s_branch MD0_%=_r\n"
  "MD1_%=:\n"
  "s_cmp_ge_u32 s60, s63\n"
  "s_cbranch_scc1 MD1_%=_r\n"
  "s_nop 7\n"
  "s_andn2_b32 s55, s60, 0x3fff\n"
  "s_lshr_b32 s55, s55, 8\n"
  "s_add_i32 s55, s55, -256\n"
  "v_subrev_u32 v16, s55, v22\n"
  "v_mov_b32 v17, 0xff800000\n"
  "v_cmp_gt_i32 s[40:41], 0, v16\n"
  "v_cmp_gt_i32 s[42:43], 1, v16\n"
  "v_cmp_gt_i32 s[44:45], 2, v16\n"
  "v_cmp_gt_i32 s[46:47], 3, v16\n"
  "v_cndmask_b32 v224, v224, v17, s[40:41]\n"
  "v_cndmask_b32 v225, v225, v17, s[42:43]\n"
  "v_cndmask_b32 v226, v226, v17, s[44:45]\n"
  "v_cndmask_b32 v227, v227, v17, s[46:47]\n"
  "v_cmp_gt_i32 s[40:41], 8, v16\n"
  "v_cmp_gt_i32 s[42:43], 9, v16\n"
  "v_cmp_gt_i32 s[44:45], 10, v16\n"
  "v_cmp_gt_i32 s[46:47], 11, v16\n"
  "v_cndmask_b32 v228, v228, v17, s[40:41]\n"
  "v_cndmask_b32 v229, v229, v17, s[42:43]\n"
  "v_cndmask_b32 v230, v230, v17, s[44:45]\n"
  "v_cndmask_b32 v231, v231, v17, s[46:47]\n"
  "v_cmp_gt_i32 s[40:41], 16, v16\n"
  "v_cmp_gt_i32 s[42:43], 17, v16\n"
  "v_cmp_gt_i32 s[44:45], 18, v16\n"
  "v_cmp_gt_i32 s[46:47], 19, v16\n"
  "v_cndmask_b32 v232, v232, v17, s[40:41]\n"
  "v_cndmask_b32 v233, v233, v17, s[42:43]\n"
  "v_cndmask_b32 v234, v234, v17, s[44:45]\n"
  "v_cndmask_b32 v235, v235, v17, s[46:47]\n"
  "v_cmp_gt_i32 s[40:41], 24, v16\n"
  "v_cmp_gt_i32 s[42:43], 25, v16\n"
  "v_cmp_gt_i32 s[44:45], 26, v16\n"
  "v_cmp_gt_i32 s[46:47], 27, v16\n"
  "v_cndmask_b32 v236, v236, v17, s[40:41]\n"
  "v_cndmask_b32 v237, v237, v17, s[42:43]\n"
  "v_cndmask_b32 v238, v238, v17, s[44:45]\n"
  "v_cndmask_b32 v239, v239, v17, s[46:47]\n"
  "v_cmp_gt_i32 s[40:41], 32, v16\n"
  "v_cmp_gt_i32 s[42:43], 33, v16\n"
  "v_cmp_gt_i32 s[44:45], 34, v16\n"
  "v_cmp_gt_i32 s[46:47], 35, v16\n"
  "v_cndmask_b32 v240, v240, v17, s[40:41]\n"
  "v_cndmask_b32 v241, v241, v17, s[42:43]\n"
  "v_cndmask_b32 v242, v242, v17, s[44:45]\n"
  "v_cndmask_b32 v243, v243, v17, s[46:47]\n"
  "v_cmp_gt_i32 s[40:41], 40, v16\n"
  "v_cmp_gt_i32 s[42:43], 41, v16\n"
  "v_cmp_gt_i32 s[44:45], 42, v16\n"
  "v_cmp_gt_i32 s[46:47], 43, v16\n"
  "v_cndmask_b32 v244, v244, v17, s[40:41]\n"
  "v_cndmask_b32 v245, v245, v17, s[42:43]\n"
  "v_cndmask_b32 v246, v246, v17, s[44:45]\n"
  "v_cndmask_b32 v247, v247, v17, s[46:47]\n"
  "v_cmp_gt_i32 s[40:41], 48, v16\n"
  "v_cmp_gt_i32 s[42:43], 49, v16\n"
  "v_cmp_gt_i32 s[44:45], 50, v16\n"
  "v_cmp_gt_i32 s[46:47], 51, v16\n"
  "v_cndmask_b32 v248, v248, v17, s[40:41]\n"
  "v_cndmask_b32 v249, v249, v17, s[42:43]\n"
  "v_cndmask_b32 v250, v250, v17, s[44:45]\n"
  "v_cndmask_b32 v251, v251, v17, s[46:47]\n"
  "v_cmp_gt_i32 s[40:41], 56, v16\n"
  "v_cmp_gt_i32 s[42:43], 57, v16\n"
  "v_cmp_gt_i32 s[44:45], 58, v16\n"
  "v_cmp_gt_i32 s[46:47], 59, v16\n"
  "v_cndmask_b32 v252, v252, v17, s[40:41]\n"
  "v_cndmask_b32 v253, v253, v17, s[42:43]\n"
  "v_cndmask_b32 v254, v254, v17, s[44:45]\n"
  "v_cndmask_b32 v255, v255, v17, s[46:47]\n"
  "s_nop 1\n"
  "s_branch MD1_%=_r\n"
  "PW_%=:\n"
  "s_mov_b32 s57, 0x20000\n"
  "PW_%=_l:\n"
  "s_sleep 2\n"
  "buffer_load_dwordx2 v[18:19], v24, s[48:51], s23 offen sc1 \n"
  "s_waitcnt vmcnt(0)\n"
  "v_cmp_ne_u64 vcc, s[70:71], v[18:19]\n"
  "s_sub_u32 s57, s57, 1\n"
  "s_cbranch_scc1 PW_%=_r\n"
  "s_cbranch_vccnz PW_%=_l\n"
  "s_branch PW_%=_r\n"
  "MT_%=:\n"
  "v_lshlrev_b32 v19, 3, v20\n"
  "buffer_load_dword v18, v19, s[36:39], s90 offen sc1\n"
  "buffer_load_dword v18, v19, s[32:35], s91 offen\n"
  "s_nop 7\n"
  "s_andn2_b32 s55, s60, 0x3fff\n"
  "s_lshr_b32 s55, s55, 8\n"
  "s_add_i32 s55, s55, -192\n"
  "v_subrev_u32 v16, s55, v21\n"
  "v_mov_b32 v17, 0xff800000\n"
  "v_cmp_gt_i32 s[40:41], 0, v16\n"
  "v_cmp_gt_i32 s[42:43], 1, v16\n"
  "v_cmp_gt_i32 s[44:45], 2, v16\n"
  "v_cmp_gt_i32 s[46:47], 3, v16\n"
  "v_cndmask_b32 v192, v192, v17, s[40:41]\n"
  "v_cndmask_b32 v193, v193, v17, s[42:43]\n"
  "v_cndmask_b32 v194, v194, v17, s[44:45]\n"
  "v_cndmask_b32 v195, v195, v17, s[46:47]\n"
  "v_cmp_gt_i32 s[40:41], 8, v16\n"
  "v_cmp_gt_i32 s[42:43], 9, v16\n"
  "v_cmp_gt_i32 s[44:45], 10, v16\n"
  "v_cmp_gt_i32 s[46:47], 11, v16\n"
  "v_cndmask_b32 v196, v196, v17, s[40:41]\n"
  "v_cndmask_b32 v197, v197, v17, s[42:43]\n"
  "v_cndmask_b32 v198, v198, v17, s[44:45]\n"
  "v_cndmask_b32 v199, v199, v17, s[46:47]\n"
  "v_cmp_gt_i32 s[40:41], 16, v16\n"
  "v_cmp_gt_i32 s[42:43], 17, v16\n"
  "v_cmp_gt_i32 s[44:45], 18, v16\n"
  "v_cmp_gt_i32 s[46:47], 19, v16\n"
  "v_cndmask_b32 v200, v200, v17, s[40:41]\n"
  "v_cndmask_b32 v201, v201, v17, s[42:43]\n"
  "v_cndmask_b32 v202, v202, v17, s[44:45]\n"
  "v_cndmask_b32 v203, v203, v17, s[46:47]\n"
  "v_cmp_gt_i32 s[40:41], 24, v16\n"
  "v_cmp_gt_i32 s[42:43], 25, v16\n"
  "v_cmp_gt_i32 s[44:45], 26, v16\n"
  "v_cmp_gt_i32 s[46:47], 27, v16\n"
  "v_cndmask_b32 v204, v204, v17, s[40:41]\n"
  "v_cndmask_b32 v205, v205, v17, s[42:43]\n"
  "v_cndmask_b32 v206, v206, v17, s[44:45]\n"
  "v_cndmask_b32 v207, v207, v17, s[46:47]\n"
  "v_cmp_gt_i32 s[40:41], 32, v16\n"
  "v_cmp_gt_i32 s[42:43], 33, v16\n"
  "v_cmp_gt_i32 s[44:45], 34, v16\n"
  "v_cmp_gt_i32 s[46:47], 35, v16\n"
  "v_cndmask_b32 v208, v208, v17, s[40:41]\n"
  "v_cndmask_b32 v209, v209, v17, s[42:43]\n"
  "v_cndmask_b32 v210, v210, v17, s[44:45]\n"
  "v_cndmask_b32 v211, v211, v17, s[46:47]\n"
  "v_cmp_gt_i32 s[40:41], 40, v16\n"
  "v_cmp_gt_i32 s[42:43], 41, v16\n"
  "v_cmp_gt_i32 s[44:45], 42, v16\n"
  "v_cmp_gt_i32 s[46:47], 43, v16\n"
  "v_cndmask_b32 v212, v212, v17, s[40:41]\n"
  "v_cndmask_b32 v213, v213, v17, s[42:43]\n"
  "v_cndmask_b32 v214, v214, v17, s[44:45]\n"
  "v_cndmask_b32 v215, v215, v17, s[46:47]\n"
  "v_cmp_gt_i32 s[40:41], 48, v16\n"
  "v_cmp_gt_i32 s[42:43], 49, v16\n"
  "v_cmp_gt_i32 s[44:45], 50, v16\n"
  "v_cmp_gt_i32 s[46:47], 51, v16\n"
  "v_cndmask_b32 v216, v216, v17, s[40:41]\n"
  "v_cndmask_b32 v217, v217, v17, s[42:43]\n"
  "v_cndmask_b32 v218, v218, v17, s[44:45]\n"
  "v_cndmask_b32 v219, v219, v17, s[46:47]\n"
  "v_cmp_gt_i32 s[40:41], 56, v16\n"
  "v_cmp_gt_i32 s[42:43], 57, v16\n"
  "v_cmp_gt_i32 s[44:45], 58, v16\n"
  "v_cmp_gt_i32 s[46:47], 59, v16\n"
  "v_cndmask_b32 v220, v220, v17, s[40:41]\n"
  "v_cndmask_b32 v221, v221, v17, s[42:43]\n"
  "v_cndmask_b32 v222, v222, v17, s[44:45]\n"
  "v_cndmask_b32 v223, v223, v17, s[46:47]\n"
  "s_nop 1\n"
  "s_branch MT_%=_r\n"
  "FBS0_%=:\n"
  "s_nop 15\n"
  "v_max3_f32 v16, v32, v33, v34\n"
  "v_max3_f32 v17, v35, v36, v37\n"
  "v_max3_f32 v16, v16, v38, v39\n"
  "v_max3_f32 v17, v17, v40, v41\n"
  "v_max3_f32 v16, v16, v42, v43\n"
  "v_max3_f32 v17, v17, v44, v45\n"
  "v_max3_f32 v16, v16, v46, v47\n"
  "v_max3_f32 v17, v17, v48, v49\n"
  "v_max3_f32 v16, v16, v50, v51\n"
  "v_max3_f32 v17, v17, v52, v53\n"
  "v_max3_f32 v16, v16, v54, v55\n"
  "v_max3_f32 v17, v17, v56, v57\n"
  "v_max3_f32 v16, v16, v58, v59\n"
  "v_max3_f32 v17, v17, v60, v61\n"
  "v_max3_f32 v16, v16, v62, v63\n"
  "v_max_f32 v16, v16, v17\n"
  "v_mov_b32 v17, v16\n"
  "s_nop 1\n"
  "v_permlane32_swap_b32 v16, v17\n"
  "v_max_f32 v16, v16, v17\n"
  "v_max_f32 v16, 0, v16\n"
  "v_exp_f32 v17, -v16\n"
  "v_sub_f32 v32, v32, v16\n"
  "v_sub_f32 v33, v33, v16\n"
  "v_sub_f32 v34, v34, v16\n"
  "v_sub_f32 v35, v35, v16\n"
  "v_sub_f32 v36, v36, v16\n"
  "v_sub_f32 v37, v37, v16\n"
  "v_sub_f32 v38, v38, v16\n"
  "v_sub_f32 v39, v39, v16\n"
  "v_sub_f32 v40, v40, v16\n"
  "v_sub_f32 v41, v41, v16\n"
  "v_sub_f32 v42, v42, v16\n"
  "v_sub_f32 v43, v43, v16\n"
  "v_sub_f32 v44, v44, v16\n"
  "v_sub_f32 v45, v45, v16\n"
  "v_sub_f32 v46, v46, v16\n"
  "v_sub_f32 v47, v47, v16\n"
  "v_sub_f32 v48, v48, v16\n"
  "v_sub_f32 v49, v49, v16\n"
  "v_sub_f32 v50, v50, v16\n"
  "v_sub_f32 v51, v51, v16\n"
  "v_sub_f32 v52, v52, v16\n"
  "v_sub_f32 v53, v53, v16\n"
  "v_sub_f32 v54, v54, v16\n"
  "v_sub_f32 v55, v55, v16\n"
  "v_sub_f32 v56, v56, v16\n"
  "v_sub_f32 v57, v57, v16\n"
  "v_sub_f32 v58, v58, v16\n"
  "v_sub_f32 v59, v59, v16\n"
  "v_sub_f32 v60, v60, v16\n"
  "v_sub_f32 v61, v61, v16\n"
  "v_sub_f32 v62, v62, v16\n"
  "v_sub_f32 v63, v63, v16\n"
  "v_sub_f32 v192, v192, v16\n"
  "v_sub_f32 v193, v193, v16\n"
  "v_sub_f32 v194, v194, v16\n"
  "v_sub_f32 v195, v195, v16\n"
  "v_sub_f32 v196, v196, v16\n"
  "v_sub_f32 v197, v197, v16\n"
  "v_sub_f32 v198, v198, v16\n"
  "v_sub_f32 v199, v199, v16\n"
  "v_sub_f32 v200, v200, v16\n"
  "v_sub_f32 v201, v201, v16\n"
  "v_sub_f32 v202, v202, v16\n"
  "v_sub_f32 v203, v203, v16\n"
  "v_sub_f32 v204, v204, v16\n"
  "v_sub_f32 v205, v205, v16\n"
  "v_sub_f32 v206, v206, v16\n"
  "v_sub_f32 v207, v207, v16\n"
  "v_sub_f32 v208, v208, v16\n"
  "v_sub_f32 v209, v209, v16\n"
  "v_sub_f32 v210, v210, v16\n"
  "v_sub_f32 v211, v211, v16\n"
  "v_sub_f32 v212, v212, v16\n"
  "v_sub_f32 v213, v213, v16\n"
  "v_sub_f32 v214, v214, v16\n"
  "v_sub_f32 v215, v215, v16\n"
  "v_sub_f32 v216, v216, v16\n"
  "v_sub_f32 v217, v217, v16\n"
  "v_sub_f32 v218, v218, v16\n"
  "v_sub_f32 v219, v219, v16\n"
  "v_sub_f32 v220, v220, v16\n"
  "v_sub_f32 v221, v221, v16\n"
  "v_sub_f32 v222, v222, v16\n"
  "v_sub_f32 v223, v223, v16\n"
  "v_sub_f32 v64, v64, v16\n"
  "v_sub_f32 v65, v65, v16\n"
  "v_sub_f32 v66, v66, v16\n"
  "v_sub_f32 v67, v67, v16\n"
  "v_sub_f32 v68, v68, v16\n"
  "v_sub_f32 v69, v69, v16\n"
  "v_sub_f32 v70, v70, v16\n"
  "v_sub_f32 v71, v71, v16\n"
  "v_sub_f32 v72, v72, v16\n"
  "v_sub_f32 v73, v73, v16\n"
  "v_sub_f32 v74, v74, v16\n"
  "v_sub_f32 v75, v75, v16\n"
  "v_sub_f32 v76, v76, v16\n"
  "v_sub_f32 v77, v77, v16\n"
  "v_sub_f32 v78, v78, v16\n"
  "v_sub_f32 v79, v79, v16\n"
  "v_sub_f32 v80, v80, v16\n"
  "v_sub_f32 v81, v81, v16\n"
  "v_sub_f32 v82, v82, v16\n"
  "v_sub_f32 v83, v83, v16\n"
  "v_sub_f32 v84, v84, v16\n"
  "v_sub_f32 v85, v85, v16\n"
  "v_sub_f32 v86, v86, v16\n"
  "v_sub_f32 v87, v87, v16\n"
  "v_sub_f32 v88, v88, v16\n"
  "v_sub_f32 v89, v89, v16\n"
  "v_sub_f32 v90, v90, v16\n"
  "v_sub_f32 v91, v91, v16\n"
  "v_sub_f32 v92, v92, v16\n"
  "v_sub_f32 v93, v93, v16\n"
  "v_sub_f32 v94, v94, v16\n"
  "v_sub_f32 v95, v95, v16\n"
  "v_mul_f32 v14, v14, v17\n"
  "v_accvgpr_read_b32 v24, a0\n"
  "v_accvgpr_read_b32 v25, a1\n"
  "v_accvgpr_read_b32 v26, a2\n"
  "v_accvgpr_read_b32 v27, a3\n"
  "v_accvgpr_read_b32 v28, a4\n"
  "v_accvgpr_read_b32 v29, a5\n"
  "v_accvgpr_read_b32 v30, a6\n"
  "v_accvgpr_read_b32 v31, a7\n"
  "v_mul_f32 v24, v24, v17\n"
  "v_mul_f32 v25, v25, v17\n"
  "v_mul_f32 v26, v26, v17\n"
  "v_mul_f32 v27, v27, v17\n"
  "v_mul_f32 v28, v28, v17\n"
  "v_mul_f32 v29, v29, v17\n"
  "v_mul_f32 v30, v30, v17\n"
  "v_mul_f32 v31, v31, v17\n"
  "v_accvgpr_write_b32 a0, v24\n"
  "v_accvgpr_write_b32 a1, v25\n"
  "v_accvgpr_write_b32 a2, v26\n"
  "v_accvgpr_write_b32 a3, v27\n"
  "v_accvgpr_write_b32 a4, v28\n"
  "v_accvgpr_write_b32 a5, v29\n"
  "v_accvgpr_write_b32 a6, v30\n"
  "v_accvgpr_write_b32 a7, v31\n"
  "v_accvgpr_read_b32 v24, a8\n"
  "v_accvgpr_read_b32 v25, a9\n"
  "v_accvgpr_read_b32 v26, a10\n"
  "v_accvgpr_read_b32 v27, a11\n"
  "v_accvgpr_read_b32 v28, a12\n"
  "v_accvgpr_read_b32 v29, a13\n"
  "v_accvgpr_read_b32 v30, a14\n"
  "v_accvgpr_read_b32 v31, a15\n"
  "v_mul_f32 v24, v24, v17\n"
  "v_mul_f32 v25, v25, v17\n"
  "v_mul_f32 v26, v26, v17\n"
  "v_mul_f32 v27, v27, v17\n"
  "v_mul_f32 v28, v28, v17\n"
  "v_mul_f32 v29, v29, v17\n"
  "v_mul_f32 v30, v30, v17\n"
  "v_mul_f32 v31, v31, v17\n"
  "v_accvgpr_write_b32 a8, v24\n"
  "v_accvgpr_write_b32 a9, v25\n"
  "v_accvgpr_write_b32 a10, v26\n"
  "v_accvgpr_write_b32 a11, v27\n"
  "v_accvgpr_write_b32 a12, v28\n"
  "v_accvgpr_write_b32 a13, v29\n"
  "v_accvgpr_write_b32 a14, v30\n"
  "v_accvgpr_write_b32 a15, v31\n"
  "v_accvgpr_read_b32 v24, a16\n"
  "v_accvgpr_read_b32 v25, a17\n"
  "v_accvgpr_read_b32 v26, a18\n"
  "v_accvgpr_read_b32 v27, a19\n"
  "v_accvgpr_read_b32 v28, a20\n"
  "v_accvgpr_read_b32 v29, a21\n"
  "v_accvgpr_read_b32 v30, a22\n"
  "v_accvgpr_read_b32 v31, a23\n"
  "v_mul_f32 v24, v24, v17\n"
  "v_mul_f32 v25, v25, v17\n"
  "v_mul_f32 v26, v26, v17\n"
  "v_mul_f32 v27, v27, v17\n"
  "v_mul_f32 v28, v28, v17\n"
  "v_mul_f32 v29, v29, v17\n"
  "v_mul_f32 v30, v30, v17\n"
  "v_mul_f32 v31, v31, v17\n"
  "v_accvgpr_write_b32 a16, v24\n"
  "v_accvgpr_write_b32 a17, v25\n"
  "v_accvgpr_write_b32 a18, v26\n"
  "v_accvgpr_write_b32 a19, v27\n"
  "v_accvgpr_write_b32 a20, v28\n"
  "v_accvgpr_write_b32 a21, v29\n"
  "v_accvgpr_write_b32 a22, v30\n"
  "v_accvgpr_write_b32 a23, v31\n"
  "v_accvgpr_read_b32 v24, a24\n"
  "v_accvgpr_read_b32 v25, a25\n"
  "v_accvgpr_read_b32 v26, a26\n"
  "v_accvgpr_read_b32 v27, a27\n"
  "v_accvgpr_read_b32 v28, a28\n"
  "v_accvgpr_read_b32 v29, a29\n"
  "v_accvgpr_read_b32 v30, a30\n"
  "v_accvgpr_read_b32 v31, a31\n"
  "v_mul_f32 v24, v24, v17\n"
  "v_mul_f32 v25, v25, v17\n"
  "v_mul_f32 v26, v26, v17\n"
  "v_mul_f32 v27, v27, v17\n"
  "v_mul_f32 v28, v28, v17\n"
  "v_mul_f32 v29, v29, v17\n"
  "v_mul_f32 v30, v30, v17\n"
  "v_mul_f32 v31, v31, v17\n"
  "v_accvgpr_write_b32 a24, v24\n"
  "v_accvgpr_write_b32 a25, v25\n"
  "v_accvgpr_write_b32 a26, v26\n"
  "v_accvgpr_write_b32 a27, v27\n"
  "v_accvgpr_write_b32 a28, v28\n"
  "v_accvgpr_write_b32 a29, v29\n"
  "v_accvgpr_write_b32 a30, v30\n"
  "v_accvgpr_write_b32 a31, v31\n"
  "v_accvgpr_read_b32 v24, a32\n"
  "v_accvgpr_read_b32 v25, a33\n"
  "v_accvgpr_read_b32 v26, a34\n"
  "v_accvgpr_read_b32 v27, a35\n"
  "v_accvgpr_read_b32 v28, a36\n"
  "v_accvgpr_read_b32 v29, a37\n"
  "v_accvgpr_read_b32 v30, a38\n"
  "v_accvgpr_read_b32 v31, a39\n"
  "v_mul_f32 v24, v24, v17\n"
  "v_mul_f32 v25, v25, v17\n"
  "v_mul_f32 v26, v26, v17\n"
  "v_mul_f32 v27, v27, v17\n"
  "v_mul_f32 v28, v28, v17\n"
  "v_mul_f32 v29, v29, v17\n"
  "v_mul_f32 v30, v30, v17\n"
  "v_mul_f32 v31, v31, v17\n"
  "v_accvgpr_write_b32 a32, v24\n"
  "v_accvgpr_write_b32 a33, v25\n"
  "v_accvgpr_write_b32 a34, v26\n"
  "v_accvgpr_write_b32 a35, v27\n"
  "v_accvgpr_write_b32 a36, v28\n"
  "v_accvgpr_write_b32 a37, v29\n"
  "v_accvgpr_write_b32 a38, v30\n"
  "v_accvgpr_write_b32 a39, v31\n"
  "v_accvgpr_read_b32 v24, a40\n"
  "v_accvgpr_read_b32 v25, a41\n"
  "v_accvgpr_read_b32 v26, a42\n"
  "v_accvgpr_read_b32 v27, a43\n"
  "v_accvgpr_read_b32 v28, a44\n"
  "v_accvgpr_read_b32 v29, a45\n"
  "v_accvgpr_read_b32 v30, a46\n"
  "v_accvgpr_read_b32 v31, a47\n"
  "v_mul_f32 v24, v24, v17\n"
  "v_mul_f32 v25, v25, v17\n"
  "v_mul_f32 v26, v26, v17\n"
  "v_mul_f32 v27, v27, v17\n"
  "v_mul_f32 v28, v28, v17\n"
  "v_mul_f32 v29, v29, v17\n"
  "v_mul_f32 v30, v30, v17\n"
  "v_mul_f32 v31, v31, v17\n"
  "v_accvgpr_write_b32 a40, v24\n"
  "v_accvgpr_write_b32 a41, v25\n"
  "v_accvgpr_write_b32 a42, v26\n"
  "v_accvgpr_write_b32 a43, v27\n"
  "v_accvgpr_write_b32 a44, v28\n"
  "v_accvgpr_write_b32 a45, v29\n"
  "v_accvgpr_write_b32 a46, v30\n"
  "v_accvgpr_write_b32 a47, v31\n"
  "v_accvgpr_read_b32 v24, a48\n"
  "v_accvgpr_read_b32 v25, a49\n"
  "v_accvgpr_read_b32 v26, a50\n"
  "v_accvgpr_read_b32 v27, a51\n"
  "v_accvgpr_read_b32 v28, a52\n"
  "v_accvgpr_read_b32 v29, a53\n"
  "v_accvgpr_read_b32 v30, a54\n"
  "v_accvgpr_read_b32 v31, a55\n"
  "v_mul_f32 v24, v24, v17\n"
  "v_mul_f32 v25, v25, v17\n"
  "v_mul_f32 v26, v26, v17\n"
  "v_mul_f32 v27, v27, v17\n"
  "v_mul_f32 v28, v28, v17\n"
  "v_mul_f32 v29, v29, v17\n"
  "v_mul_f32 v30, v30, v17\n"
  "v_mul_f32 v31, v31, v17\n"
  "v_accvgpr_write_b32 a48, v24\n"
  "v_accvgpr_write_b32 a49, v25\n"
  "v_accvgpr_write_b32 a50, v26\n"
  "v_accvgpr_write_b32 a51, v27\n"
  "v_accvgpr_write_b32 a52, v28\n"
  "v_accvgpr_write_b32 a53, v29\n"
  "v_accvgpr_write_b32 a54, v30\n"
  "v_accvgpr_write_b32 a55, v31\n"
  "v_accvgpr_read_b32 v24, a56\n"
  "v_accvgpr_read_b32 v25, a57\n"
  "v_accvgpr_read_b32 v26, a58\n"
  "v_accvgpr_read_b32 v27, a59\n"
  "v_accvgpr_read_b32 v28, a60\n"
  "v_accvgpr_read_b32 v29, a61\n"
  "v_accvgpr_read_b32 v30, a62\n"
  "v_accvgpr_read_b32 v31, a63\n"
  "v_mul_f32 v24, v24, v17\n"
  "v_mul_f32 v25, v25, v17\n"
  "v_mul_f32 v26, v26, v17\n"
  "v_mul_f32 v27, v27, v17\n"
  "v_mul_f32 v28, v28, v17\n"
  "v_mul_f32 v29, v29, v17\n"
  "v_mul_f32 v30, v30, v17\n"
  "v_mul_f32 v31, v31, v17\n"
  "v_accvgpr_write_b32 a56, v24\n"
  "v_accvgpr_write_b32 a57, v25\n"
  "v_accvgpr_write_b32 a58, v26\n"
  "v_accvgpr_write_b32 a59, v27\n"
  "v_accvgpr_write_b32 a60, v28\n"
  "v_accvgpr_write_b32 a61, v29\n"
  "v_accvgpr_write_b32 a62, v30\n"
  "v_accvgpr_write_b32 a63, v31\n"
  "v_exp_f32 v2, v32\n"
  "v_exp_f32 v3, v33\n"
  "s_nop 0\n"
  "v_cvt_pk_bf16_f32 v96, v2, v3\n"
  "v_exp_f32 v4, v34\n"
  "s_nop 0\n"
  "v_add_f32 v10, v2, v4\n"
  "v_exp_f32 v5, v35\n"
  "s_nop 0\n"
  "v_add_f32 v11, v3, v5\n"
  "v_cvt_pk_bf16_f32 v97, v4, v5\n"
  "v_exp_f32 v6, v36\n"
  "s_nop 0\n"
  "v_add_f32 v10, v10, v6\n"
  "v_exp_f32 v7, v37\n"
  "s_nop 0\n"
  "v_add_f32 v11, v11, v7\n"
  "v_cvt_pk_bf16_f32 v98, v6, v7\n"
  "v_exp_f32 v8, v38\n"
  "s_nop 0\n"
  "v_add_f32 v10, v10, v8\n"
  "v_exp_f32 v9, v39\n"
  "s_nop 0\n"
  "v_add_f32 v11, v11, v9\n"
  "v_cvt_pk_bf16_f32 v99, v8, v9\n"
  "v_exp_f32 v2, v40\n"
  "s_nop 0\n"
  "v_add_f32 v10, v10, v2\n"
  "v_exp_f32 v3, v41\n"
  "s_nop 0\n"
  "v_add_f32 v11, v11, v3\n"
  "v_cvt_pk_bf16_f32 v100, v2, v3\n"
  "v_exp_f32 v4, v42\n"
  "s_nop 0\n"
  "v_add_f32 v10, v10, v4\n"
  "v_exp_f32 v5, v43\n"
  "s_nop 0\n"
  "v_add_f32 v11, v11, v5\n"
  "v_cvt_pk_bf16_f32 v101, v4, v5\n"
  "v_exp_f32 v6, v44\n"
  "s_nop 0\n"
  "v_add_f32 v10, v10, v6\n"
  "v_exp_f32 v7, v45\n"
  "s_nop 0\n"
  "v_add_f32 v11, v11, v7\n"
  "v_cvt_pk_bf16_f32 v102, v6, v7\n"
  "v_exp_f32 v8, v46\n"
  "s_nop 0\n"
  "v_add_f32 v10, v10, v8\n"
  "v_exp_f32 v9, v47\n"
  "s_nop 0\n"
  "v_add_f32 v11, v11, v9\n"
  "v_cvt_pk_bf16_f32 v103, v8, v9\n"
  "v_exp_f32 v2, v48\n"
  "s_nop 0\n"
  "v_add_f32 v10, v10, v2\n"
  "v_exp_f32 v3, v49\n"
  "s_nop 0\n"
  "v_add_f32 v11, v11, v3\n"
  "v_cvt_pk_bf16_f32 v104, v2, v3\n"
  "v_exp_f32 v4, v50\n"
  "s_nop 0\n"
  "v_add_f32 v10, v10, v4\n"
  "v_exp_f32 v5, v51\n"
  "s_nop 0\n"
  "v_add_f32 v11, v11, v5\n"
  "v_cvt_pk_bf16_f32 v105, v4, v5\n"
  "v_exp_f32 v6, v52\n"
  "s_nop 0\n"
  "v_add_f32 v10, v10, v6\n"
  "v_exp_f32 v7, v53\n"
  "s_nop 0\n"
  "v_add_f32 v11, v11, v7\n"
  "v_cvt_pk_bf16_f32 v106, v6, v7\n"
  "v_exp_f32 v8, v54\n"
  "s_nop 0\n"
  "v_add_f32 v10, v10, v8\n"
  "v_exp_f32 v9, v55\n"
  "s_nop 0\n"
  "v_add_f32 v11, v11, v9\n"
  "v_cvt_pk_bf16_f32 v107, v8, v9\n"
  "v_exp_f32 v2, v56\n"
  "s_nop 0\n"
  "v_add_f32 v10, v10, v2\n"
  "v_exp_f32 v3, v57\n"
  "s_nop 0\n"
  "v_add_f32 v11, v11, v3\n"
  "v_cvt_pk_bf16_f32 v108, v2, v3\n"
  "v_exp_f32 v4, v58\n"
  "s_nop 0\n"
  "v_add_f32 v10, v10, v4\n"
  "v_exp_f32 v5, v59\n"
  "s_nop 0\n"
  "v_add_f32 v11, v11, v5\n"
  "v_cvt_pk_bf16_f32 v109, v4, v5\n"
  "v_exp_f32 v6, v60\n"
  "s_nop 0\n"
  "v_add_f32 v10, v10, v6\n"
  "v_exp_f32 v7, v61\n"
  "s_nop 0\n"
  "v_add_f32 v11, v11, v7\n"
  "v_cvt_pk_bf16_f32 v110, v6, v7\n"
  "v_exp_f32 v8, v62\n"
  "s_nop 0\n"
  "v_add_f32 v10, v10, v8\n"
  "v_exp_f32 v9, v63\n"
  "s_nop 0\n"
  "v_add_f32 v11, v11, v9\n"
  "v_cvt_pk_bf16_f32 v111, v8, v9\n"
  "v_add_f32 v10, v10, v11\n"
  "v_exp_f32 v2, v64\n"
  "v_exp_f32 v3, v65\n"
  "s_nop 3\n"
  "s_branch FBS0_%=_r\n"
  "FBS1_%=:\n"
  "s_nop 15\n"
  "v_max3_f32 v16, v64, v65, v66\n"
  "v_max3_f32 v17, v67, v68, v69\n"
  "v_max3_f32 v16, v16, v70, v71\n"
  "v_max3_f32 v17, v17, v72, v73\n"
  "v_max3_f32 v16, v16, v74, v75\n"
  "v_max3_f32 v17, v17, v76, v77\n"
  "v_max3_f32 v16, v16, v78, v79\n"
  "v_max3_f32 v17, v17, v80, v81\n"
  "v_max3_f32 v16, v16, v82, v83\n"
  "v_max3_f32 v17, v17, v84, v85\n"
  "v_max3_f32 v16, v16, v86, v87\n"
  "v_max3_f32 v17, v17, v88, v89\n"
  "v_max3_f32 v16, v16, v90, v91\n"
  "v_max3_f32 v17, v17, v92, v93\n"
  "v_max3_f32 v16, v16, v94, v95\n"
  "v_max_f32 v16, v16, v17\n"
  "v_mov_b32 v17, v16\n"
  "s_nop 1\n"
  "v_permlane32_swap_b32 v16, v17\n"
  "v_max_f32 v16, v16, v17\n"
  "v_max_f32 v16, 0, v16\n"
  "v_exp_f32 v17, -v16\n"
  "v_sub_f32 v64, v64, v16\n"
  "v_sub_f32 v65, v65, v16\n"
  "v_sub_f32 v66, v66, v16\n"
  "v_sub_f32 v67, v67, v16\n"
  "v_sub_f32 v68, v68, v16\n"
  "v_sub_f32 v69, v69, v16\n"
  "v_sub_f32 v70, v70, v16\n"
  "v_sub_f32 v71, v71, v16\n"
  "v_sub_f32 v72, v72, v16\n"
  "v_sub_f32 v73, v73, v16\n"
  "v_sub_f32 v74, v74, v16\n"
  "v_sub_f32 v75, v75, v16\n"
  "v_sub_f32 v76, v76, v16\n"
  "v_sub_f32 v77, v77, v16\n"
  "v_sub_f32 v78, v78, v16\n"
  "v_sub_f32 v79, v79, v16\n"
  "v_sub_f32 v80, v80, v16\n"
  "v_sub_f32 v81, v81, v16\n"
  "v_sub_f32 v82, v82, v16\n"
  "v_sub_f32 v83, v83, v16\n"
  "v_sub_f32 v84, v84, v16\n"
  "v_sub_f32 v85, v85, v16\n"
  "v_sub_f32 v86, v86, v16\n"
  "v_sub_f32 v87, v87, v16\n"
  "v_sub_f32 v88, v88, v16\n"
  "v_sub_f32 v89, v89, v16\n"
  "v_sub_f32 v90, v90, v16\n"
  "v_sub_f32 v91, v91, v16\n"
  "v_sub_f32 v92, v92, v16\n"
  "v_sub_f32 v93, v93, v16\n"
  "v_sub_f32 v94, v94, v16\n"
  "v_sub_f32 v95, v95, v16\n"
  "v_sub_f32 v192, v192, v16\n"
  "v_sub_f32 v193, v193, v16\n"
  "v_sub_f32 v194, v194, v16\n"
  "v_sub_f32 v195, v195, v16\n"
  "v_sub_f32 v196, v196, v16\n"
  "v_sub_f32 v197, v197, v16\n"
  "v_sub_f32 v198, v198, v16\n"
  "v_sub_f32 v199, v199, v16\n"
  "v_sub_f32 v200, v200, v16\n"
  "v_sub_f32 v201, v201, v16\n"
  "v_sub_f32 v202, v202, v16\n"
  "v_sub_f32 v203, v203, v16\n"
  "v_sub_f32 v204, v204, v16\n"
  "v_sub_f32 v205, v205, v16\n"
  "v_sub_f32 v206, v206, v16\n"
  "v_sub_f32 v207, v207, v16\n"
  "v_sub_f32 v208, v208, v16\n"
  "v_sub_f32 v209, v209, v16\n"
  "v_sub_f32 v210, v210, v16\n"
  "v_sub_f32 v211, v211, v16\n"
  "v_sub_f32 v212, v212, v16\n"
  "v_sub_f32 v213, v213, v16\n"
  "v_sub_f32 v214, v214, v16\n"
  "v_sub_f32 v215, v215, v16\n"
  "v_sub_f32 v216, v216, v16\n"
  "v_sub_f32 v217, v217, v16\n"
  "v_sub_f32 v218, v218, v16\n"
  "v_sub_f32 v219, v219, v16\n"
  "v_sub_f32 v220, v220, v16\n"
  "v_sub_f32 v221, v221, v16\n"
  "v_sub_f32 v222, v222, v16\n"
  "v_sub_f32 v223, v223, v16\n"
  "v_sub_f32 v32, v32, v16\n"
  "v_sub_f32 v33, v33, v16\n"
  "v_sub_f32 v34, v34, v16\n"
  "v_sub_f32 v35, v35, v16\n"
  "v_sub_f32 v36, v36, v16\n"
  "v_sub_f32 v37, v37, v16\n"
  "v_sub_f32 v38, v38, v16\n"
  "v_sub_f32 v39, v39, v16\n"
  "v_sub_f32 v40, v40, v16\n"
  "v_sub_f32 v41, v41, v16\n"
  "v_sub_f32 v42, v42, v16\n"
  "v_sub_f32 v43, v43, v16\n"
  "v_sub_f32 v44, v44, v16\n"
  "v_sub_f32 v45, v45, v16\n"
  "v_sub_f32 v46, v46, v16\n"
  "v_sub_f32 v47, v47, v16\n"
  "v_sub_f32 v48, v48, v16\n"
  "v_sub_f32 v49, v49, v16\n"
  "v_sub_f32 v50, v50, v16\n"
  "v_sub_f32 v51, v51, v16\n"
  "v_sub_f32 v52, v52, v16\n"
  "v_sub_f32 v53, v53, v16\n"
  "v_sub_f32 v54, v54, v16\n"
  "v_sub_f32 v55, v55, v16\n"
  "v_sub_f32 v56, v56, v16\n"
  "v_sub_f32 v57, v57, v16\n"
  "v_sub_f32 v58, v58, v16\n"
  "v_sub_f32 v59, v59, v16\n"
  "v_sub_f32 v60, v60, v16\n"
  "v_sub_f32 v61, v61, v16\n"
  "v_sub_f32 v62, v62, v16\n"
  "v_sub_f32 v63, v63, v16\n"
  "v_mul_f32 v14, v14, v17\n"
  "v_accvgpr_read_b32 v24, a0\n"
  "v_accvgpr_read_b32 v25, a1\n"
  "v_accvgpr_read_b32 v26, a2\n"
  "v_accvgpr_read_b32 v27, a3\n"
  "v_accvgpr_read_b32 v28, a4\n"
  "v_accvgpr_read_b32 v29, a5\n"
  "v_accvgpr_read_b32 v30, a6\n"
  "v_accvgpr_read_b32 v31, a7\n"
  "v_mul_f32 v24, v24, v17\n"
  "v_mul_f32 v25, v25, v17\n"
  "v_mul_f32 v26, v26, v17\n"
  "v_mul_f32 v27, v27, v17\n"
  "v_mul_f32 v28, v28, v17\n"
  "v_mul_f32 v29, v29, v17\n"
  "v_mul_f32 v30, v30, v17\n"
  "v_mul_f32 v31, v31, v17\n"
  "v_accvgpr_write_b32 a0, v24\n"
  "v_accvgpr_write_b32 a1, v25\n"
  "v_accvgpr_write_b32 a2, v26\n"
  "v_accvgpr_write_b32 a3, v27\n"
  "v_accvgpr_write_b32 a4, v28\n"
  "v_accvgpr_write_b32 a5, v29\n"
  "v_accvgpr_write_b32 a6, v30\n"
  "v_accvgpr_write_b32 a7, v31\n"
  "v_accvgpr_read_b32 v24, a8\n"
  "v_accvgpr_read_b32 v25, a9\n"
  "v_accvgpr_read_b32 v26, a10\n"
  "v_accvgpr_read_b32 v27, a11\n"
  "v_accvgpr_read_b32 v28, a12\n"
  "v_accvgpr_read_b32 v29, a13\n"
  "v_accvgpr_read_b32 v30, a14\n"
  "v_accvgpr_read_b32 v31, a15\n"
  "v_mul_f32 v24, v24, v17\n"
  "v_mul_f32 v25, v25, v17\n"
  "v_mul_f32 v26, v26, v17\n"
  "v_mul_f32 v27, v27, v17\n"
  "v_mul_f32 v28, v28, v17\n"
  "v_mul_f32 v29, v29, v17\n"
  "v_mul_f32 v30, v30, v17\n"
  "v_mul_f32 v31, v31, v17\n"
  "v_accvgpr_write_b32 a8, v24\n"
  "v_accvgpr_write_b32 a9, v25\n"
  "v_accvgpr_write_b32 a10, v26\n"
  "v_accvgpr_write_b32 a11, v27\n"
  "v_accvgpr_write_b32 a12, v28\n"
  "v_accvgpr_write_b32 a13, v29\n"
  "v_accvgpr_write_b32 a14, v30\n"
  "v_accvgpr_write_b32 a15, v31\n"
  "v_accvgpr_read_b32 v24, a16\n"
  "v_accvgpr_read_b32 v25, a17\n"
  "v_accvgpr_read_b32 v26, a18\n"
  "v_accvgpr_read_b32 v27, a19\n"
  "v_accvgpr_read_b32 v28, a20\n"
  "v_accvgpr_read_b32 v29, a21\n"
  "v_accvgpr_read_b32 v30, a22\n"
  "v_accvgpr_read_b32 v31, a23\n"
  "v_mul_f32 v24, v24, v17\n"
  "v_mul_f32 v25, v25, v17\n"
  "v_mul_f32 v26, v26, v17\n"
  "v_mul_f32 v27, v27, v17\n"
  "v_mul_f32 v28, v28, v17\n"
  "v_mul_f32 v29, v29, v17\n"
  "v_mul_f32 v30, v30, v17\n"
  "v_mul_f32 v31, v31, v17\n"
  "v_accvgpr_write_b32 a16, v24\n"
  "v_accvgpr_write_b32 a17, v25\n"
  "v_accvgpr_write_b32 a18, v26\n"
  "v_accvgpr_write_b32 a19, v27\n"
  "v_accvgpr_write_b32 a20, v28\n"
  "v_accvgpr_write_b32 a21, v29\n"
  "v_accvgpr_write_b32 a22, v30\n"
  "v_accvgpr_write_b32 a23, v31\n"
  "v_accvgpr_read_b32 v24, a24\n"
  "v_accvgpr_read_b32 v25, a25\n"
  "v_accvgpr_read_b32 v26, a26\n"
  "v_accvgpr_read_b32 v27, a27\n"
  "v_accvgpr_read_b32 v28, a28\n"
  "v_accvgpr_read_b32 v29, a29\n"
  "v_accvgpr_read_b32 v30, a30\n"
  "v_accvgpr_read_b32 v31, a31\n"
  "v_mul_f32 v24, v24, v17\n"
  "v_mul_f32 v25, v25, v17\n"
  "v_mul_f32 v26, v26, v17\n"
  "v_mul_f32 v27, v27, v17\n"
  "v_mul_f32 v28, v28, v17\n"
  "v_mul_f32 v29, v29, v17\n"
  "v_mul_f32 v30, v30, v17\n"
  "v_mul_f32 v31, v31, v17\n"
  "v_accvgpr_write_b32 a24, v24\n"
  "v_accvgpr_write_b32 a25, v25\n"
  "v_accvgpr_write_b32 a26, v26\n"
  "v_accvgpr_write_b32 a27, v27\n"
  "v_accvgpr_write_b32 a28, v28\n"
  "v_accvgpr_write_b32 a29, v29\n"
  "v_accvgpr_write_b32 a30, v30\n"
  "v_accvgpr_write_b32 a31, v31\n"
  "v_accvgpr_read_b32 v24, a32\n"
  "v_accvgpr_read_b32 v25, a33\n"
  "v_accvgpr_read_b32 v26, a34\n"
  "v_accvgpr_read_b32 v27, a35\n"
  "v_accvgpr_read_b32 v28, a36\n"
  "v_accvgpr_read_b32 v29, a37\n"
  "v_accvgpr_read_b32 v30, a38\n"
  "v_accvgpr_read_b32 v31, a39\n"
  "v_mul_f32 v24, v24, v17\n"
  "v_mul_f32 v25, v25, v17\n"
  "v_mul_f32 v26, v26, v17\n"
  "v_mul_f32 v27, v27, v17\n"
  "v_mul_f32 v28, v28, v17\n"
  "v_mul_f32 v29, v29, v17\n"
  "v_mul_f32 v30, v30, v17\n"
  "v_mul_f32 v31, v31, v17\n"
  "v_accvgpr_write_b32 a32, v24\n"
  "v_accvgpr_write_b32 a33, v25\n"
  "v_accvgpr_write_b32 a34, v26\n"
  "v_accvgpr_write_b32 a35, v27\n"
  "v_accvgpr_write_b32 a36, v28\n"
  "v_accvgpr_write_b32 a37, v29\n"
  "v_accvgpr_write_b32 a38, v30\n"
  "v_accvgpr_write_b32 a39, v31\n"
  "v_accvgpr_read_b32 v24, a40\n"
  "v_accvgpr_read_b32 v25, a41\n"
  "v_accvgpr_read_b32 v26, a42\n"
  "v_accvgpr_read_b32 v27, a43\n"
  "v_accvgpr_read_b32 v28, a44\n"
  "v_accvgpr_read_b32 v29, a45\n"
  "v_accvgpr_read_b32 v30, a46\n"
  "v_accvgpr_read_b32 v31, a47\n"
  "v_mul_f32 v24, v24, v17\n"
  "v_mul_f32 v25, v25, v17\n"
  "v_mul_f32 v26, v26, v17\n"
  "v_mul_f32 v27, v27, v17\n"
  "v_mul_f32 v28, v28, v17\n"
  "v_mul_f32 v29, v29, v17\n"
  "v_mul_f32 v30, v30, v17\n"
  "v_mul_f32 v31, v31, v17\n"
  "v_accvgpr_write_b32 a40, v24\n"
  "v_accvgpr_write_b32 a41, v25\n"
  "v_accvgpr_write_b32 a42, v26\n"
  "v_accvgpr_write_b32 a43, v27\n"
  "v_accvgpr_write_b32 a44, v28\n"
  "v_accvgpr_write_b32 a45, v29\n"
  "v_accvgpr_write_b32 a46, v30\n"
  "v_accvgpr_write_b32 a47, v31\n"
  "v_accvgpr_read_b32 v24, a48\n"
  "v_accvgpr_read_b32 v25, a49\n"
  "v_accvgpr_read_b32 v26, a50\n"
  "v_accvgpr_read_b32 v27, a51\n"
  "v_accvgpr_read_b32 v28, a52\n"
  "v_accvgpr_read_b32 v29, a53\n"
  "v_accvgpr_read_b32 v30, a54\n"
  "v_accvgpr_read_b32 v31, a55\n"
  "v_mul_f32 v24, v24, v17\n"
  "v_mul_f32 v25, v25, v17\n"
  "v_mul_f32 v26, v26, v17\n"
  "v_mul_f32 v27, v27, v17\n"
  "v_mul_f32 v28, v28, v17\n"
  "v_mul_f32 v29, v29, v17\n"
  "v_mul_f32 v30, v30, v17\n"
  "v_mul_f32 v31, v31, v17\n"
  "v_accvgpr_write_b32 a48, v24\n"
  "v_accvgpr_write_b32 a49, v25\n"
  "v_accvgpr_write_b32 a50, v26\n"
  "v_accvgpr_write_b32 a51, v27\n"
  "v_accvgpr_write_b32 a52, v28\n"
  "v_accvgpr_write_b32 a53, v29\n"
  "v_accvgpr_write_b32 a54, v30\n"
  "v_accvgpr_write_b32 a55, v31\n"
  "v_accvgpr_read_b32 v24, a56\n"
  "v_accvgpr_read_b32 v25, a57\n"
  "v_accvgpr_read_b32 v26, a58\n"
  "v_accvgpr_read_b32 v27, a59\n"
  "v_accvgpr_read_b32 v28, a60\n"
  "v_accvgpr_read_b32 v29, a61\n"
  "v_accvgpr_read_b32 v30, a62\n"
  "v_accvgpr_read_b32 v31, a63\n"
  "v_mul_f32 v24, v24, v17\n"
  "v_mul_f32 v25, v25, v17\n"
  "v_mul_f32 v26, v26, v17\n"
  "v_mul_f32 v27, v27, v17\n"
  "v_mul_f32 v28, v28, v17\n"
  "v_mul_f32 v29, v29, v17\n"
  "v_mul_f32 v30, v30, v17\n"
  "v_mul_f32 v31, v31, v17\n"
  "v_accvgpr_write_b32 a56, v24\n"
  "v_accvgpr_write_b32 a57, v25\n"
  "v_accvgpr_write_b32 a58, v26\n"
  "v_accvgpr_write_b32 a59, v27\n"
  "v_accvgpr_write_b32 a60, v28\n"
  "v_accvgpr_write_b32 a61, v29\n"
  "v_accvgpr_write_b32 a62, v30\n"
  "v_accvgpr_write_b32 a63, v31\n"
  "v_exp_f32 v2, v64\n"
  "v_exp_f32 v3, v65\n"
  "s_nop 0\n"
  "v_cvt_pk_bf16_f32 v112, v2, v3\n"
  "v_exp_f32 v4, v66\n"
  "s_nop 0\n"
  "v_add_f32 v12, v2, v4\n"
  "v_exp_f32 v5, v67\n"
  "s_nop 0\n"
  "v_add_f32 v13, v3, v5\n"
  "v_cvt_pk_bf16_f32 v113, v4, v5\n"
  "v_exp_f32 v6, v68\n"
  "s_nop 0\n"
  "v_add_f32 v12, v12, v6\n"
  "v_exp_f32 v7, v69\n"
  "s_nop 0\n"
  "v_add_f32 v13, v13, v7\n"
  "v_cvt_pk_bf16_f32 v114, v6, v7\n"
  "v_exp_f32 v8, v70\n"
  "s_nop 0\n"
  "v_add_f32 v12, v12, v8\n"
  "v_exp_f32 v9, v71\n"
  "s_nop 0\n"
  "v_add_f32 v13, v13, v9\n"
  "v_cvt_pk_bf16_f32 v115, v8, v9\n"
  "v_exp_f32 v2, v72\n"
  "s_nop 0\n"
  "v_add_f32 v12, v12, v2\n"
  "v_exp_f32 v3, v73\n"
  "s_nop 0\n"
  "v_add_f32 v13, v13, v3\n"
  "v_cvt_pk_bf16_f32 v116, v2, v3\n"
  "v_exp_f32 v4, v74\n"
  "s_nop 0\n"
  "v_add_f32 v12, v12, v4\n"
  "v_exp_f32 v5, v75\n"
  "s_nop 0\n"
  "v_add_f32 v13, v13, v5\n"
  "v_cvt_pk_bf16_f32 v117, v4, v5\n"
  "v_exp_f32 v6, v76\n"
  "s_nop 0\n"
  "v_add_f32 v12, v12, v6\n"
  "v_exp_f32 v7, v77\n"
  "s_nop 0\n"
  "v_add_f32 v13, v13, v7\n"
  "v_cvt_pk_bf16_f32 v118, v6, v7\n"
  "v_exp_f32 v8, v78\n"
  "s_nop 0\n"
  "v_add_f32 v12, v12, v8\n"
  "v_exp_f32 v9, v79\n"
  "s_nop 0\n"
  "v_add_f32 v13, v13, v9\n"
  "v_cvt_pk_bf16_f32 v119, v8, v9\n"
  "v_exp_f32 v2, v80\n"
  "s_nop 0\n"
  "v_add_f32 v12, v12, v2\n"
  "v_exp_f32 v3, v81\n"
  "s_nop 0\n"
  "v_add_f32 v13, v13, v3\n"
  "v_cvt_pk_bf16_f32 v120, v2, v3\n"
  "v_exp_f32 v4, v82\n"
  "s_nop 0\n"
  "v_add_f32 v12, v12, v4\n"
  "v_exp_f32 v5, v83\n"
  "s_nop 0\n"
  "v_add_f32 v13, v13, v5\n"
  "v_cvt_pk_bf16_f32 v121, v4, v5\n"
  "v_exp_f32 v6, v84\n"
  "s_nop 0\n"
  "v_add_f32 v12, v12, v6\n"
  "v_exp_f32 v7, v85\n"
  "s_nop 0\n"
  "v_add_f32 v13, v13, v7\n"
  "v_cvt_pk_bf16_f32 v122, v6, v7\n"
  "v_exp_f32 v8, v86\n"
  "s_nop 0\n"
  "v_add_f32 v12, v12, v8\n"
  "v_exp_f32 v9, v87\n"
  "s_nop 0\n"
  "v_add_f32 v13, v13, v9\n"
  "v_cvt_pk_bf16_f32 v123, v8, v9\n"
  "v_exp_f32 v2, v88\n"
  "s_nop 0\n"
  "v_add_f32 v12, v12, v2\n"
  "v_exp_f32 v3, v89\n"
  "s_nop 0\n"
  "v_add_f32 v13, v13, v3\n"
  "v_cvt_pk_bf16_f32 v124, v2, v3\n"
  "v_exp_f32 v4, v90\n"
  "s_nop 0\n"
  "v_add_f32 v12, v12, v4\n"
  "v_exp_f32 v5, v91\n"
  "s_nop 0\n"
  "v_add_f32 v13, v13, v5\n"
  "v_cvt_pk_bf16_f32 v125, v4, v5\n"
  "v_exp_f32 v6, v92\n"
  "s_nop 0\n"
  "v_add_f32 v12, v12, v6\n"
  "v_exp_f32 v7, v93\n"
  "s_nop 0\n"
  "v_add_f32 v13, v13, v7\n"
  "v_cvt_pk_bf16_f32 v126, v6, v7\n"
  "v_exp_f32 v8, v94\n"
  "s_nop 0\n"
  "v_add_f32 v12, v12, v8\n"
  "v_exp_f32 v9, v95\n"
  "s_nop 0\n"
  "v_add_f32 v13, v13, v9\n"
  "v_cvt_pk_bf16_f32 v127, v8, v9\n"
  "v_add_f32 v12, v12, v13\n"
  "v_exp_f32 v2, v32\n"
  "v_exp_f32 v3, v33\n"
  "s_nop 3\n"
  "s_branch FBS1_%=_r\n"
  "MS0_%=:\n"
  "v_lshlrev_b32 v19, 3, v20\n"
  "buffer_load_dword v18, v19, s[36:39], s90 offen sc1\n"
  "buffer_load_dword v18, v19, s[32:35], s91 offen\n"
  "s_nop 7\n"
  "s_andn2_b32 s55, s60, 0x3fff\n"
  "s_lshr_b32 s55, s55, 8\n"
  "s_add_i32 s55, s55, -192\n"
  "v_subrev_u32 v16, s55, v21\n"
  "v_mov_b32 v17, 0xff800000\n"
  "v_cmp_gt_i32 s[40:41], 0, v16\n"
  "v_cmp_gt_i32 s[42:43], 1, v16\n"
  "v_cmp_gt_i32 s[44:45], 2, v16\n"
  "v_cmp_gt_i32 s[46:47], 3, v16\n"
  "v_cndmask_b32 v192, v192, v17, s[40:41]\n"
  "v_cndmask_b32 v193, v193, v17, s[42:43]\n"
  "v_cndmask_b32 v194, v194, v17, s[44:45]\n"
  "v_cndmask_b32 v195, v195, v17, s[46:47]\n"
  "v_cmp_gt_i32 s[40:41], 8, v16\n"
  "v_cmp_gt_i32 s[42:43], 9, v16\n"
  "v_cmp_gt_i32 s[44:45], 10, v16\n"
  "v_cmp_gt_i32 s[46:47], 11, v16\n"
  "v_cndmask_b32 v196, v196, v17, s[40:41]\n"
  "v_cndmask_b32 v197, v197, v17, s[42:43]\n"
  "v_cndmask_b32 v198, v198, v17, s[44:45]\n"
  "v_cndmask_b32 v199, v199, v17, s[46:47]\n"
  "v_cmp_gt_i32 s[40:41], 16, v16\n"
  "v_cmp_gt_i32 s[42:43], 17, v16\n"
  "v_cmp_gt_i32 s[44:45], 18, v16\n"
  "v_cmp_gt_i32 s[46:47], 19, v16\n"
  "v_cndmask_b32 v200, v200, v17, s[40:41]\n"
  "v_cndmask_b32 v201, v201, v17, s[42:43]\n"
  "v_cndmask_b32 v202, v202, v17, s[44:45]\n"
  "v_cndmask_b32 v203, v203, v17, s[46:47]\n"
  "v_cmp_gt_i32 s[40:41], 24, v16\n"
  "v_cmp_gt_i32 s[42:43], 25, v16\n"
  "v_cmp_gt_i32 s[44:45], 26, v16\n"
  "v_cmp_gt_i32 s[46:47], 27, v16\n"
  "v_cndmask_b32 v204, v204, v17, s[40:41]\n"
  "v_cndmask_b32 v205, v205, v17, s[42:43]\n"
  "v_cndmask_b32 v206, v206, v17, s[44:45]\n"
  "v_cndmask_b32 v207, v207, v17, s[46:47]\n"
  "v_cmp_gt_i32 s[40:41], 32, v16\n"
  "v_cmp_gt_i32 s[42:43], 33, v16\n"
  "v_cmp_gt_i32 s[44:45], 34, v16\n"
  "v_cmp_gt_i32 s[46:47], 35, v16\n"
  "v_cndmask_b32 v208, v208, v17, s[40:41]\n"
  "v_cndmask_b32 v209, v209, v17, s[42:43]\n"
  "v_cndmask_b32 v210, v210, v17, s[44:45]\n"
  "v_cndmask_b32 v211, v211, v17, s[46:47]\n"
  "v_cmp_gt_i32 s[40:41], 40, v16\n"
  "v_cmp_gt_i32 s[42:43], 41, v16\n"
  "v_cmp_gt_i32 s[44:45], 42, v16\n"
  "v_cmp_gt_i32 s[46:47], 43, v16\n"
  "v_cndmask_b32 v212, v212, v17, s[40:41]\n"
  "v_cndmask_b32 v213, v213, v17, s[42:43]\n"
  "v_cndmask_b32 v214, v214, v17, s[44:45]\n"
  "v_cndmask_b32 v215, v215, v17, s[46:47]\n"
  "v_cmp_gt_i32 s[40:41], 48, v16\n"
  "v_cmp_gt_i32 s[42:43], 49, v16\n"
  "v_cmp_gt_i32 s[44:45], 50, v16\n"
  "v_cmp_gt_i32 s[46:47], 51, v16\n"
  "v_cndmask_b32 v216, v216, v17, s[40:41]\n"
  "v_cndmask_b32 v217, v217, v17, s[42:43]\n"
  "v_cndmask_b32 v218, v218, v17, s[44:45]\n"
  "v_cndmask_b32 v219, v219, v17, s[46:47]\n"
  "v_cmp_gt_i32 s[40:41], 56, v16\n"
  "v_cmp_gt_i32 s[42:43], 57, v16\n"
  "v_cmp_gt_i32 s[44:45], 58, v16\n"
  "v_cmp_gt_i32 s[46:47], 59, v16\n"
  "v_cndmask_b32 v220, v220, v17, s[40:41]\n"
  "v_cndmask_b32 v221, v221, v17, s[42:43]\n"
  "v_cndmask_b32 v222, v222, v17, s[44:45]\n"
  "v_cndmask_b32 v223, v223, v17, s[46:47]\n"
  "s_nop 1\n"
  "s_branch MS0_%=_r\n"
  "MS1_%=:\n"
  "s_nop 7\n"
  "s_andn2_b32 s55, s60, 0x3fff\n"
  "s_lshr_b32 s55, s55, 8\n"
  "s_add_i32 s55, s55, -192\n"
  "v_subrev_u32 v16, s55, v21\n"
  "v_mov_b32 v17, 0xff800000\n"
  "v_cmp_gt_i32 s[40:41], 0, v16\n"
  "v_cmp_gt_i32 s[42:43], 1, v16\n"
  "v_cmp_gt_i32 s[44:45], 2, v16\n"
  "v_cmp_gt_i32 s[46:47], 3, v16\n"
  "v_cndmask_b32 v192, v192, v17, s[40:41]\n"
  "v_cndmask_b32 v193, v193, v17, s[42:43]\n"
  "v_cndmask_b32 v194, v194, v17, s[44:45]\n"
  "v_cndmask_b32 v195, v195, v17, s[46:47]\n"
  "v_cmp_gt_i32 s[40:41], 8, v16\n"
  "v_cmp_gt_i32 s[42:43], 9, v16\n"
  "v_cmp_gt_i32 s[44:45], 10, v16\n"
  "v_cmp_gt_i32 s[46:47], 11, v16\n"
  "v_cndmask_b32 v196, v196, v17, s[40:41]\n"
  "v_cndmask_b32 v197, v197, v17, s[42:43]\n"
  "v_cndmask_b32 v198, v198, v17, s[44:45]\n"
  "v_cndmask_b32 v199, v199, v17, s[46:47]\n"
  "v_cmp_gt_i32 s[40:41], 16, v16\n"
  "v_cmp_gt_i32 s[42:43], 17, v16\n"
  "v_cmp_gt_i32 s[44:45], 18, v16\n"
  "v_cmp_gt_i32 s[46:47], 19, v16\n"
  "v_cndmask_b32 v200, v200, v17, s[40:41]\n"
  "v_cndmask_b32 v201, v201, v17, s[42:43]\n"
  "v_cndmask_b32 v202, v202, v17, s[44:45]\n"
  "v_cndmask_b32 v203, v203, v17, s[46:47]\n"
  "v_cmp_gt_i32 s[40:41], 24, v16\n"
  "v_cmp_gt_i32 s[42:43], 25, v16\n"
  "v_cmp_gt_i32 s[44:45], 26, v16\n"
  "v_cmp_gt_i32 s[46:47], 27, v16\n"
  "v_cndmask_b32 v204, v204, v17, s[40:41]\n"
  "v_cndmask_b32 v205, v205, v17, s[42:43]\n"
  "v_cndmask_b32 v206, v206, v17, s[44:45]\n"
  "v_cndmask_b32 v207, v207, v17, s[46:47]\n"
  "v_cmp_gt_i32 s[40:41], 32, v16\n"
  "v_cmp_gt_i32 s[42:43], 33, v16\n"
  "v_cmp_gt_i32 s[44:45], 34, v16\n"
  "v_cmp_gt_i32 s[46:47], 35, v16\n"
  "v_cndmask_b32 v208, v208, v17, s[40:41]\n"
  "v_cndmask_b32 v209, v209, v17, s[42:43]\n"
  "v_cndmask_b32 v210, v210, v17, s[44:45]\n"
  "v_cndmask_b32 v211, v211, v17, s[46:47]\n"
  "v_cmp_gt_i32 s[40:41], 40, v16\n"
  "v_cmp_gt_i32 s[42:43], 41, v16\n"
  "v_cmp_gt_i32 s[44:45], 42, v16\n"
  "v_cmp_gt_i32 s[46:47], 43, v16\n"
  "v_cndmask_b32 v212, v212, v17, s[40:41]\n"
  "v_cndmask_b32 v213, v213, v17, s[42:43]\n"
  "v_cndmask_b32 v214, v214, v17, s[44:45]\n"
  "v_cndmask_b32 v215, v215, v17, s[46:47]\n"
  "v_cmp_gt_i32 s[40:41], 48, v16\n"
  "v_cmp_gt_i32 s[42:43], 49, v16\n"
  "v_cmp_gt_i32 s[44:45], 50, v16\n"
  "v_cmp_gt_i32 s[46:47], 51, v16\n"
  "v_cndmask_b32 v216, v216, v17, s[40:41]\n"
  "v_cndmask_b32 v217, v217, v17, s[42:43]\n"
  "v_cndmask_b32 v218, v218, v17, s[44:45]\n"
  "v_cndmask_b32 v219, v219, v17, s[46:47]\n"
  "v_cmp_gt_i32 s[40:41], 56, v16\n"
  "v_cmp_gt_i32 s[42:43], 57, v16\n"
  "v_cmp_gt_i32 s[44:45], 58, v16\n"
  "v_cmp_gt_i32 s[46:47], 59, v16\n"
  "v_cndmask_b32 v220, v220, v17, s[40:41]\n"
  "v_cndmask_b32 v221, v221, v17, s[42:43]\n"
  "v_cndmask_b32 v222, v222, v17, s[44:45]\n"
  "v_cndmask_b32 v223, v223, v17, s[46:47]\n"
  "s_nop 1\n"
  "s_branch MS1_%=_r\n"
  "LEND_%=:\n"
  : "+{v0}"(v[0]), "+{v1}"(v[1]), "+{v2}"(v[2]), "+{v3}"(v[3]), "+{v4}"(v[4]), "+{v5}"(v[5]), "+{v6}"(v[6]), "+{v7}"(v[7]), "+{v8}"(v[8]), "+{v9}"(v[9]), "+{v10}"(v[10]), "+{v11}"(v[11]), "+{v12}"(v[12]), "+{v13}"(v[13]), "+{v14}"(v[14]), "+{v15}"(v[15]), "+{v16}"(v[16]), "+{v17}"(v[17]), "+{v18}"(v[18]), "+{v19}"(v[19]), "+{v20}"(v[20]), "+{v21}"(v[21]), "+{v22}"(v[22]), "+{v23}"(v[23]), "+{v24}"(v[24]), "+{v25}"(v[25]), "+{v26}"(v[26]), "+{v27}"(v[27]), "+{v28}"(v[28]), "+{v29}"(v[29]), "+{v30}"(v[30]), "+{s90}"(s90), "+{s91}"(s91), "+{s95}"(s95), "+{s20}"(s20), "+{s21}"(s21), "+{s22}"(s22), "+{s23}"(s23), "+{s52}"(s52), "+{s53}"(s53), "+{s58}"(s58), "+{s62}"(s62), "+{s63}"(s63), "+{s64}"(s64), "+{s72}"(s72), "+{s73}"(s73), "+{s74}"(s74), "+{s75}"(s75), "+{s76}"(s76), "+{s77}"(s77), "+{s78}"(s78), "+{s79}"(s79), "+{s[24:27]}"(d24), "+{s[28:31]}"(d28), "+{s[32:35]}"(d32), "+{s[36:39]}"(d36), "+{s[40:43]}"(d40), "+{s[44:47]}"(d44), "+{s[48:51]}"(d48), "={s[70:71]}"(p70), "={s[14:15]}"(p14), "+{s[88:89]}"(p88), "={a[0:31]}"(O0), "={a[32:63]}"(O1), "={a[64:95]}"(O2), "+{a[96:127]}"(O3)
  :
  : "v31","v32","v33","v34","v35","v36","v37","v38","v39","v40","v41","v42","v43","v44","v45","v46","v47","v48","v49","v50","v51","v52","v53","v54","v55","v56","v57","v58","v59","v60","v61","v62","v63","v64","v65","v66","v67","v68","v69","v70","v71","v72","v73","v74","v75","v76","v77","v78","v79","v80","v81","v82","v83","v84","v85","v86","v87","v88","v89","v90","v91","v92","v93","v94","v95","v96","v97","v98","v99","v100","v101","v102","v103","v104","v105","v106","v107","v108","v109","v110","v111","v112","v113","v114","v115","v116","v117","v118","v119","v120","v121","v122","v123","v124","v125","v126","v127","v128","v129","v130","v131","v132","v133","v134","v135","v136","v137","v138","v139","v140","v141","v142","v143","v144","v145","v146","v147","v148","v149","v150","v151","v152","v153","v154","v155","v156","v157","v158","v159","v160","v161","v162","v163","v164","v165","v166","v167","v168","v169","v170","v171","v172","v173","v174","v175","v176","v177","v178","v179","v180","v181","v182","v183","v184","v185","v186","v187","v188","v189","v190","v191","v192","v193","v194","v195","v196","v197","v198","v199","v200","v201","v202","v203","v204","v205","v206","v207","v208","v209","v210","v211","v212","v213","v214","v215","v216","v217","v218","v219","v220","v221","v222","v223","v224","v225","v226","v227","v228","v229","v230","v231","v232","v233","v234","v235","v236","v237","v238","v239","v240","v241","v242","v243","v244","v245","v246","v247","v248","v249","v250","v251","v252","v253","v254","v255","a128","a129","a130","a131","a132","a133","a134","a135","a136","a137","a138","a139","a140","a141","a142","a143","a144","a145","a146","a147","a148","a149","a150","a151","a152","a153","a154","a155","a156","a157","a158","a159","a160","a161","a162","a163","a164","a165","a166","a167","a168","a169","a170","a171","a172","a173","a174","a175","a176","a177","a178","a179","a180","a181","a182","a183","a184","a185","a186","a187","a188","a189","a190","a191","a192","a193","a194","a195","a196","a197","a198","a199","a200","a201","a202","a203","a204","a205","a206","a207","a208","a209","a210","a211","a212","a213","a214","a215","a216","a217","a218","a219","a220","a221","a222","a223","a224","a225","a226","a227","a228","a229","a230","a231","a232","a233","a234","a235","a236","a237","a238","a239","a240","a241","a242","a243","a244","a245","a246","a247","a248","a249","a250","a251","a252","a253","a254","a255","s12","s13","s16","s17","s18","s19","s54","s55","s56","s57","s59","s60","s61","s65","s66","s67","s68","s69","s80","s81","s82","s83","s84","s85","s86","s87","vcc","scc","memory");
  tag = rfl64(p70); ka = (KArg)rfl64(p88); w = rfl(s53) >> 12; b = rfl(s79) >> 14; bx = (const char*)p14; ws = (char*)(d36[0] | ((unsigned long long)d36[1] << 32)); }
  asm volatile("" : "+s"(w), "+s"(b), "+s"(ka));
  l = lane_id();
  unsigned s62 = w * 4096, s63 = w * 5120;
  u32x4 d52 = srd(bx, 0, 2048 * 1280), d56;
  {
  IDS
  for (int i = 0; i < 5; i++) { unsigned row = 40 * w + 8 * i + (l >> 3); v[23 + i] = (128 * rb - 32 + row) * 1280 + (((l & 7) ^ ((row >> 1) & 7)) << 4) - 1024 * (i & 3); }
  v[28] = w * 4096 + l * 16; d56 = srd(ws, OFF_W + g * 163840, 163840);
  asm volatile(
  "s_nop 4\n"
  "s_mov_b32 s80, 0x0\n"
  "s_mov_b32 s81, 0x0\n"
  "s_add_u32 m0, s62, 0x5000\n"
  "s_nop 0\n"
  "buffer_load_dwordx4 v28, s[56:59], s81 offen offset:0 sc1 lds\n"
  "buffer_load_dwordx4 v28, s[56:59], s81 offen offset:1024 sc1 lds\n"
  "buffer_load_dwordx4 v28, s[56:59], s81 offen offset:2048 sc1 lds\n"
  "buffer_load_dwordx4 v28, s[56:59], s81 offen offset:3072 sc1 lds\n"
  "s_add_u32 m0, s63, 0x0\n"
  "s_nop 0\n"
  "buffer_load_dwordx4 v23, s[52:55], s80 offen offset:0 lds\n"
  "buffer_load_dwordx4 v24, s[52:55], s80 offen offset:1024 lds\n"
  "buffer_load_dwordx4 v25, s[52:55], s80 offen offset:2048 lds\n"
  "buffer_load_dwordx4 v26, s[52:55], s80 offen offset:3072 lds\n"
  "s_add_u32 m0, s63, 0x1000\n"
  "s_nop 0\n"
  "buffer_load_dwordx4 v27, s[52:55], s80 offen offset:0 lds\n"
  "s_mov_b32 s80, 0x80\n"
  "s_mov_b32 s81, 0x4000\n"
  "s_add_u32 m0, s62, 0xe000\n"
  "s_nop 0\n"
  "buffer_load_dwordx4 v28, s[56:59], s81 offen offset:0 sc1 lds\n"
  "buffer_load_dwordx4 v28, s[56:59], s81 offen offset:1024 sc1 lds\n"
  "buffer_load_dwordx4 v28, s[56:59], s81 offen offset:2048 sc1 lds\n"
  "buffer_load_dwordx4 v28, s[56:59], s81 offen offset:3072 sc1 lds\n"
  "s_add_u32 m0, s63, 0x9000\n"
  "s_nop 0\n"
  "buffer_load_dwordx4 v23, s[52:55], s80 offen offset:0 lds\n"
  "buffer_load_dwordx4 v24, s[52:55], s80 offen offset:1024 lds\n"
  "buffer_load_dwordx4 v25, s[52:55], s80 offen offset:2048 lds\n"
  "buffer_load_dwordx4 v26, s[52:55], s80 offen offset:3072 lds\n"
  "s_add_u32 m0, s63, 0xa000\n"
  "s_nop 0\n"
  "buffer_load_dwordx4 v27, s[52:55], s80 offen offset:0 lds\n"
  : "+{v23}"(v[23]), "+{v24}"(v[24]), "+{v25}"(v[25]), "+{v26}"(v[26]), "+{v27}"(v[27]), "+{v28}"(v[28]), "+v"(l)
  : "{s62}"(s62), "{s63}"(s63), "{s[52:55]}"(d52), "{s[56:59]}"(d56)
  : "s80", "s81", "scc", "memory");
  }
  asm volatile("" : "+s"(w), "+s"(b), "+s"(ka));
  asm volatile("s_load_dwordx2 %0, %4, 0x28\n\ts_load_dwordx2 %1, %4, 0x40\n\ts_load_dwordx2 %2, %4, 0x48\n\ts_load_dwordx2 %3, %4, 0x50\n\ts_waitcnt lgkmcnt(0)"
               : "=&s"(wo), "=&s"(wob), "=&s"(ws), "=&s"(out) : "s"(ka));
  {
  IDS
  l &= 63;
  const unsigned n = s < 3 ? 19 : 64 - 3 * 19, st0 = 19 * s, r3 = rt * 4 + w;
  const unsigned r = l & 31, hf = l >> 5, q = (l >> 2) & 3, p = l & 3, g1 = (l >> 4) & 1;
  const unsigned a0 = r * 128 + ((hf ^ ((r >> 1) & 7)) << 4), w0 = (8 * hf + q) * 256 + g1 * 32 + p * 8;
  for (int i = 0; i < 4; i++) v[18 + i] = a0 ^ (i * 32);
  v[22] = 20480 + w0 + ((w ^ q) << 6);
  v[1] = 65536 + (4 * hf + q) * 320 + g1 * 32 + p * 8; v[0] = v[1] + 61440; v[2] = l * 16;
  for (int i = 0; i < 5; i++) { unsigned t = i * 64 + l; v[3 + i] = (16 * w + t / 20) * 1280 + ct * 320 + (t % 20) * 16 - 1024 * (i & 3); }
  v[8] = (4 * hf + (r & 1)) * 2560 + (r & 30) * 2; v[29] = (r & 1) ? 0x03020706u : 0x05040100u; v[9] = v[8] + 5120;
  const unsigned ri = (w & 1) * 64 + l, hh = w >> 1, pos = rb * 128 + ri;
  v[13] = pos; v[16] = (18 + ri) * 368 + 48 * hh; v[17] = (18 + ri) * 368 + 192 + 80 * hh;
  unsigned aw[4], ao[5];
  for (int i = 0; i < 4; i++) { const unsigned c0 = 32 * w + 8 * i + 4 * hf; aw[i] = r * 368 + min(max(int(2 * c0 + 8 * ((c0 * 205) >> 12)), int(4 * c0) - 128), 352); }
  for (int i = 0; i < 5; i++) { const unsigned k = (2 * g + hh) * 20 + 4 * i; ao[i] = OFF_O - OFF_CTX + (pos >> 5) * 40960 + (pos & 31) * 16 + (k >> 4) * 1024 + ((k >> 2) & 1) * 512 + ((k >> 3) & 1) * 8; }
  unsigned a224 = aw[0], a225 = aw[1], a226 = aw[2], a227 = aw[3], a228 = ao[0], a229 = ao[1], a230 = ao[2], a231 = ao[3], a232 = ao[4];
  u32x4 d20 = srd(ws, OFF_CTX + r3 * 262144 + st0 * 4096, n * 4096), d24 = srd(wo, st0 * 81920, n * 81920);
  u32x4 d28 = srd(ws, OFF_O + r3 * 40960, 40960), d32 = srd(wob, 0, 819200), d36 = srd(ws, OFF_P + tile * 147456, 147456), d40 = srd(out, 0, 2048 * 2560);
  u32x4 d44 = srd(ws, OFF_F, F_END), d48 = srd(ws, OFF_CTX, OFF_P - OFF_CTX);
  unsigned long long p60 = tag;
  unsigned s64 = 65536 + w * 5120, s65 = n, s66 = s, s67 = w * 9216, s68 = s * 36864 + w * 9216;
  unsigned s69 = (r3 * 32 + 8 * s) * 2560 + ct * 320, s70 = r3 * 32 * 2560 + 1280 + ct * 320, s71 = (rh >> 5) * 262144 + h * 8192;
  unsigned s73 = w == 0 ? F_CTX + (j * 32 + h) * 8 : NOWHERE, s74 = w == 0 ? F_O + (rb * 16 + g) * 8 : NOWHERE;
  unsigned s76 = F_CTX + j * 256, s77 = F_O + rt * 128, s88 = (b | w) == 0 ? 0 : NOWHERE;
  asm volatile(
  "v_mov_b32 v12, v14\n"
  "s_nop 1\n"
  "v_permlane32_swap_b32 v12, v14\n"
  "v_add_f32 v12, v12, v14\n"
  "v_rcp_f32 v12, v12\n"
  "s_waitcnt vmcnt(9)\n"
  "s_barrier\n"
  "ds_read_b64_tr_b16 a[196:197], v22 offset:0\n"
  "ds_read_b64_tr_b16 a[198:199], v22 offset:1024\n"
  "ds_read_b128 a[176:179], v18 offset:0\n"
  "ds_read_b128 a[180:183], v18 offset:4096\n"
  "ds_read_b128 a[184:187], v18 offset:8192\n"
  "ds_read_b128 a[188:191], v18 offset:12288\n"
  "ds_read_b128 a[192:195], v18 offset:16384\n"
  "s_waitcnt lgkmcnt(4)\n"
  "v_mfma_f32_32x32x16_bf16 v[128:143], a[196:199], a[176:179], 0\n"
  "ds_read_b64_tr_b16 a[220:221], v22 offset:4096\n"
  "ds_read_b64_tr_b16 a[222:223], v22 offset:5120\n"
  "v_accvgpr_read_b32 v32, a0\n"
  "v_accvgpr_read_b32 v33, a1\n"
  "v_accvgpr_read_b32 v34, a2\n"
  "v_accvgpr_read_b32 v35, a3\n"
  "s_waitcnt lgkmcnt(5)\n"
  "v_mfma_f32_32x32x16_bf16 v[144:159], a[196:199], a[180:183], 0\n"
  "s_waitcnt lgkmcnt(2)\n"
  "s_mov_b32 s80, 0x100\n"
  "s_mov_b32 s81, 0x8000\n"
  "s_add_u32 m0, s62, 0x17000\n"
  "s_nop 0\n"
  "buffer_load_dwordx4 v28, s[56:59], s81 offen offset:0 sc1 lds\n"
  "ds_read_b128 a[200:203], v19 offset:0\n"
  "ds_read_b128 a[204:207], v19 offset:4096\n"
  "v_accvgpr_read_b32 v36, a4\n"
  "v_accvgpr_read_b32 v37, a5\n"
  "v_accvgpr_read_b32 v38, a6\n"
  "v_mfma_f32_32x32x16_bf16 v[160:175], a[196:199], a[184:187], 0\n"
  "buffer_load_dwordx4 v28, s[56:59], s81 offen offset:1024 sc1 lds\n"
  "ds_read_b128 a[208:211], v19 offset:8192\n"
  "ds_read_b128 a[212:215], v19 offset:12288\n"
  "v_accvgpr_read_b32 v39, a7\n"
  "v_pk_mul_f32 v[32:33], v[32:33], v[12:13] op_sel_hi:[1,0]\n"
  "v_pk_mul_f32 v[34:35], v[34:35], v[12:13] op_sel_hi:[1,0]\n"
  "v_mfma_f32_32x32x16_bf16 v[176:191], a[196:199], a[188:191], 0\n"
  "ds_read_b128 a[216:219], v19 offset:16384\n"
  "v_pk_mul_f32 v[36:37], v[36:37], v[12:13] op_sel_hi:[1,0]\n"
  "v_pk_mul_f32 v[38:39], v[38:39], v[12:13] op_sel_hi:[1,0]\n"
  "v_cvt_pk_bf16_f32 v32, v32, v33\n"
  "v_mfma_f32_32x32x16_bf16 v[192:207], a[196:199], a[192:195], 0\n"
  "buffer_load_dwordx4 v28, s[56:59], s81 offen offset:2048 sc1 lds\n"
  "v_cvt_pk_bf16_f32 v33, v34, v35\n"
  "v_cvt_pk_bf16_f32 v34, v36, v37\n"
  "v_cvt_pk_bf16_f32 v35, v38, v39\n"
  "buffer_store_dwordx4 v[32:35], v2, s[48:51], s71 offen offset:0 sc1\n"
  "s_waitcnt lgkmcnt(3)\n"
  "v_mfma_f32_32x32x16_bf16 v[128:143], a[220:223], a[200:203], v[128:143]\n"
  "ds_read_b64_tr_b16 a[196:197], v22 offset:8192\n"
  "ds_read_b64_tr_b16 a[198:199], v22 offset:9216\n"
  "v_accvgpr_read_b32 v40, a8\n"
  "v_accvgpr_read_b32 v41, a9\n"
  "v_accvgpr_read_b32 v42, a10\n"
  "v_accvgpr_read_b32 v43, a11\n"
  "v_mfma_f32_32x32x16_bf16 v[144:159], a[220:223], a[204:207], v[144:159]\n"
  "s_waitcnt lgkmcnt(2)\n"
  "buffer_load_dwordx4 v28, s[56:59], s81 offen offset:3072 sc1 lds\n"
  "ds_read_b128 a[176:179], v20 offset:0\n"
  "ds_read_b128 a[180:183], v20 offset:4096\n"
  "v_accvgpr_read_b32 v44, a12\n"
  "v_accvgpr_read_b32 v45, a13\n"
  "v_accvgpr_read_b32 v46, a14\n"
  "v_mfma_f32_32x32x16_bf16 v[160:175], a[220:223], a[208:211], v[160:175]\n"
  "s_add_u32 m0, s63, 0x12000\n"
  "s_nop 0\n"
  "buffer_load_dwordx4 v23, s[52:55], s80 offen offset:0 lds\n"
  "ds_read_b128 a[184:187], v20 offset:8192\n"
  "ds_read_b128 a[188:191], v20 offset:12288\n"
  "v_accvgpr_read_b32 v47, a15\n"
  "v_pk_mul_f32 v[40:41], v[40:41], v[12:13] op_sel_hi:[1,0]\n"
  "v_pk_mul_f32 v[42:43], v[42:43], v[12:13] op_sel_hi:[1,0]\n"
  "v_mfma_f32_32x32x16_bf16 v[176:191], a[220:223], a[212:215], v[176:191]\n"
  "ds_read_b128 a[192:195], v20 offset:16384\n"
  "v_pk_mul_f32 v[44:45], v[44:45], v[12:13] op_sel_hi:[1,0]\n"
  "v_pk_mul_f32 v[46:47], v[46:47], v[12:13] op_sel_hi:[1,0]\n"
  "v_cvt_pk_bf16_f32 v40, v40, v41\n"
  "v_mfma_f32_32x32x16_bf16 v[192:207], a[220:223], a[216:219], v[192:207]\n"
  "buffer_load_dwordx4 v24, s[52:55], s80 offen offset:1024 lds\n"
  "v_cvt_pk_bf16_f32 v41, v42, v43\n"
  "v_cvt_pk_bf16_f32 v42, v44, v45\n"
  "v_cvt_pk_bf16_f32 v43, v46, v47\n"
  "buffer_store_dwordx4 v[40:43], v2, s[48:51], s71 offen offset:1024 sc1\n"
  "s_waitcnt lgkmcnt(3)\n"
  "v_mfma_f32_32x32x16_bf16 v[128:143], a[196:199], a[176:179], v[128:143]\n"
  "ds_read_b64_tr_b16 a[220:221], v22 offset:12288\n"
  "ds_read_b64_tr_b16 a[222:223], v22 offset:13312\n"
  "v_accvgpr_read_b32 v48, a16\n"
  "v_accvgpr_read_b32 v49, a17\n"
  "v_accvgpr_read_b32 v50, a18\n"
  "v_accvgpr_read_b32 v51, a19\n"
  "v_mfma_f32_32x32x16_bf16 v[144:159], a[196:199], a[180:183], v[144:159]\n"
  "s_waitcnt lgkmcnt(2)\n"
  "buffer_load_dwordx4 v25, s[52:55], s80 offen offset:2048 lds\n"
  "ds_read_b128 a[200:203], v21 offset:0\n"
  "ds_read_b128 a[204:207], v21 offset:4096\n"
  "v_accvgpr_read_b32 v52, a20\n"
  "v_accvgpr_read_b32 v53, a21\n"
  "v_accvgpr_read_b32 v54, a22\n"
  "v_mfma_f32_32x32x16_bf16 v[160:175], a[196:199], a[184:187], v[160:175]\n"
  "buffer_load_dwordx4 v26, s[52:55], s80 offen offset:3072 lds\n"
  "ds_read_b128 a[208:211], v21 offset:8192\n"
  "ds_read_b128 a[212:215], v21 offset:12288\n"
  "v_accvgpr_read_b32 v55, a23\n"
  "v_pk_mul_f32 v[48:49], v[48:49], v[12:13] op_sel_hi:[1,0]\n"
  "v_pk_mul_f32 v[50:51], v[50:51], v[12:13] op_sel_hi:[1,0]\n"
  "v_mfma_f32_32x32x16_bf16 v[176:191], a[196:199], a[188:191], v[176:191]\n"
  "ds_read_b128 a[216:219], v21 offset:16384\n"
  "v_pk_mul_f32 v[52:53], v[52:53], v[12:13] op_sel_hi:[1,0]\n"
  "v_pk_mul_f32 v[54:55], v[54:55], v[12:13] op_sel_hi:[1,0]\n"
  "v_cvt_pk_bf16_f32 v48, v48, v49\n"
  "v_mfma_f32_32x32x16_bf16 v[192:207], a[196:199], a[192:195], v[192:207]\n"
  "s_add_u32 m0, s63, 0x13000\n"
  "s_nop 0\n"
  "buffer_load_dwordx4 v27, s[52:55], s80 offen offset:0 lds\n"
  "v_cvt_pk_bf16_f32 v49, v50, v51\n"
  "v_cvt_pk_bf16_f32 v50, v52, v53\n"
  "v_cvt_pk_bf16_f32 v51, v54, v55\n"
  "buffer_store_dwordx4 v[48:51], v2, s[48:51], s71 offen offset:2048 sc1\n"
  "s_waitcnt lgkmcnt(3)\n"
  "v_mfma_f32_32x32x16_bf16 v[128:143], a[220:223], a[200:203], v[128:143]\n"
  "s_waitcnt vmcnt(12) lgkmcnt(0)\n"
  "s_barrier\n"
  "v_add_u32 v18, 0x9000, v18\n"
  "v_add_u32 v22, 0x9000, v22\n"
  "ds_read_b64_tr_b16 a[196:197], v22 offset:0\n"
  "ds_read_b64_tr_b16 a[198:199], v22 offset:1024\n"
  "v_accvgpr_read_b32 v56, a24\n"
  "v_accvgpr_read_b32 v57, a25\n"
  "v_accvgpr_read_b32 v58, a26\n"
  "v_accvgpr_read_b32 v59, a27\n"
  "v_mfma_f32_32x32x16_bf16 v[144:159], a[220:223], a[204:207], v[144:159]\n"
  "s_mov_b32 s80, 0x180\n"
  "s_mov_b32 s81, 0xc000\n"
  "s_add_u32 m0, s62, 0x20000\n"
  "s_nop 0\n"
  "buffer_load_dwordx4 v28, s[56:59], s81 offen offset:0 sc1 lds\n"
  "ds_read_b128 a[176:179], v18 offset:0\n"
  "ds_read_b128 a[180:183], v18 offset:4096\n"
  "v_accvgpr_read_b32 v60, a28\n"
  "v_accvgpr_read_b32 v61, a29\n"
  "v_accvgpr_read_b32 v62, a30\n"
  "v_mfma_f32_32x32x16_bf16 v[160:175], a[220:223], a[208:211], v[160:175]\n"
  "buffer_load_dwordx4 v28, s[56:59], s81 offen offset:1024 sc1 lds\n"
  "ds_read_b128 a[184:187], v18 offset:8192\n"
  "ds_read_b128 a[188:191], v18 offset:12288\n"
  "v_accvgpr_read_b32 v63, a31\n"
  "v_pk_mul_f32 v[56:57], v[56:57], v[12:13] op_sel_hi:[1,0]\n"
  "v_pk_mul_f32 v[58:59], v[58:59], v[12:13] op_sel_hi:[1,0]\n"
  "v_mfma_f32_32x32x16_bf16 v[176:191], a[220:223], a[212:215], v[176:191]\n"
  "ds_read_b128 a[192:195], v18 offset:16384\n"
  "v_add_u32 v19, 0x9000, v19\n"
  "v_add_u32 v20, 0x9000, v20\n"
  "v_add_u32 v21, 0x9000, v21\n"
  "v_pk_mul_f32 v[60:61], v[60:61], v[12:13] op_sel_hi:[1,0]\n"
  "v_pk_mul_f32 v[62:63], v[62:63], v[12:13] op_sel_hi:[1,0]\n"
  "v_cvt_pk_bf16_f32 v56, v56, v57\n"
  "v_mfma_f32_32x32x16_bf16 v[192:207], a[220:223], a[216:219], v[192:207]\n"
  "buffer_load_dwordx4 v28, s[56:59], s81 offen offset:2048 sc1 lds\n"
  "v_cvt_pk_bf16_f32 v57, v58, v59\n"
  "v_cvt_pk_bf16_f32 v58, v60, v61\n"
  "v_cvt_pk_bf16_f32 v59, v62, v63\n"
  "buffer_store_dwordx4 v[56:59], v2, s[48:51], s71 offen offset:3072 sc1\n"
  "s_waitcnt lgkmcnt(3)\n"
  "v_mfma_f32_32x32x16_bf16 v[128:143], a[196:199], a[176:179], v[128:143]\n"
  "ds_read_b64_tr_b16 a[220:221], v22 offset:4096\n"
  "ds_read_b64_tr_b16 a[222:223], v22 offset:5120\n"
  "v_accvgpr_read_b32 v64, a32\n"
  "v_accvgpr_read_b32 v65, a33\n"
  "v_accvgpr_read_b32 v66, a34\n"
  "v_accvgpr_read_b32 v67, a35\n"
  "v_mfma_f32_32x32x16_bf16 v[144:159], a[196:199], a[180:183], v[144:159]\n"
  "s_waitcnt lgkmcnt(2)\n"
  "buffer_load_dwordx4 v28, s[56:59], s81 offen offset:3072 sc1 lds\n"
  "ds_read_b128 a[200:203], v19 offset:0\n"
  "ds_read_b128 a[204:207], v19 offset:4096\n"
  "v_accvgpr_read_b32 v68, a36\n"
  "v_accvgpr_read_b32 v69, a37\n"
  "v_accvgpr_read_b32 v70, a38\n"
  "v_mfma_f32_32x32x16_bf16 v[160:175], a[196:199], a[184:187], v[160:175]\n"
  "s_add_u32 m0, s63, 0x1b000\n"
  "s_nop 0\n"
  "buffer_load_dwordx4 v23, s[52:55], s80 offen offset:0 lds\n"
  "ds_read_b128 a[208:211], v19 offset:8192\n"
  "ds_read_b128 a[212:215], v19 offset:12288\n"
  "v_accvgpr_read_b32 v71, a39\n"
  "v_pk_mul_f32 v[64:65], v[64:65], v[12:13] op_sel_hi:[1,0]\n"
  "v_pk_mul_f32 v[66:67], v[66:67], v[12:13] op_sel_hi:[1,0]\n"
  "v_mfma_f32_32x32x16_bf16 v[176:191], a[196:199], a[188:191], v[176:191]\n"
  "ds_read_b128 a[216:219], v19 offset:16384\n"
  "v_pk_mul_f32 v[68:69], v[68:69], v[12:13] op_sel_hi:[1,0]\n"
  "v_pk_mul_f32 v[70:71], v[70:71], v[12:13] op_sel_hi:[1,0]\n"
  "v_cvt_pk_bf16_f32 v64, v64, v65\n"
  "v_mfma_f32_32x32x16_bf16 v[192:207], a[196:199], a[192:195], v[192:207]\n"
  "buffer_load_dwordx4 v24, s[52:55], s80 offen offset:1024 lds\n"
  "s_add_u32 s71, s71, 0x1000\n"
  "v_cvt_pk_bf16_f32 v65, v66, v67\n"
  "v_cvt_pk_bf16_f32 v66, v68, v69\n"
  "v_cvt_pk_bf16_f32 v67, v70, v71\n"
  "buffer_store_dwordx4 v[64:67], v2, s[48:51], s71 offen offset:0 sc1\n"
  "s_waitcnt lgkmcnt(3)\n"
  "v_mfma_f32_32x32x16_bf16 v[128:143], a[220:223], a[200:203], v[128:143]\n"
  "ds_read_b64_tr_b16 a[196:197], v22 offset:8192\n"
  "ds_read_b64_tr_b16 a[198:199], v22 offset:9216\n"
  "v_accvgpr_read_b32 v72, a40\n"
  "v_accvgpr_read_b32 v73, a41\n"
  "v_accvgpr_read_b32 v74, a42\n"
  "v_accvgpr_read_b32 v75, a43\n"
  "v_mfma_f32_32x32x16_bf16 v[144:159], a[220:223], a[204:207], v[144:159]\n"
  "s_waitcnt lgkmcnt(2)\n"
  "buffer_load_dwordx4 v25, s[52:55], s80 offen offset:2048 lds\n"
  "ds_read_b128 a[176:179], v20 offset:0\n"
  "ds_read_b128 a[180:183], v20 offset:4096\n"
  "v_accvgpr_read_b32 v76, a44\n"
  "v_accvgpr_read_b32 v77, a45\n"
  "v_accvgpr_read_b32 v78, a46\n"
  "v_mfma_f32_32x32x16_bf16 v[160:175], a[220:223], a[208:211], v[160:175]\n"
  "buffer_load_dwordx4 v26, s[52:55], s80 offen offset:3072 lds\n"
  "ds_read_b128 a[184:187], v20 offset:8192\n"
  "ds_read_b128 a[188:191], v20 offset:12288\n"
  "v_accvgpr_read_b32 v79, a47\n"
  "v_pk_mul_f32 v[72:73], v[72:73], v[12:13] op_sel_hi:[1,0]\n"
  "v_pk_mul_f32 v[74:75], v[74:75], v[12:13] op_sel_hi:[1,0]\n"
  "v_mfma_f32_32x32x16_bf16 v[176:191], a[220:223], a[212:215], v[176:191]\n"
  "ds_read_b128 a[192:195], v20 offset:16384\n"
  "v_pk_mul_f32 v[76:77], v[76:77], v[12:13] op_sel_hi:[1,0]\n"
  "v_pk_mul_f32 v[78:79], v[78:79], v[12:13] op_sel_hi:[1,0]\n"
  "v_cvt_pk_bf16_f32 v72, v72, v73\n"
  "v_mfma_f32_32x32x16_bf16 v[192:207], a[220:223], a[216:219], v[192:207]\n"
  "s_add_u32 m0, s63, 0x1c000\n"
  "s_nop 0\n"
  "buffer_load_dwordx4 v27, s[52:55], s80 offen offset:0 lds\n"
  "v_cvt_pk_bf16_f32 v73, v74, v75\n"
  "v_cvt_pk_bf16_f32 v74, v76, v77\n"
  "v_cvt_pk_bf16_f32 v75, v78, v79\n"
  "buffer_store_dwordx4 v[72:75], v2, s[48:51], s71 offen offset:1024 sc1\n"
  "s_waitcnt lgkmcnt(3)\n"
  "v_mfma_f32_32x32x16_bf16 v[128:143], a[196:199], a[176:179], v[128:143]\n"
  "ds_read_b64_tr_b16 a[220:221], v22 offset:12288\n"
  "ds_read_b64_tr_b16 a[222:223], v22 offset:13312\n"
  "v_accvgpr_read_b32 v80, a48\n"
  "v_accvgpr_read_b32 v81, a49\n"
  "v_accvgpr_read_b32 v82, a50\n"
  "v_accvgpr_read_b32 v83, a51\n"
  "v_mfma_f32_32x32x16_bf16 v[144:159], a[196:199], a[180:183], v[144:159]\n"
  "s_waitcnt lgkmcnt(2)\n"
  "s_mov_b32 s80, 0x200\n"
  "s_mov_b32 s81, 0x10000\n"
  "s_add_u32 m0, s62, 0x5000\n"
  "s_nop 0\n"
  "buffer_load_dwordx4 v28, s[56:59], s81 offen offset:0 sc1 lds\n"
  "ds_read_b128 a[200:203], v21 offset:0\n"
  "ds_read_b128 a[204:207], v21 offset:4096\n"
  "v_accvgpr_read_b32 v84, a52\n"
  "v_accvgpr_read_b32 v85, a53\n"
  "v_accvgpr_read_b32 v86, a54\n"
  "v_mfma_f32_32x32x16_bf16 v[160:175], a[196:199], a[184:187], v[160:175]\n"
  "buffer_load_dwordx4 v28, s[56:59], s81 offen offset:1024 sc1 lds\n"
  "ds_read_b128 a[208:211], v21 offset:8192\n"
  "ds_read_b128 a[212:215], v21 offset:12288\n"
  "v_accvgpr_read_b32 v87, a55\n"
  "v_pk_mul_f32 v[80:81], v[80:81], v[12:13] op_sel_hi:[1,0]\n"
  "v_pk_mul_f32 v[82:83], v[82:83], v[12:13] op_sel_hi:[1,0]\n"
  "v_mfma_f32_32x32x16_bf16 v[176:191], a[196:199], a[188:191], v[176:191]\n"
  "ds_read_b128 a[216:219], v21 offset:16384\n"
  "v_pk_mul_f32 v[84:85], v[84:85], v[12:13] op_sel_hi:[1,0]\n"
  "v_pk_mul_f32 v[86:87], v[86:87], v[12:13] op_sel_hi:[1,0]\n"
  "v_cvt_pk_bf16_f32 v80, v80, v81\n"
  "v_mfma_f32_32x32x16_bf16 v[192:207], a[196:199], a[192:195], v[192:207]\n"
  "buffer_load_dwordx4 v28, s[56:59], s81 offen offset:2048 sc1 lds\n"
  "v_cvt_pk_bf16_f32 v81, v82, v83\n"
  "v_cvt_pk_bf16_f32 v82, v84, v85\n"
  "v_cvt_pk_bf16_f32 v83, v86, v87\n"
  "buffer_store_dwordx4 v[80:83], v2, s[48:51], s71 offen offset:2048 sc1\n"
  "s_waitcnt lgkmcnt(3)\n"
  "v_mfma_f32_32x32x16_bf16 v[128:143], a[220:223], a[200:203], v[128:143]\n"
  "s_waitcnt vmcnt(17) lgkmcnt(0)\n"
  "s_barrier\n"
  "v_add_u32 v18, 0x9000, v18\n"
  "v_add_u32 v22, 0x9000, v22\n"
  "ds_read_b64_tr_b16 a[196:197], v22 offset:0\n"
  "ds_read_b64_tr_b16 a[198:199], v22 offset:1024\n"
  "v_accvgpr_read_b32 v88, a56\n"
  "v_accvgpr_read_b32 v89, a57\n"
  "v_accvgpr_read_b32 v90, a58\n"
  "v_accvgpr_read_b32 v91, a59\n"
  "v_mfma_f32_32x32x16_bf16 v[144:159], a[220:223], a[204:207], v[144:159]\n"
  "buffer_load_dwordx4 v28, s[56:59], s81 offen offset:3072 sc1 lds\n"
  "ds_read_b128 a[176:179], v18 offset:0\n"
  "ds_read_b128 a[180:183], v18 offset:4096\n"
  "v_accvgpr_read_b32 v92, a60\n"
  "v_accvgpr_read_b32 v93, a61\n"
  "v_accvgpr_read_b32 v94, a62\n"
  "v_mfma_f32_32x32x16_bf16 v[160:175], a[220:223], a[208:211], v[160:175]\n"
  "s_add_u32 m0, s63, 0x0\n"
  "s_nop 0\n"
  "buffer_load_dwordx4 v23, s[52:55], s80 offen offset:0 lds\n"
  "ds_read_b128 a[184:187], v18 offset:8192\n"
  "ds_read_b128 a[188:191], v18 offset:12288\n"
  "v_accvgpr_read_b32 v95, a63\n"
  "v_pk_mul_f32 v[88:89], v[88:89], v[12:13] op_sel_hi:[1,0]\n"
  "v_pk_mul_f32 v[90:91], v[90:91], v[12:13] op_sel_hi:[1,0]\n"
  "v_mfma_f32_32x32x16_bf16 v[176:191], a[220:223], a[212:215], v[176:191]\n"
  "ds_read_b128 a[192:195], v18 offset:16384\n"
  "v_add_u32 v19, 0x9000, v19\n"
  "v_add_u32 v20, 0x9000, v20\n"
  "v_add_u32 v21, 0x9000, v21\n"
  "v_pk_mul_f32 v[92:93], v[92:93], v[12:13] op_sel_hi:[1,0]\n"
  "v_pk_mul_f32 v[94:95], v[94:95], v[12:13] op_sel_hi:[1,0]\n"
  "v_cvt_pk_bf16_f32 v88, v88, v89\n"
  "v_mfma_f32_32x32x16_bf16 v[192:207], a[220:223], a[216:219], v[192:207]\n"
  "buffer_load_dwordx4 v24, s[52:55], s80 offen offset:1024 lds\n"
  "v_cvt_pk_bf16_f32 v89, v90, v91\n"
  "v_cvt_pk_bf16_f32 v90, v92, v93\n"
  "v_cvt_pk_bf16_f32 v91, v94, v95\n"
  "buffer_store_dwordx4 v[88:91], v2, s[48:51], s71 offen offset:3072 sc1\n"
  "s_waitcnt lgkmcnt(3)\n"
  "v_mfma_f32_32x32x16_bf16 v[128:143], a[196:199], a[176:179], v[128:143]\n"
  "ds_read_b64_tr_b16 a[220:221], v22 offset:4096\n"
  "ds_read_b64_tr_b16 a[222:223], v22 offset:5120\n"
  "v_mfma_f32_32x32x16_bf16 v[144:159], a[196:199], a[180:183], v[144:159]\n"
  "s_waitcnt lgkmcnt(2)\n"
  "buffer_load_dwordx4 v25, s[52:55], s80 offen offset:2048 lds\n"
  "ds_read_b128 a[200:203], v19 offset:0\n"
  "ds_read_b128 a[204:207], v19 offset:4096\n"
  "v_mfma_f32_32x32x16_bf16 v[160:175], a[196:199], a[184:187], v[160:175]\n"
  "buffer_load_dwordx4 v26, s[52:55], s80 offen offset:3072 lds\n"
  "ds_read_b128 a[208:211], v19 offset:8192\n"
  "ds_read_b128 a[212:215], v19 offset:12288\n"
  "v_mfma_f32_32x32x16_bf16 v[176:191], a[196:199], a[188:191], v[176:191]\n"
  "ds_read_b128 a[216:219], v19 offset:16384\n"
  "v_mfma_f32_32x32x16_bf16 v[192:207], a[196:199], a[192:195], v[192:207]\n"
  "s_add_u32 m0, s63, 0x1000\n"
  "s_nop 0\n"
  "buffer_load_dwordx4 v27, s[52:55], s80 offen offset:0 lds\n"
  "s_waitcnt lgkmcnt(3)\n"
  "v_mfma_f32_32x32x16_bf16 v[128:143], a[220:223], a[200:203], v[128:143]\n"
  "ds_read_b64_tr_b16 a[196:197], v22 offset:8192\n"
  "ds_read_b64_tr_b16 a[198:199], v22 offset:9216\n"
  "v_mfma_f32_32x32x16_bf16 v[144:159], a[220:223], a[204:207], v[144:159]\n"
  "s_waitcnt lgkmcnt(2)\n"
  "s_mov_b32 s80, 0x280\n"
  "s_mov_b32 s81, 0x14000\n"
  "s_add_u32 m0, s62, 0xe000\n"
  "s_nop 0\n"
  "buffer_load_dwordx4 v28, s[56:59], s81 offen offset:0 sc1 lds\n"
  "ds_read_b128 a[176:179], v20 offset:0\n"
  "ds_read_b128 a[180:183], v20 offset:4096\n"
  "v_mfma_f32_32x32x16_bf16 v[160:175], a[220:223], a[208:211], v[160:175]\n"
  "buffer_load_dwordx4 v28, s[56:59], s81 offen offset:1024 sc1 lds\n"
  "ds_read_b128 a[184:187], v20 offset:8192\n"
  "ds_read_b128 a[188:191], v20 offset:12288\n"
  "v_mfma_f32_32x32x16_bf16 v[176:191], a[220:223], a[212:215], v[176:191]\n"
  "ds_read_b128 a[192:195], v20 offset:16384\n"
  "v_mfma_f32_32x32x16_bf16 v[192:207], a[220:223], a[216:219], v[192:207]\n"
  "buffer_load_dwordx4 v28, s[56:59], s81 offen offset:2048 sc1 lds\n"
  "s_waitcnt lgkmcnt(3)\n"
  "v_mfma_f32_32x32x16_bf16 v[128:143], a[196:199], a[176:179], v[128:143]\n"
  "ds_read_b64_tr_b16 a[220:221], v22 offset:12288\n"
  "ds_read_b64_tr_b16 a[222:223], v22 offset:13312\n"
  "v_mfma_f32_32x32x16_bf16 v[144:159], a[196:199], a[180:183], v[144:159]\n"
  "s_waitcnt lgkmcnt(2)\n"
  "buffer_load_dwordx4 v28, s[56:59], s81 offen offset:3072 sc1 lds\n"
  "ds_read_b128 a[200:203], v21 offset:0\n"
  "ds_read_b128 a[204:207], v21 offset:4096\n"
  "v_mfma_f32_32x32x16_bf16 v[160:175], a[196:199], a[184:187], v[160:175]\n"
  "s_add_u32 m0, s63, 0x9000\n"
  "s_nop 0\n"
  "buffer_load_dwordx4 v23, s[52:55], s80 offen offset:0 lds\n"
  "ds_read_b128 a[208:211], v21 offset:8192\n"
  "ds_read_b128 a[212:215], v21 offset:12288\n"
  "v_mfma_f32_32x32x16_bf16 v[176:191], a[196:199], a[188:191], v[176:191]\n"
  "ds_read_b128 a[216:219], v21 offset:16384\n"
  "v_mfma_f32_32x32x16_bf16 v[192:207], a[196:199], a[192:195], v[192:207]\n"
  "buffer_load_dwordx4 v24, s[52:55], s80 offen offset:1024 lds\n"
  "s_waitcnt lgkmcnt(3)\n"
  "v_mfma_f32_32x32x16_bf16 v[128:143], a[220:223], a[200:203], v[128:143]\n"
  "s_waitcnt vmcnt(18) lgkmcnt(0)\n"
  "s_waitcnt vmcnt(9)\n"
  "s_barrier\n"
  "v_add_u32 v18, 0x9000, v18\n"
  "v_add_u32 v22, 0x9000, v22\n"
  "ds_read_b64_tr_b16 a[196:197], v22 offset:0\n"
  "ds_read_b64_tr_b16 a[198:199], v22 offset:1024\n"
  "v_mfma_f32_32x32x16_bf16 v[144:159], a[220:223], a[204:207], v[144:159]\n"
  "buffer_load_dwordx4 v25, s[52:55], s80 offen offset:2048 lds\n"
  "ds_read_b128 a[176:179], v18 offset:0\n"
  "ds_read_b128 a[180:183], v18 offset:4096\n"
  "v_mfma_f32_32x32x16_bf16 v[160:175], a[220:223], a[208:211], v[160:175]\n"
  "buffer_load_dwordx4 v26, s[52:55], s80 offen offset:3072 lds\n"
  "ds_read_b128 a[184:187], v18 offset:8192\n"
  "ds_read_b128 a[188:191], v18 offset:12288\n"
  "v_mfma_f32_32x32x16_bf16 v[176:191], a[220:223], a[212:215], v[176:191]\n"
  "ds_read_b128 a[192:195], v18 offset:16384\n"
  "v_add_u32 v19, 0x9000, v19\n"
  "v_add_u32 v20, 0x9000, v20\n"
  "v_add_u32 v21, 0x9000, v21\n"
  "v_mfma_f32_32x32x16_bf16 v[192:207], a[220:223], a[216:219], v[192:207]\n"
  "v_mov_b32 v14, s60\n"
  "v_mov_b32 v15, s61\n"
  "s_mov_b64 exec, 1\n"
  "buffer_store_dwordx2 v[14:15], off, s[44:47], s73 sc1\n"
  "s_mov_b64 exec, -1\n"
  "s_add_u32 m0, s63, 0xa000\n"
  "s_nop 0\n"
  "buffer_load_dwordx4 v27, s[52:55], s80 offen offset:0 lds\n"
  "s_waitcnt lgkmcnt(3)\n"
  "v_mfma_f32_32x32x16_bf16 v[128:143], a[196:199], a[176:179], v[128:143]\n"
  "ds_read_b64_tr_b16 a[220:221], v22 offset:4096\n"
  "ds_read_b64_tr_b16 a[222:223], v22 offset:5120\n"
  "v_mfma_f32_32x32x16_bf16 v[144:159], a[196:199], a[180:183], v[144:159]\n"
  "s_waitcnt lgkmcnt(2)\n"
  "s_mov_b32 s80, 0x300\n"
  "s_mov_b32 s81, 0x18000\n"
  "s_add_u32 m0, s62, 0x17000\n"
  "s_nop 0\n"
  "buffer_load_dwordx4 v28, s[56:59], s81 offen offset:0 sc1 lds\n"
  "ds_read_b128 a[200:203], v19 offset:0\n"
  "ds_read_b128 a[204:207], v19 offset:4096\n"
  "v_mfma_f32_32x32x16_bf16 v[160:175], a[196:199], a[184:187], v[160:175]\n"
  "buffer_load_dwordx4 v28, s[56:59], s81 offen offset:1024 sc1 lds\n"
  "ds_read_b128 a[208:211], v19 offset:8192\n"
  "ds_read_b128 a[212:215], v19 offset:12288\n"
  "v_mfma_f32_32x32x16_bf16 v[176:191], a[196:199], a[188:191], v[176:191]\n"
  "ds_read_b128 a[216:219], v19 offset:16384\n"
  "v_mfma_f32_32x32x16_bf16 v[192:207], a[196:199], a[192:195], v[192:207]\n"
  "buffer_load_dwordx4 v28, s[56:59], s81 offen offset:2048 sc1 lds\n"
  "s_waitcnt lgkmcnt(3)\n"
  "v_mfma_f32_32x32x16_bf16 v[128:143], a[220:223], a[200:203], v[128:143]\n"
  "ds_read_b64_tr_b16 a[196:197], v22 offset:8192\n"
  "ds_read_b64_tr_b16 a[198:199], v22 offset:9216\n"
  "v_mfma_f32_32x32x16_bf16 v[144:159], a[220:223], a[204:207], v[144:159]\n"
  "s_waitcnt lgkmcnt(2)\n"
  "buffer_load_dwordx4 v28, s[56:59], s81 offen offset:3072 sc1 lds\n"
  "ds_read_b128 a[176:179], v20 offset:0\n"
  "ds_read_b128 a[180:183], v20 offset:4096\n"
  "v_mfma_f32_32x32x16_bf16 v[160:175], a[220:223], a[208:211], v[160:175]\n"
  "s_add_u32 m0, s63, 0x12000\n"
  "s_nop 0\n"
  "buffer_load_dwordx4 v23, s[52:55], s80 offen offset:0 lds\n"
  "ds_read_b128 a[184:187], v20 offset:8192\n"
  "ds_read_b128 a[188:191], v20 offset:12288\n"
  "v_mfma_f32_32x32x16_bf16 v[176:191], a[220:223], a[212:215], v[176:191]\n"
  "ds_read_b128 a[192:195], v20 offset:16384\n"
  "v_mfma_f32_32x32x16_bf16 v[192:207], a[220:223], a[216:219], v[192:207]\n"
  "buffer_load_dwordx4 v24, s[52:55], s80 offen offset:1024 lds\n"
  "s_waitcnt lgkmcnt(3)\n"
  "v_mfma_f32_32x32x16_bf16 v[128:143], a[196:199], a[176:179], v[128:143]\n"
  "ds_read_b64_tr_b16 a[220:221], v22 offset:12288\n"
  "ds_read_b64_tr_b16 a[222:223], v22 offset:13312\n"
  "v_mfma_f32_32x32x16_bf16 v[144:159], a[196:199], a[180:183], v[144:159]\n"
  "s_waitcnt lgkmcnt(2)\n"
  "buffer_load_dwordx4 v25, s[52:55], s80 offen offset:2048 lds\n"
  "ds_read_b128 a[200:203], v21 offset:0\n"
  "ds_read_b128 a[204:207], v21 offset:4096\n"
  "v_mfma_f32_32x32x16_bf16 v[160:175], a[196:199], a[184:187], v[160:175]\n"
  "buffer_load_dwordx4 v26, s[52:55], s80 offen offset:3072 lds\n"
  "ds_read_b128 a[208:211], v21 offset:8192\n"
  "ds_read_b128 a[212:215], v21 offset:12288\n"
  "v_mfma_f32_32x32x16_bf16 v[176:191], a[196:199], a[188:191], v[176:191]\n"
  "ds_read_b128 a[216:219], v21 offset:16384\n"
  "v_mfma_f32_32x32x16_bf16 v[192:207], a[196:199], a[192:195], v[192:207]\n"
  "s_add_u32 m0, s63, 0x13000\n"
  "s_nop 0\n"
  "buffer_load_dwordx4 v27, s[52:55], s80 offen offset:0 lds\n"
  "s_waitcnt lgkmcnt(3)\n"
  "v_mfma_f32_32x32x16_bf16 v[128:143], a[220:223], a[200:203], v[128:143]\n"
  "s_waitcnt vmcnt(19) lgkmcnt(0)\n"
  "s_barrier\n"
  "v_add_u32 v18, 0xfffe5000, v18\n"
  "v_add_u32 v22, 0xfffe5000, v22\n"
  "ds_read_b64_tr_b16 a[196:197], v22 offset:0\n"
  "ds_read_b64_tr_b16 a[198:199], v22 offset:1024\n"
  "v_mfma_f32_32x32x16_bf16 v[144:159], a[220:223], a[204:207], v[144:159]\n"
  "s_mov_b32 s80, 0x380\n"
  "s_mov_b32 s81, 0x1c000\n"
  "s_add_u32 m0, s62, 0x20000\n"
  "s_nop 0\n"
  "buffer_load_dwordx4 v28, s[56:59], s81 offen offset:0 sc1 lds\n"
  "ds_read_b128 a[176:179], v18 offset:0\n"
  "ds_read_b128 a[180:183], v18 offset:4096\n"
  "v_mfma_f32_32x32x16_bf16 v[160:175], a[220:223], a[208:211], v[160:175]\n"
  "buffer_load_dwordx4 v28, s[56:59], s81 offen offset:1024 sc1 lds\n"
  "ds_read_b128 a[184:187], v18 offset:8192\n"
  "ds_read_b128 a[188:191], v18 offset:12288\n"
  "v_mfma_f32_32x32x16_bf16 v[176:191], a[220:223], a[212:215], v[176:191]\n"
  "ds_read_b128 a[192:195], v18 offset:16384\n"
  "v_add_u32 v19, 0xfffe5000, v19\n"
  "v_add_u32 v20, 0xfffe5000, v20\n"
  "v_add_u32 v21, 0xfffe5000, v21\n"
  "v_mfma_f32_32x32x16_bf16 v[192:207], a[220:223], a[216:219], v[192:207]\n"
  "buffer_load_dwordx4 v28, s[56:59], s81 offen offset:2048 sc1 lds\n"
  "s_waitcnt lgkmcnt(3)\n"
  "v_mfma_f32_32x32x16_bf16 v[128:143], a[196:199], a[176:179], v[128:143]\n"
  "ds_read_b64_tr_b16 a[220:221], v22 offset:4096\n"
  "ds_read_b64_tr_b16 a[222:223], v22 offset:5120\n"
  "v_mfma_f32_32x32x16_bf16 v[144:159], a[196:199], a[180:183], v[144:159]\n"
  "s_waitcnt lgkmcnt(2)\n"
  "buffer_load_dwordx4 v28, s[56:59], s81 offen offset:3072 sc1 lds\n"
  "ds_read_b128 a[200:203], v19 offset:0\n"
  "ds_read_b128 a[204:207], v19 offset:4096\n"
  "v_mfma_f32_32x32x16_bf16 v[160:175], a[196:199], a[184:187], v[160:175]\n"
  "s_add_u32 m0, s63, 0x1b000\n"
  "s_nop 0\n"
  "buffer_load_dwordx4 v23, s[52:55], s80 offen offset:0 lds\n"
  "ds_read_b128 a[208:211], v19 offset:8192\n"
  "ds_read_b128 a[212:215], v19 offset:12288\n"
  "v_mfma_f32_32x32x16_bf16 v[176:191], a[196:199], a[188:191], v[176:191]\n"
  "ds_read_b128 a[216:219], v19 offset:16384\n"
  "v_mfma_f32_32x32x16_bf16 v[192:207], a[196:199], a[192:195], v[192:207]\n"
  "buffer_load_dwordx4 v24, s[52:55], s80 offen offset:1024 lds\n"
  "s_waitcnt lgkmcnt(3)\n"
  "v_mfma_f32_32x32x16_bf16 v[128:143], a[220:223], a[200:203], v[128:143]\n"
  "ds_read_b64_tr_b16 a[196:197], v22 offset:8192\n"
  "ds_read_b64_tr_b16 a[198:199], v22 offset:9216\n"
  "v_mfma_f32_32x32x16_bf16 v[144:159], a[220:223], a[204:207], v[144:159]\n"
  "s_waitcnt lgkmcnt(2)\n"
  "buffer_load_dwordx4 v25, s[52:55], s80 offen offset:2048 lds\n"
  "ds_read_b128 a[176:179], v20 offset:0\n"
  "ds_read_b128 a[180:183], v20 offset:4096\n"
  "v_mfma_f32_32x32x16_bf16 v[160:175], a[220:223], a[208:211], v[160:175]\n"
  "buffer_load_dwordx4 v26, s[52:55], s80 offen offset:3072 lds\n"
  "ds_read_b128 a[184:187], v20 offset:8192\n"
  "ds_read_b128 a[188:191], v20 offset:12288\n"
  "v_mfma_f32_32x32x16_bf16 v[176:191], a[220:223], a[212:215], v[176:191]\n"
  "ds_read_b128 a[192:195], v20 offset:16384\n"
  "v_mfma_f32_32x32x16_bf16 v[192:207], a[220:223], a[216:219], v[192:207]\n"
  "s_add_u32 m0, s63, 0x1c000\n"
  "s_nop 0\n"
  "buffer_load_dwordx4 v27, s[52:55], s80 offen offset:0 lds\n"
  "s_waitcnt lgkmcnt(3)\n"
  "v_mfma_f32_32x32x16_bf16 v[128:143], a[196:199], a[176:179], v[128:143]\n"
  "ds_read_b64_tr_b16 a[220:221], v22 offset:12288\n"
  "ds_read_b64_tr_b16 a[222:223], v22 offset:13312\n"
  "v_mfma_f32_32x32x16_bf16 v[144:159], a[196:199], a[180:183], v[144:159]\n"
  "s_waitcnt lgkmcnt(2)\n"
  "ds_read_b128 a[200:203], v21 offset:0\n"
  "ds_read_b128 a[204:207], v21 offset:4096\n"
  "v_mfma_f32_32x32x16_bf16 v[160:175], a[196:199], a[184:187], v[160:175]\n"
  "ds_read_b128 a[208:211], v21 offset:8192\n"
  "ds_read_b128 a[212:215], v21 offset:12288\n"
  "v_mfma_f32_32x32x16_bf16 v[176:191], a[196:199], a[188:191], v[176:191]\n"
  "ds_read_b128 a[216:219], v21 offset:16384\n"
  "v_mfma_f32_32x32x16_bf16 v[192:207], a[196:199], a[192:195], v[192:207]\n"
  "s_waitcnt lgkmcnt(3)\n"
  "v_mfma_f32_32x32x16_bf16 v[128:143], a[220:223], a[200:203], v[128:143]\n"
  "s_waitcnt vmcnt(18) lgkmcnt(0)\n"
  "s_barrier\n"
  "v_add_u32 v18, 0x9000, v18\n"
  "v_add_u32 v22, 0x9000, v22\n"
  "ds_read_b64_tr_b16 a[196:197], v22 offset:0\n"
  "ds_read_b64_tr_b16 a[198:199], v22 offset:1024\n"
  "v_mfma_f32_32x32x16_bf16 v[144:159], a[220:223], a[204:207], v[144:159]\n"
  "s_mov_b32 s80, 0x400\n"
  "s_mov_b32 s81, 0x20000\n"
  "s_add_u32 m0, s62, 0x5000\n"
  "s_nop 0\n"
  "buffer_load_dwordx4 v28, s[56:59], s81 offen offset:0 sc1 lds\n"
  "ds_read_b128 a[176:179], v18 offset:0\n"
  "ds_read_b128 a[180:183], v18 offset:4096\n"
  "v_mfma_f32_32x32x16_bf16 v[160:175], a[220:223], a[208:211], v[160:175]\n"
  "buffer_load_dwordx4 v28, s[56:59], s81 offen offset:1024 sc1 lds\n"
  "ds_read_b128 a[184:187], v18 offset:8192\n"
  "ds_read_b128 a[188:191], v18 offset:12288\n"
  "v_mfma_f32_32x32x16_bf16 v[176:191], a[220:223], a[212:215], v[176:191]\n"
  "ds_read_b128 a[192:195], v18 offset:16384\n"
  "v_add_u32 v19, 0x9000, v19\n"
  "v_add_u32 v20, 0x9000, v20\n"
  "v_add_u32 v21, 0x9000, v21\n"
  "v_mfma_f32_32x32x16_bf16 v[192:207], a[220:223], a[216:219], v[192:207]\n"
  "buffer_load_dwordx4 v28, s[56:59], s81 offen offset:2048 sc1 lds\n"
  "s_waitcnt lgkmcnt(3)\n"
  "v_mfma_f32_32x32x16_bf16 v[128:143], a[196:199], a[176:179], v[128:143]\n"
  "ds_read_b64_tr_b16 a[220:221], v22 offset:4096\n"
  "ds_read_b64_tr_b16 a[222:223], v22 offset:5120\n"
  "v_mfma_f32_32x32x16_bf16 v[144:159], a[196:199], a[180:183], v[144:159]\n"
  "s_waitcnt lgkmcnt(2)\n"
  "buffer_load_dwordx4 v28, s[56:59], s81 offen offset:3072 sc1 lds\n"
  "ds_read_b128 a[200:203], v19 offset:0\n"
  "ds_read_b128 a[204:207], v19 offset:4096\n"
  "v_mfma_f32_32x32x16_bf16 v[160:175], a[196:199], a[184:187], v[160:175]\n"
  "s_add_u32 m0, s63, 0x0\n"
  "s_nop 0\n"
  "buffer_load_dwordx4 v23, s[52:55], s80 offen offset:0 lds\n"
  "ds_read_b128 a[208:211], v19 offset:8192\n"
  "ds_read_b128 a[212:215], v19 offset:12288\n"
  "v_mfma_f32_32x32x16_bf16 v[176:191], a[196:199], a[188:191], v[176:191]\n"
  "ds_read_b128 a[216:219], v19 offset:16384\n"
  "v_mfma_f32_32x32x16_bf16 v[192:207], a[196:199], a[192:195], v[192:207]\n"
  "buffer_load_dwordx4 v24, s[52:55], s80 offen offset:1024 lds\n"
  "s_waitcnt lgkmcnt(3)\n"
  "v_mfma_f32_32x32x16_bf16 v[128:143], a[220:223], a[200:203], v[128:143]\n"
  "ds_read_b64_tr_b16 a[196:197], v22 offset:8192\n"
  "ds_read_b64_tr_b16 a[198:199], v22 offset:9216\n"
  "v_mfma_f32_32x32x16_bf16 v[144:159], a[220:223], a[204:207], v[144:159]\n"
  "s_waitcnt lgkmcnt(2)\n"
  "buffer_load_dwordx4 v25, s[52:55], s80 offen offset:2048 lds\n"
  "ds_read_b128 a[176:179], v20 offset:0\n"
  "ds_read_b128 a[180:183], v20 offset:4096\n"
  "v_mfma_f32_32x32x16_bf16 v[160:175], a[220:223], a[208:211], v[160:175]\n"
  "buffer_load_dwordx4 v26, s[52:55], s80 offen offset:3072 lds\n"
  "ds_read_b128 a[184:187], v20 offset:8192\n"
  "ds_read_b128 a[188:191], v20 offset:12288\n"
  "v_mfma_f32_32x32x16_bf16 v[176:191], a[220:223], a[212:215], v[176:191]\n"
  "ds_read_b128 a[192:195], v20 offset:16384\n"
  "v_mfma_f32_32x32x16_bf16 v[192:207], a[220:223], a[216:219], v[192:207]\n"
  "s_add_u32 m0, s63, 0x1000\n"
  "s_nop 0\n"
  "buffer_load_dwordx4 v27, s[52:55], s80 offen offset:0 lds\n"
  "s_waitcnt lgkmcnt(3)\n"
  "v_mfma_f32_32x32x16_bf16 v[128:143], a[196:199], a[176:179], v[128:143]\n"
  "ds_read_b64_tr_b16 a[220:221], v22 offset:12288\n"
  "ds_read_b64_tr_b16 a[222:223], v22 offset:13312\n"
  "v_mfma_f32_32x32x16_bf16 v[144:159], a[196:199], a[180:183], v[144:159]\n"
  "s_waitcnt lgkmcnt(2)\n"
  "ds_read_b128 a[200:203], v21 offset:0\n"
  "ds_read_b128 a[204:207], v21 offset:4096\n"
  "v_mfma_f32_32x32x16_bf16 v[160:175], a[196:199], a[184:187], v[160:175]\n"
  "ds_read_b128 a[208:211], v21 offset:8192\n"
  "ds_read_b128 a[212:215], v21 offset:12288\n"
  "v_mfma_f32_32x32x16_bf16 v[176:191], a[196:199], a[188:191], v[176:191]\n"
  "ds_read_b128 a[216:219], v21 offset:16384\n"
  "v_mfma_f32_32x32x16_bf16 v[192:207], a[196:199], a[192:195], v[192:207]\n"
  "s_waitcnt lgkmcnt(3)\n"
  "v_mfma_f32_32x32x16_bf16 v[128:143], a[220:223], a[200:203], v[128:143]\n"
  "s_waitcnt vmcnt(18) lgkmcnt(0)\n"
  "s_barrier\n"
  "v_add_u32 v18, 0x9000, v18\n"
  "v_add_u32 v22, 0x9000, v22\n"
  "ds_read_b64_tr_b16 a[196:197], v22 offset:0\n"
  "ds_read_b64_tr_b16 a[198:199], v22 offset:1024\n"
  "v_mfma_f32_32x32x16_bf16 v[144:159], a[220:223], a[204:207], v[144:159]\n"
  "s_mov_b32 s80, 0x480\n"
  "s_mov_b32 s81, 0x24000\n"
  "s_add_u32 m0, s62, 0xe000\n"
  "s_nop 0\n"
  "buffer_load_dwordx4 v28, s[56:59], s81 offen offset:0 sc1 lds\n"
  "ds_read_b128 a[176:179], v18 offset:0\n"
  "ds_read_b128 a[180:183], v18 offset:4096\n"
  "v_mfma_f32_32x32x16_bf16 v[160:175], a[220:223], a[208:211], v[160:175]\n"
  "buffer_load_dwordx4 v28, s[56:59], s81 offen offset:1024 sc1 lds\n"
  "ds_read_b128 a[184:187], v18 offset:8192\n"
  "ds_read_b128 a[188:191], v18 offset:12288\n"
  "v_mfma_f32_32x32x16_bf16 v[176:191], a[220:223], a[212:215], v[176:191]\n"
  "ds_read_b128 a[192:195], v18 offset:16384\n"
  "v_add_u32 v19, 0x9000, v19\n"
  "v_add_u32 v20, 0x9000, v20\n"
  "v_add_u32 v21, 0x9000, v21\n"
  "v_mfma_f32_32x32x16_bf16 v[192:207], a[220:223], a[216:219], v[192:207]\n"
  "buffer_load_dwordx4 v28, s[56:59], s81 offen offset:2048 sc1 lds\n"
  "s_waitcnt lgkmcnt(3)\n"
  "v_mfma_f32_32x32x16_bf16 v[128:143], a[196:199], a[176:179], v[128:143]\n"
  "ds_read_b64_tr_b16 a[220:221], v22 offset:4096\n"
  "ds_read_b64_tr_b16 a[222:223], v22 offset:5120\n"
  "v_mfma_f32_32x32x16_bf16 v[144:159], a[196:199], a[180:183], v[144:159]\n"
  "s_waitcnt lgkmcnt(2)\n"
  "buffer_load_dwordx4 v28, s[56:59], s81 offen offset:3072 sc1 lds\n"
  "ds_read_b128 a[200:203], v19 offset:0\n"
  "ds_read_b128 a[204:207], v19 offset:4096\n"
  "v_mfma_f32_32x32x16_bf16 v[160:175], a[196:199], a[184:187], v[160:175]\n"
  "s_add_u32 m0, s63, 0x9000\n"
  "s_nop 0\n"
  "buffer_load_dwordx4 v23, s[52:55], s80 offen offset:0 lds\n"
  "ds_read_b128 a[208:211], v19 offset:8192\n"
  "ds_read_b128 a[212:215], v19 offset:12288\n"
  "v_mfma_f32_32x32x16_bf16 v[176:191], a[196:199], a[188:191], v[176:191]\n"
  "ds_read_b128 a[216:219], v19 offset:16384\n"
  "v_mfma_f32_32x32x16_bf16 v[192:207], a[196:199], a[192:195], v[192:207]\n"
  "buffer_load_dwordx4 v24, s[52:55], s80 offen offset:1024 lds\n"
  "s_waitcnt lgkmcnt(3)\n"
  "v_mfma_f32_32x32x16_bf16 v[128:143], a[220:223], a[200:203], v[128:143]\n"
  "ds_read_b64_tr_b16 a[196:197], v22 offset:8192\n"
  "ds_read_b64_tr_b16 a[198:199], v22 offset:9216\n"
  "v_mfma_f32_32x32x16_bf16 v[144:159], a[220:223], a[204:207], v[144:159]\n"
  "s_waitcnt lgkmcnt(2)\n"
  "buffer_load_dwordx4 v25, s[52:55], s80 offen offset:2048 lds\n"
  "ds_read_b128 a[176:179], v20 offset:0\n"
  "ds_read_b128 a[180:183], v20 offset:4096\n"
  "v_mfma_f32_32x32x16_bf16 v[160:175], a[220:223], a[208:211], v[160:175]\n"
  "buffer_load_dwordx4 v26, s[52:55], s80 offen offset:3072 lds\n"
  "ds_read_b128 a[184:187], v20 offset:8192\n"
  "ds_read_b128 a[188:191], v20 offset:12288\n"
  "v_mfma_f32_32x32x16_bf16 v[176:191], a[220:223], a[212:215], v[176:191]\n"
  "ds_read_b128 a[192:195], v20 offset:16384\n"
  "v_mfma_f32_32x32x16_bf16 v[192:207], a[220:223], a[216:219], v[192:207]\n"
  "s_add_u32 m0, s63, 0xa000\n"
  "s_nop 0\n"
  "buffer_load_dwordx4 v27, s[52:55], s80 offen offset:0 lds\n"
  "s_waitcnt lgkmcnt(3)\n"
  "v_mfma_f32_32x32x16_bf16 v[128:143], a[196:199], a[176:179], v[128:143]\n"
  "ds_read_b64_tr_b16 a[220:221], v22 offset:12288\n"
  "ds_read_b64_tr_b16 a[222:223], v22 offset:13312\n"
  "v_mfma_f32_32x32x16_bf16 v[144:159], a[196:199], a[180:183], v[144:159]\n"
  "s_waitcnt lgkmcnt(2)\n"
  "ds_read_b128 a[200:203], v21 offset:0\n"
  "ds_read_b128 a[204:207], v21 offset:4096\n"
  "v_mfma_f32_32x32x16_bf16 v[160:175], a[196:199], a[184:187], v[160:175]\n"
  "ds_read_b128 a[208:211], v21 offset:8192\n"
  "ds_read_b128 a[212:215], v21 offset:12288\n"
  "v_mfma_f32_32x32x16_bf16 v[176:191], a[196:199], a[188:191], v[176:191]\n"
  "ds_read_b128 a[216:219], v21 offset:16384\n"
  "v_mfma_f32_32x32x16_bf16 v[192:207], a[196:199], a[192:195], v[192:207]\n"
  "s_waitcnt lgkmcnt(3)\n"
  "v_mfma_f32_32x32x16_bf16 v[128:143], a[220:223], a[200:203], v[128:143]\n"
  "s_waitcnt vmcnt(18) lgkmcnt(0)\n"
  "s_barrier\n"
  "v_add_u32 v18, 0x9000, v18\n"
  "v_add_u32 v22, 0x9000, v22\n"
  "ds_read_b64_tr_b16 a[196:197], v22 offset:0\n"
  "ds_read_b64_tr_b16 a[198:199], v22 offset:1024\n"
  "v_mfma_f32_32x32x16_bf16 v[144:159], a[220:223], a[204:207], v[144:159]\n"
  "ds_read_b128 a[176:179], v18 offset:0\n"
  "ds_read_b128 a[180:183], v18 offset:4096\n"
  "v_mfma_f32_32x32x16_bf16 v[160:175], a[220:223], a[208:211], v[160:175]\n"
  "ds_read_b128 a[184:187], v18 offset:8192\n"
  "ds_read_b128 a[188:191], v18 offset:12288\n"
  "v_mfma_f32_32x32x16_bf16 v[176:191], a[220:223], a[212:215], v[176:191]\n"
  "ds_read_b128 a[192:195], v18 offset:16384\n"
  "v_add_u32 v19, 0x9000, v19\n"
  "v_add_u32 v20, 0x9000, v20\n"
  "v_add_u32 v21, 0x9000, v21\n"
  "v_mfma_f32_32x32x16_bf16 v[192:207], a[220:223], a[216:219], v[192:207]\n"
  "s_waitcnt lgkmcnt(3)\n"
  "v_mfma_f32_32x32x16_bf16 v[128:143], a[196:199], a[176:179], v[128:143]\n"
  "ds_read_b64_tr_b16 a[220:221], v22 offset:4096\n"
  "ds_read_b64_tr_b16 a[222:223], v22 offset:5120\n"
  "v_mfma_f32_32x32x16_bf16 v[144:159], a[196:199], a[180:183], v[144:159]\n"
  "s_waitcnt lgkmcnt(2)\n"
  "ds_read_b128 a[200:203], v19 offset:0\n"
  "ds_read_b128 a[204:207], v19 offset:4096\n"
  "v_mfma_f32_32x32x16_bf16 v[160:175], a[196:199], a[184:187], v[160:175]\n"
  "ds_read_b128 a[208:211], v19 offset:8192\n"
  "ds_read_b128 a[212:215], v19 offset:12288\n"
  "v_mfma_f32_32x32x16_bf16 v[176:191], a[196:199], a[188:191], v[176:191]\n"
  "ds_read_b128 a[216:219], v19 offset:16384\n"
  "v_mfma_f32_32x32x16_bf16 v[192:207], a[196:199], a[192:195], v[192:207]\n"
  "s_waitcnt lgkmcnt(3)\n"
  "v_mfma_f32_32x32x16_bf16 v[128:143], a[220:223], a[200:203], v[128:143]\n"
  "ds_read_b64_tr_b16 a[196:197], v22 offset:8192\n"
  "ds_read_b64_tr_b16 a[198:199], v22 offset:9216\n"
  "v_mfma_f32_32x32x16_bf16 v[144:159], a[220:223], a[204:207], v[144:159]\n"
  "s_waitcnt lgkmcnt(2)\n"
  "ds_read_b128 a[176:179], v20 offset:0\n"
  "ds_read_b128 a[180:183], v20 offset:4096\n"
  "v_mfma_f32_32x32x16_bf16 v[160:175], a[220:223], a[208:211], v[160:175]\n"
  "ds_read_b128 a[184:187], v20 offset:8192\n"
  "ds_read_b128 a[188:191], v20 offset:12288\n"
  "v_mfma_f32_32x32x16_bf16 v[176:191], a[220:223], a[212:215], v[176:191]\n"
  "ds_read_b128 a[192:195], v20 offset:16384\n"
  "v_mfma_f32_32x32x16_bf16 v[192:207], a[220:223], a[216:219], v[192:207]\n"
  "s_waitcnt lgkmcnt(3)\n"
  "v_mfma_f32_32x32x16_bf16 v[128:143], a[196:199], a[176:179], v[128:143]\n"
  "ds_read_b64_tr_b16 a[220:221], v22 offset:12288\n"
  "ds_read_b64_tr_b16 a[222:223], v22 offset:13312\n"
  "v_mfma_f32_32x32x16_bf16 v[144:159], a[196:199], a[180:183], v[144:159]\n"
  "s_waitcnt lgkmcnt(2)\n"
  "ds_read_b128 a[200:203], v21 offset:0\n"
  "ds_read_b128 a[204:207], v21 offset:4096\n"
  "v_mfma_f32_32x32x16_bf16 v[160:175], a[196:199], a[184:187], v[160:175]\n"
  "ds_read_b128 a[208:211], v21 offset:8192\n"
  "ds_read_b128 a[212:215], v21 offset:12288\n"
  "v_mfma_f32_32x32x16_bf16 v[176:191], a[196:199], a[188:191], v[176:191]\n"
  "ds_read_b128 a[216:219], v21 offset:16384\n"
  "v_mfma_f32_32x32x16_bf16 v[192:207], a[196:199], a[192:195], v[192:207]\n"
  "s_waitcnt lgkmcnt(3)\n"
  "v_mfma_f32_32x32x16_bf16 v[128:143], a[220:223], a[200:203], v[128:143]\n"
  "s_waitcnt vmcnt(9) lgkmcnt(0)\n"
  "s_barrier\n"
  "v_add_u32 v18, 0xfffe5000, v18\n"
  "v_add_u32 v22, 0xfffe5000, v22\n"
  "ds_read_b64_tr_b16 a[196:197], v22 offset:0\n"
  "ds_read_b64_tr_b16 a[198:199], v22 offset:1024\n"
  "v_mfma_f32_32x32x16_bf16 v[144:159], a[220:223], a[204:207], v[144:159]\n"
  "ds_read_b128 a[176:179], v18 offset:0\n"
  "ds_read_b128 a[180:183], v18 offset:4096\n"
  "v_mfma_f32_32x32x16_bf16 v[160:175], a[220:223], a[208:211], v[160:175]\n"
  "ds_read_b128 a[184:187], v18 offset:8192\n"
  "ds_read_b128 a[188:191], v18 offset:12288\n"
  "v_mfma_f32_32x32x16_bf16 v[176:191], a[220:223], a[212:215], v[176:191]\n"
  "ds_read_b128 a[192:195], v18 offset:16384\n"
  "v_add_u32 v19, 0xfffe5000, v19\n"
  "v_add_u32 v20, 0xfffe5000, v20\n"
  "v_add_u32 v21, 0xfffe5000, v21\n"
  "v_mfma_f32_32x32x16_bf16 v[192:207], a[220:223], a[216:219], v[192:207]\n"
  "s_waitcnt lgkmcnt(3)\n"
  "v_mfma_f32_32x32x16_bf16 v[128:143], a[196:199], a[176:179], v[128:143]\n"
  "ds_read_b64_tr_b16 a[220:221], v22 offset:4096\n"
  "ds_read_b64_tr_b16 a[222:223], v22 offset:5120\n"
  "v_mfma_f32_32x32x16_bf16 v[144:159], a[196:199], a[180:183], v[144:159]\n"
  "s_waitcnt lgkmcnt(2)\n"
  "ds_read_b128 a[200:203], v19 offset:0\n"
  "ds_read_b128 a[204:207], v19 offset:4096\n"
  "v_mfma_f32_32x32x16_bf16 v[160:175], a[196:199], a[184:187], v[160:175]\n"
  "ds_read_b128 a[208:211], v19 offset:8192\n"
  "ds_read_b128 a[212:215], v19 offset:12288\n"
  "v_mfma_f32_32x32x16_bf16 v[176:191], a[196:199], a[188:191], v[176:191]\n"
  "ds_read_b128 a[216:219], v19 offset:16384\n"
  "v_mfma_f32_32x32x16_bf16 v[192:207], a[196:199], a[192:195], v[192:207]\n"
  "s_waitcnt lgkmcnt(3)\n"
  "v_mfma_f32_32x32x16_bf16 v[128:143], a[220:223], a[200:203], v[128:143]\n"
  "ds_read_b64_tr_b16 a[196:197], v22 offset:8192\n"
  "ds_read_b64_tr_b16 a[198:199], v22 offset:9216\n"
  "v_mfma_f32_32x32x16_bf16 v[144:159], a[220:223], a[204:207], v[144:159]\n"
  "s_waitcnt lgkmcnt(2)\n"
  "ds_read_b128 a[176:179], v20 offset:0\n"
  "ds_read_b128 a[180:183], v20 offset:4096\n"
  "v_mfma_f32_32x32x16_bf16 v[160:175], a[220:223], a[208:211], v[160:175]\n"
  "ds_read_b128 a[184:187], v20 offset:8192\n"
  "ds_read_b128 a[188:191], v20 offset:12288\n"
  "v_mfma_f32_32x32x16_bf16 v[176:191], a[220:223], a[212:215], v[176:191]\n"
  "ds_read_b128 a[192:195], v20 offset:16384\n"
  "v_mfma_f32_32x32x16_bf16 v[192:207], a[220:223], a[216:219], v[192:207]\n"
  "s_waitcnt lgkmcnt(3)\n"
  "v_mfma_f32_32x32x16_bf16 v[128:143], a[196:199], a[176:179], v[128:143]\n"
  "ds_read_b64_tr_b16 a[220:221], v22 offset:12288\n"
  "ds_read_b64_tr_b16 a[222:223], v22 offset:13312\n"
  "v_mfma_f32_32x32x16_bf16 v[144:159], a[196:199], a[180:183], v[144:159]\n"
  "s_waitcnt lgkmcnt(2)\n"
  "ds_read_b128 a[200:203], v21 offset:0\n"
  "ds_read_b128 a[204:207], v21 offset:4096\n"
  "v_mfma_f32_32x32x16_bf16 v[160:175], a[196:199], a[184:187], v[160:175]\n"
  "ds_read_b128 a[208:211], v21 offset:8192\n"
  "ds_read_b128 a[212:215], v21 offset:12288\n"
  "v_mfma_f32_32x32x16_bf16 v[176:191], a[196:199], a[188:191], v[176:191]\n"
  "ds_read_b128 a[216:219], v21 offset:16384\n"
  "v_mfma_f32_32x32x16_bf16 v[192:207], a[196:199], a[192:195], v[192:207]\n"
  "s_waitcnt lgkmcnt(3)\n"
  "v_mfma_f32_32x32x16_bf16 v[128:143], a[220:223], a[200:203], v[128:143]\n"
  "s_waitcnt vmcnt(0) lgkmcnt(0)\n"
  "s_barrier\n"
  "v_add_u32 v18, 0x9000, v18\n"
  "v_add_u32 v22, 0x9000, v22\n"
  "ds_read_b64_tr_b16 a[196:197], v22 offset:0\n"
  "ds_read_b64_tr_b16 a[198:199], v22 offset:1024\n"
  "v_mfma_f32_32x32x16_bf16 v[144:159], a[220:223], a[204:207], v[144:159]\n"
  "ds_read_b128 a[176:179], v18 offset:0\n"
  "ds_read_b128 a[180:183], v18 offset:4096\n"
  "v_mfma_f32_32x32x16_bf16 v[160:175], a[220:223], a[208:211], v[160:175]\n"
  "ds_read_b128 a[184:187], v18 offset:8192\n"
  "ds_read_b128 a[188:191], v18 offset:12288\n"
  "v_mfma_f32_32x32x16_bf16 v[176:191], a[220:223], a[212:215], v[176:191]\n"
  "ds_read_b128 a[192:195], v18 offset:16384\n"
  "v_add_u32 v19, 0x9000, v19\n"
  "v_add_u32 v20, 0x9000, v20\n"
  "v_add_u32 v21, 0x9000, v21\n"
  "v_mfma_f32_32x32x16_bf16 v[192:207], a[220:223], a[216:219], v[192:207]\n"
  "s_waitcnt lgkmcnt(3)\n"
  "v_mfma_f32_32x32x16_bf16 v[128:143], a[196:199], a[176:179], v[128:143]\n"
  "ds_read_b64_tr_b16 a[220:221], v22 offset:4096\n"
  "ds_read_b64_tr_b16 a[222:223], v22 offset:5120\n"
  "v_mfma_f32_32x32x16_bf16 v[144:159], a[196:199], a[180:183], v[144:159]\n"
  "s_waitcnt lgkmcnt(2)\n"
  "ds_read_b128 a[200:203], v19 offset:0\n"
  "ds_read_b128 a[204:207], v19 offset:4096\n"
  "v_mfma_f32_32x32x16_bf16 v[160:175], a[196:199], a[184:187], v[160:175]\n"
  "ds_read_b128 a[208:211], v19 offset:8192\n"
  "ds_read_b128 a[212:215], v19 offset:12288\n"
  "v_mfma_f32_32x32x16_bf16 v[176:191], a[196:199], a[188:191], v[176:191]\n"
  "ds_read_b128 a[216:219], v19 offset:16384\n"
  "v_mfma_f32_32x32x16_bf16 v[192:207], a[196:199], a[192:195], v[192:207]\n"
  "s_waitcnt lgkmcnt(3)\n"
  "v_mfma_f32_32x32x16_bf16 v[128:143], a[220:223], a[200:203], v[128:143]\n"
  "ds_read_b64_tr_b16 a[196:197], v22 offset:8192\n"
  "ds_read_b64_tr_b16 a[198:199], v22 offset:9216\n"
  "v_lshrrev_b32 v12, 1, v2\n"
  "v_and_b32 v12, 0xf8, v12\n"
  "buffer_load_dwordx2 v[14:15], v12, s[44:47], s76 offen sc1\n"
  "v_mfma_f32_32x32x16_bf16 v[144:159], a[220:223], a[204:207], v[144:159]\n"
  "s_waitcnt lgkmcnt(2)\n"
  "ds_read_b128 a[176:179], v20 offset:0\n"
  "ds_read_b128 a[180:183], v20 offset:4096\n"
  "v_mfma_f32_32x32x16_bf16 v[160:175], a[220:223], a[208:211], v[160:175]\n"
  "ds_read_b128 a[184:187], v20 offset:8192\n"
  "ds_read_b128 a[188:191], v20 offset:12288\n"
  "v_mfma_f32_32x32x16_bf16 v[176:191], a[220:223], a[212:215], v[176:191]\n"
  "ds_read_b128 a[192:195], v20 offset:16384\n"
  "v_mfma_f32_32x32x16_bf16 v[192:207], a[220:223], a[216:219], v[192:207]\n"
  "s_waitcnt lgkmcnt(3)\n"
  "v_mfma_f32_32x32x16_bf16 v[128:143], a[196:199], a[176:179], v[128:143]\n"
  "ds_read_b64_tr_b16 a[220:221], v22 offset:12288\n"
  "ds_read_b64_tr_b16 a[222:223], v22 offset:13312\n"
  "v_mfma_f32_32x32x16_bf16 v[144:159], a[196:199], a[180:183], v[144:159]\n"
  "s_waitcnt lgkmcnt(2)\n"
  "ds_read_b128 a[200:203], v21 offset:0\n"
  "ds_read_b128 a[204:207], v21 offset:4096\n"
  "v_mfma_f32_32x32x16_bf16 v[160:175], a[196:199], a[184:187], v[160:175]\n"
  "ds_read_b128 a[208:211], v21 offset:8192\n"
  "ds_read_b128 a[212:215], v21 offset:12288\n"
  "v_mfma_f32_32x32x16_bf16 v[176:191], a[196:199], a[188:191], v[176:191]\n"
  "ds_read_b128 a[216:219], v21 offset:16384\n"
  "v_mfma_f32_32x32x16_bf16 v[192:207], a[196:199], a[192:195], v[192:207]\n"
  "s_waitcnt lgkmcnt(3)\n"
  "v_mfma_f32_32x32x16_bf16 v[128:143], a[220:223], a[200:203], v[128:143]\n"
  "v_mfma_f32_32x32x16_bf16 v[144:159], a[220:223], a[204:207], v[144:159]\n"
  "s_waitcnt lgkmcnt(0)\n"
  "v_mfma_f32_32x32x16_bf16 v[160:175], a[220:223], a[208:211], v[160:175]\n"
  "v_mfma_f32_32x32x16_bf16 v[176:191], a[220:223], a[212:215], v[176:191]\n"
  "v_mfma_f32_32x32x16_bf16 v[192:207], a[220:223], a[216:219], v[192:207]\n"
  "s_waitcnt lgkmcnt(0)\n"
  "s_barrier\n"
  "v_accvgpr_read_b32 v18, a224\n"
  "v_accvgpr_read_b32 v19, a225\n"
  "v_accvgpr_read_b32 v20, a226\n"
  "v_accvgpr_read_b32 v21, a227\n"
  "s_mov_b32 s87, 0\n"
  "s_mov_b32 s80, 0\n"
  "s_mov_b32 s81, 0\n"
  "s_add_u32 m0, s64, 0x0\n"
  "s_nop 0\n"
  "buffer_load_dwordx4 v3, s[24:27], s81 offen offset:0 lds\n"
  "buffer_load_dwordx4 v4, s[24:27], s81 offen offset:1024 lds\n"
  "buffer_load_dwordx4 v5, s[24:27], s81 offen offset:2048 lds\n"
  "buffer_load_dwordx4 v6, s[24:27], s81 offen offset:3072 lds\n"
  "s_add_u32 m0, s64, 0x1000\n"
  "s_nop 0\n"
  "buffer_load_dwordx4 v7, s[24:27], s81 offen offset:0 lds\n"
  "s_add_u32 s81, s81, 0x14000\n"
  "s_nop 3\n"
  "s_cmp_ge_u32 s62, 0x3000\n"
  "s_cbranch_scc1 LW0_%=\n"
  "v_cvt_pk_bf16_f32 v128, v128, v129\n"
  "v_cvt_pk_bf16_f32 v129, v130, v131\n"
  "v_cvt_pk_bf16_f32 v130, v132, v133\n"
  "v_cvt_pk_bf16_f32 v131, v134, v135\n"
  "ds_write_b64 v18, v[128:129] offset:0\n"
  "ds_write_b64 v19, v[130:131] offset:0\n"
  "v_cvt_pk_bf16_f32 v144, v144, v145\n"
  "v_cvt_pk_bf16_f32 v145, v146, v147\n"
  "v_cvt_pk_bf16_f32 v146, v148, v149\n"
  "v_cvt_pk_bf16_f32 v147, v150, v151\n"
  "ds_write_b64 v18, v[144:145] offset:11776\n"
  "ds_write_b64 v19, v[146:147] offset:11776\n"
  "v_cvt_pk_bf16_f32 v160, v160, v161\n"
  "v_cvt_pk_bf16_f32 v161, v162, v163\n"
  "v_cvt_pk_bf16_f32 v162, v164, v165\n"
  "v_cvt_pk_bf16_f32 v163, v166, v167\n"
  "ds_write_b64 v18, v[160:161] offset:23552\n"
  "ds_write_b64 v19, v[162:163] offset:23552\n"
  "v_cvt_pk_bf16_f32 v176, v176, v177\n"
  "v_cvt_pk_bf16_f32 v177, v178, v179\n"
  "v_cvt_pk_bf16_f32 v178, v180, v181\n"
  "v_cvt_pk_bf16_f32 v179, v182, v183\n"
  "ds_write_b64 v18, v[176:177] offset:35328\n"
  "ds_write_b64 v19, v[178:179] offset:35328\n"
  "v_cvt_pk_bf16_f32 v192, v192, v193\n"
  "v_cvt_pk_bf16_f32 v193, v194, v195\n"
  "v_cvt_pk_bf16_f32 v194, v196, v197\n"
  "v_cvt_pk_bf16_f32 v195, v198, v199\n"
  "ds_write_b64 v18, v[192:193] offset:47104\n"
  "ds_write_b64 v19, v[194:195] offset:47104\n"
  "LW0_%=_r:\n"
  "s_cmp_ge_u32 s62, 0x2000\n"
  "s_cbranch_scc1 LW2_%=\n"
  "v_cvt_pk_bf16_f32 v136, v136, v137\n"
  "v_cvt_pk_bf16_f32 v137, v138, v139\n"
  "v_cvt_pk_bf16_f32 v138, v140, v141\n"
  "v_cvt_pk_bf16_f32 v139, v142, v143\n"
  "ds_write_b64 v20, v[136:137] offset:0\n"
  "ds_write_b64 v21, v[138:139] offset:0\n"
  "v_cvt_pk_bf16_f32 v152, v152, v153\n"
  "v_cvt_pk_bf16_f32 v153, v154, v155\n"
  "v_cvt_pk_bf16_f32 v154, v156, v157\n"
  "v_cvt_pk_bf16_f32 v155, v158, v159\n"
  "ds_write_b64 v20, v[152:153] offset:11776\n"
  "ds_write_b64 v21, v[154:155] offset:11776\n"
  "v_cvt_pk_bf16_f32 v168, v168, v169\n"
  "v_cvt_pk_bf16_f32 v169, v170, v171\n"
  "v_cvt_pk_bf16_f32 v170, v172, v173\n"
  "v_cvt_pk_bf16_f32 v171, v174, v175\n"
  "ds_write_b64 v20, v[168:169] offset:23552\n"
  "ds_write_b64 v21, v[170:171] offset:23552\n"
  "v_cvt_pk_bf16_f32 v184, v184, v185\n"
  "v_cvt_pk_bf16_f32 v185, v186, v187\n"
  "v_cvt_pk_bf16_f32 v186, v188, v189\n"
  "v_cvt_pk_bf16_f32 v187, v190, v191\n"
  "ds_write_b64 v20, v[184:185] offset:35328\n"
  "ds_write_b64 v21, v[186:187] offset:35328\n"
  "v_cvt_pk_bf16_f32 v200, v200, v201\n"
  "v_cvt_pk_bf16_f32 v201, v202, v203\n"
  "v_cvt_pk_bf16_f32 v202, v204, v205\n"
  "v_cvt_pk_bf16_f32 v203, v206, v207\n"
  "ds_write_b64 v20, v[200:201] offset:47104\n"
  "ds_write_b64 v21, v[202:203] offset:47104\n"
  "LW2_%=_r:\n"
  "v_lshlrev_b32 v31, 3, v2\n"
  "s_waitcnt lgkmcnt(0)\n"
  "s_barrier\n"
  "ds_read_b128 v[32:35], v16 offset:5152\n"
  "ds_read_b128 v[36:39], v16 offset:5168\n"
  "ds_read_b64 v[40:41], v16 offset:5184\n"
  "ds_read_b128 v[44:47], v16 offset:5248\n"
  "ds_read_b128 v[48:51], v16 offset:5264\n"
  "ds_read_b64 v[52:53], v16 offset:5280\n"
  "ds_read_b128 v[56:59], v16 offset:4512\n"
  "ds_read_b128 v[60:63], v16 offset:4528\n"
  "ds_read_b64 v[64:65], v16 offset:4544\n"
  "ds_read_b128 v[68:71], v16 offset:3776\n"
  "ds_read_b128 v[72:75], v16 offset:3792\n"
  "ds_read_b64 v[76:77], v16 offset:3808\n"
  "ds_read_b128 v[80:83], v16 offset:3040\n"
  "ds_read_b128 v[84:87], v16 offset:3056\n"
  "ds_read_b64 v[88:89], v16 offset:3072\n"
  "ds_read_b128 v[92:95], v16 offset:2304\n"
  "ds_read_b128 v[96:99], v16 offset:2320\n"
  "ds_read_b64 v[100:101], v16 offset:2336\n"
  "ds_read_b128 v[104:107], v16 offset:1568\n"
  "ds_read_b128 v[108:111], v16 offset:1584\n"
  "ds_read_b64 v[112:113], v16 offset:1600\n"
  "ds_read_b128 v[116:119], v16 offset:832\n"
  "ds_read_b128 v[120:123], v16 offset:848\n"
  "ds_read_b64 v[124:125], v16 offset:864\n"
  "ds_read_b128 v[128:131], v16 offset:96\n"
  "ds_read_b128 v[132:135], v16 offset:112\n"
  "s_add_u32 m0, s64, 0x5000\n"
  "ds_read_b64 v[136:137], v16 offset:128\n"
  "s_waitcnt lgkmcnt(15)\n"
  "v_dot2_f32_bf16 v240, v32, v44, 0\n"
  "v_dot2_f32_bf16 v241, v32, v56, 0\n"
  "ds_read_b128 v[140:143], v17 offset:5152\n"
  "v_dot2_f32_bf16 v240, v33, v45, v240\n"
  "buffer_load_dwordx4 v3, s[24:27], s81 offen offset:0 lds\n"
  "v_dot2_f32_bf16 v241, v33, v57, v241\n"
  "ds_read_b128 v[144:147], v17 offset:5168\n"
  "v_dot2_f32_bf16 v240, v34, v46, v240\n"
  "v_dot2_f32_bf16 v241, v34, v58, v241\n"
  "ds_read_b128 v[148:151], v17 offset:5184\n"
  "buffer_load_dwordx4 v4, s[24:27], s81 offen offset:1024 lds\n"
  "v_dot2_f32_bf16 v240, v35, v47, v240\n"
  "v_dot2_f32_bf16 v241, v35, v59, v241\n"
  "ds_read_b128 v[152:155], v17 offset:5200\n"
  "v_dot2_f32_bf16 v240, v36, v48, v240\n"
  "v_dot2_f32_bf16 v241, v36, v60, v241\n"
  "buffer_load_dwordx4 v5, s[24:27], s81 offen offset:2048 lds\n"
  "ds_read_b128 v[156:159], v17 offset:5216\n"
  "v_dot2_f32_bf16 v240, v37, v49, v240\n"
  "v_dot2_f32_bf16 v241, v37, v61, v241\n"
  "ds_read_b128 v[160:163], v17 offset:4416\n"
  "v_dot2_f32_bf16 v240, v38, v50, v240\n"
  "buffer_load_dwordx4 v6, s[24:27], s81 offen offset:3072 lds\n"
  "v_dot2_f32_bf16 v241, v38, v62, v241\n"
  "ds_read_b128 v[164:167], v17 offset:4432\n"
  "v_dot2_f32_bf16 v240, v39, v51, v240\n"
  "v_dot2_f32_bf16 v241, v39, v63, v241\n"
  "ds_read_b128 v[168:171], v17 offset:4448\n"
  "s_add_u32 m0, s64, 0x6000\n"
  "v_dot2_f32_bf16 v240, v40, v52, v240\n"
  "v_dot2_f32_bf16 v241, v40, v64, v241\n"
  "ds_read_b128 v[172:175], v17 offset:4464\n"
  "v_dot2_f32_bf16 v240, v41, v53, v240\n"
  "v_dot2_f32_bf16 v241, v41, v65, v241\n"
  "buffer_load_dwordx4 v7, s[24:27], s81 offen offset:0 lds\n"
  "ds_read_b128 v[176:179], v17 offset:4480\n"
  "v_dot2_f32_bf16 v242, v32, v68, 0\n"
  "s_waitcnt lgkmcnt(15)\n"
  "v_dot2_f32_bf16 v243, v32, v80, 0\n"
  "ds_read_b128 v[180:183], v17 offset:3680\n"
  "v_dot2_f32_bf16 v242, v33, v69, v242\n"
  "s_add_u32 s81, s81, 0x14000\n"
  "v_dot2_f32_bf16 v243, v33, v81, v243\n"
  "ds_read_b128 v[184:187], v17 offset:3696\n"
  "v_dot2_f32_bf16 v242, v34, v70, v242\n"
  "v_dot2_f32_bf16 v243, v34, v82, v243\n"
  "ds_read_b128 v[188:191], v17 offset:3712\n"
  "s_add_u32 m0, s64, 0xa000\n"
  "v_dot2_f32_bf16 v242, v35, v71, v242\n"
  "v_dot2_f32_bf16 v243, v35, v83, v243\n"
  "ds_read_b128 v[192:195], v17 offset:3728\n"
  "v_dot2_f32_bf16 v242, v36, v72, v242\n"
  "v_dot2_f32_bf16 v243, v36, v84, v243\n"
  "buffer_load_dwordx4 v3, s[24:27], s81 offen offset:0 lds\n"
  "ds_read_b128 v[196:199], v17 offset:3744\n"
  "v_dot2_f32_bf16 v242, v37, v73, v242\n"
  "v_dot2_f32_bf16 v243, v37, v85, v243\n"
  "ds_read_b128 v[200:203], v17 offset:2944\n"
  "v_dot2_f32_bf16 v242, v38, v74, v242\n"
  "buffer_load_dwordx4 v4, s[24:27], s81 offen offset:1024 lds\n"
  "v_dot2_f32_bf16 v243, v38, v86, v243\n"
  "ds_read_b128 v[204:207], v17 offset:2960\n"
  "v_dot2_f32_bf16 v242, v39, v75, v242\n"
  "v_dot2_f32_bf16 v243, v39, v87, v243\n"
  "ds_read_b128 v[208:211], v17 offset:2976\n"
  "buffer_load_dwordx4 v5, s[24:27], s81 offen offset:2048 lds\n"
  "v_dot2_f32_bf16 v242, v40, v76, v242\n"
  "v_dot2_f32_bf16 v243, v40, v88, v243\n"
  "ds_read_b128 v[212:215], v17 offset:2992\n"
  "v_dot2_f32_bf16 v242, v41, v77, v242\n"
  "v_dot2_f32_bf16 v243, v41, v89, v243\n"
  "buffer_load_dwordx4 v6, s[24:27], s81 offen offset:3072 lds\n"
  "ds_read_b128 v[216:219], v17 offset:3008\n"
  "v_dot2_f32_bf16 v244, v32, v92, 0\n"
  "v_dot2_f32_bf16 v245, v32, v104, 0\n"
  "ds_read_b128 v[220:223], v17 offset:2208\n"
  "v_dot2_f32_bf16 v244, v33, v93, v244\n"
  "s_add_u32 m0, s64, 0xb000\n"
  "v_dot2_f32_bf16 v245, v33, v105, v245\n"
  "ds_read_b128 v[224:227], v17 offset:2224\n"
  "v_dot2_f32_bf16 v244, v34, v94, v244\n"
  "v_dot2_f32_bf16 v245, v34, v106, v245\n"
  "ds_read_b128 v[228:231], v17 offset:2240\n"
  "buffer_load_dwordx4 v7, s[24:27], s81 offen offset:0 lds\n"
  "v_dot2_f32_bf16 v244, v35, v95, v244\n"
  "v_dot2_f32_bf16 v245, v35, v107, v245\n"
  "ds_read_b128 v[232:235], v17 offset:2256\n"
  "v_dot2_f32_bf16 v244, v36, v96, v244\n"
  "v_dot2_f32_bf16 v245, v36, v108, v245\n"
  "s_add_u32 s81, s81, 0x14000\n"
  "ds_read_b128 v[236:239], v17 offset:2272\n"
  "v_dot2_f32_bf16 v244, v37, v97, v244\n"
  "v_dot2_f32_bf16 v245, v37, v109, v245\n"
  "v_dot2_f32_bf16 v244, v38, v98, v244\n"
  "v_dot2_f32_bf16 v245, v38, v110, v245\n"
  "s_add_u32 m0, s64, 0xf000\n"
  "v_dot2_f32_bf16 v244, v39, v99, v244\n"
  "v_dot2_f32_bf16 v245, v39, v111, v245\n"
  "v_dot2_f32_bf16 v244, v40, v100, v244\n"
  "v_dot2_f32_bf16 v245, v40, v112, v245\n"
  "v_dot2_f32_bf16 v244, v41, v101, v244\n"
  "buffer_load_dwordx4 v3, s[24:27], s81 offen offset:0 lds\n"
  "v_dot2_f32_bf16 v245, v41, v113, v245\n"
  "v_dot2_f32_bf16 v246, v32, v116, 0\n"
  "s_waitcnt lgkmcnt(15)\n"
  "v_dot2_f32_bf16 v247, v32, v128, 0\n"
  "v_dot2_f32_bf16 v246, v33, v117, v246\n"
  "v_dot2_f32_bf16 v247, v33, v129, v247\n"
  "buffer_load_dwordx4 v4, s[24:27], s81 offen offset:1024 lds\n"
  "v_dot2_f32_bf16 v246, v34, v118, v246\n"
  "v_dot2_f32_bf16 v247, v34, v130, v247\n"
  "v_dot2_f32_bf16 v246, v35, v119, v246\n"
  "v_dot2_f32_bf16 v247, v35, v131, v247\n"
  "v_dot2_f32_bf16 v246, v36, v120, v246\n"
  "buffer_load_dwordx4 v5, s[24:27], s81 offen offset:2048 lds\n"
  "v_dot2_f32_bf16 v247, v36, v132, v247\n"
  "v_dot2_f32_bf16 v246, v37, v121, v246\n"
  "v_dot2_f32_bf16 v247, v37, v133, v247\n"
  "v_dot2_f32_bf16 v246, v38, v122, v246\n"
  "v_dot2_f32_bf16 v247, v38, v134, v247\n"
  "buffer_load_dwordx4 v6, s[24:27], s81 offen offset:3072 lds\n"
  "v_dot2_f32_bf16 v246, v39, v123, v246\n"
  "v_dot2_f32_bf16 v247, v39, v135, v247\n"
  "v_dot2_f32_bf16 v246, v40, v124, v246\n"
  "v_dot2_f32_bf16 v247, v40, v136, v247\n"
  "v_dot2_f32_bf16 v246, v41, v125, v246\n"
  "s_add_u32 m0, s64, 0x10000\n"
  "v_dot2_f32_bf16 v247, v41, v137, v247\n"
  "ds_read_b128 v[64:67], v17 offset:1472\n"
  "ds_read_b128 v[68:71], v17 offset:1488\n"
  "ds_read_b128 v[72:75], v17 offset:1504\n"
  "ds_read_b128 v[76:79], v17 offset:1520\n"
  "buffer_load_dwordx4 v7, s[24:27], s81 offen offset:0 lds\n"
  "ds_read_b128 v[80:83], v17 offset:1536\n"
  "ds_read_b128 v[84:87], v17 offset:736\n"
  "ds_read_b128 v[88:91], v17 offset:752\n"
  "ds_read_b128 v[92:95], v17 offset:768\n"
  "ds_read_b128 v[96:99], v17 offset:784\n"
  "s_add_u32 s81, s81, 0x14000\n"
  "ds_read_b128 v[100:103], v17 offset:800\n"
  "ds_read_b128 v[104:107], v17 offset:0\n"
  "ds_read_b128 v[108:111], v17 offset:16\n"
  "ds_read_b128 v[112:115], v17 offset:32\n"
  "ds_read_b128 v[116:119], v17 offset:48\n"
  "ds_read_b128 v[120:123], v17 offset:64\n"
  "v_cvt_pk_bf16_f32 v248, v240, v241\n"
  "v_cvt_pk_bf16_f32 v249, v242, v243\n"
  "v_cvt_pk_bf16_f32 v250, v244, v245\n"
  "v_cvt_pk_bf16_f32 v251, v246, v247\n"
  "s_waitcnt vmcnt(20)\n"
  "v_cmp_ne_u64 vcc, s[60:61], v[14:15]\n"
  "s_nop 0\n"
  "s_cbranch_vccnz PC_%=\n"
  "PC_%=_r:\n"
  "v_lshlrev_b32 v240, 16, v248\n"
  "v_and_b32 v241, 0xffff0000, v248\n"
  "v_lshlrev_b32 v242, 16, v249\n"
  "v_and_b32 v243, 0xffff0000, v249\n"
  "v_lshlrev_b32 v244, 16, v250\n"
  "buffer_load_dwordx4 a[64:67], v2, s[20:23], s80 offen offset:0 sc1\n"
  "v_and_b32 v245, 0xffff0000, v250\n"
  "v_lshlrev_b32 v246, 16, v251\n"
  "v_and_b32 v247, 0xffff0000, v251\n"
  "v_mul_f32 v240, 0x3e64f92e, v240\n"
  "v_mul_f32 v241, 0x3e64f92e, v241\n"
  "buffer_load_dwordx4 a[68:71], v2, s[20:23], s80 offen offset:1024 sc1\n"
  "v_mul_f32 v242, 0x3e64f92e, v242\n"
  "v_mul_f32 v243, 0x3e64f92e, v243\n"
  "v_mul_f32 v244, 0x3e64f92e, v244\n"
  "v_mul_f32 v245, 0x3e64f92e, v245\n"
  "v_mul_f32 v246, 0x3e64f92e, v246\n"
  "buffer_load_dwordx4 a[72:75], v2, s[20:23], s80 offen offset:2048 sc1\n"
  "v_mul_f32 v247, 0x3e64f92e, v247\n"
  "v_cvt_pk_bf16_f32 v248, v240, v241\n"
  "v_cvt_pk_bf16_f32 v249, v242, v243\n"
  "v_cvt_pk_bf16_f32 v250, v244, v245\n"
  "v_cvt_pk_bf16_f32 v251, v246, v247\n"
  "buffer_load_dwordx4 a[76:79], v2, s[20:23], s80 offen offset:3072 sc1\n"
  "v_lshlrev_b32 v240, 16, v248\n"
  "v_and_b32 v241, 0xffff0000, v248\n"
  "v_lshlrev_b32 v242, 16, v249\n"
  "v_and_b32 v243, 0xffff0000, v249\n"
  "v_lshlrev_b32 v244, 16, v250\n"
  "s_add_u32 s80, s80, 0x1000\n"
  "v_and_b32 v245, 0xffff0000, v250\n"
  "v_lshlrev_b32 v246, 16, v251\n"
  "v_and_b32 v247, 0xffff0000, v251\n"
  "v_mov_b32 v248, 0xce6e6b28\n"
  "v_cmp_gt_u32 s[52:53], 2, v13\n"
  "buffer_load_dwordx4 a[80:83], v2, s[20:23], s80 offen offset:0 sc1\n"
  "v_cmp_gt_u32 s[54:55], 4, v13\n"
  "v_cmp_gt_u32 s[56:57], 6, v13\n"
  "v_cmp_gt_u32 s[58:59], 8, v13\n"
  "v_cndmask_b32 v241, v241, v248, s[52:53]\n"
  "v_cndmask_b32 v242, v242, v248, s[54:55]\n"
  "buffer_load_dwordx4 a[84:87], v2, s[20:23], s80 offen offset:1024 sc1\n"
  "v_cndmask_b32 v243, v243, v248, s[56:57]\n"
  "v_cndmask_b32 v244, v244, v248, s[58:59]\n"
  "v_cmp_gt_u32 s[52:53], 10, v13\n"
  "v_cmp_gt_u32 s[54:55], 12, v13\n"
  "v_cmp_gt_u32 s[56:57], 14, v13\n"
  "buffer_load_dwordx4 a[88:91], v2, s[20:23], s80 offen offset:2048 sc1\n"
  "v_cndmask_b32 v245, v245, v248, s[52:53]\n"
  "v_cndmask_b32 v246, v246, v248, s[54:55]\n"
  "v_cndmask_b32 v247, v247, v248, s[56:57]\n"
  "v_max3_f32 v248, v240, v241, v242\n"
  "v_max3_f32 v249, v243, v244, v245\n"
  "buffer_load_dwordx4 a[92:95], v2, s[20:23], s80 offen offset:3072 sc1\n"
  "v_max3_f32 v248, v248, v246, v247\n"
  "v_max_f32 v248, v248, v249\n"
  "v_sub_f32 v240, v240, v248\n"
  "v_sub_f32 v241, v241, v248\n"
  "v_sub_f32 v242, v242, v248\n"
  "s_add_u32 s80, s80, 0x1000\n"
  "v_sub_f32 v243, v243, v248\n"
  "v_sub_f32 v244, v244, v248\n"
  "v_sub_f32 v245, v245, v248\n"
  "v_sub_f32 v246, v246, v248\n"
  "v_sub_f32 v247, v247, v248\n"
  "buffer_load_dwordx4 a[96:99], v2, s[20:23], s80 offen offset:0 sc1\n"
  "v_mul_f32 v240, 0x3fb8aa3b, v240\n"
  "v_mul_f32 v241, 0x3fb8aa3b, v241\n"
  "v_mul_f32 v242, 0x3fb8aa3b, v242\n"
  "v_mul_f32 v243, 0x3fb8aa3b, v243\n"
  "v_mul_f32 v244, 0x3fb8aa3b, v244\n"
  "buffer_load_dwordx4 a[100:103], v2, s[20:23], s80 offen offset:1024 sc1\n"
  "v_mul_f32 v245, 0x3fb8aa3b, v245\n"
  "v_mul_f32 v246, 0x3fb8aa3b, v246\n"
  "v_mul_f32 v247, 0x3fb8aa3b, v247\n"
  "v_exp_f32 v240, v240\n"
  "v_exp_f32 v241, v241\n"
  "buffer_load_dwordx4 a[104:107], v2, s[20:23], s80 offen offset:2048 sc1\n"
  "v_exp_f32 v242, v242\n"
  "v_exp_f32 v243, v243\n"
  "v_exp_f32 v244, v244\n"
  "v_exp_f32 v245, v245\n"
  "v_exp_f32 v246, v246\n"
  "buffer_load_dwordx4 a[108:111], v2, s[20:23], s80 offen offset:3072 sc1\n"
  "v_exp_f32 v247, v247\n"
  "v_add_f32 v248, v240, v241\n"
  "v_add_f32 v249, v242, v243\n"
  "v_add_f32 v250, v244, v245\n"
  "v_add_f32 v251, v246, v247\n"
  "s_add_u32 s80, s80, 0x1000\n"
  "v_add_f32 v248, v248, v249\n"
  "v_add_f32 v250, v250, v251\n"
  "v_add_f32 v248, v248, v250\n"
  "v_rcp_f32 v248, v248\n"
  "s_nop 0\n"
  "v_mul_f32 v240, v240, v248\n"
  "buffer_load_dwordx4 a[112:115], v2, s[20:23], s80 offen offset:0 sc1\n"
  "v_mul_f32 v241, v241, v248\n"
  "v_mul_f32 v242, v242, v248\n"
  "v_mul_f32 v243, v243, v248\n"
  "v_mul_f32 v244, v244, v248\n"
  "v_mul_f32 v245, v245, v248\n"
  "buffer_load_dwordx4 a[116:119], v2, s[20:23], s80 offen offset:1024 sc1\n"
  "v_mul_f32 v246, v246, v248\n"
  "v_mul_f32 v247, v247, v248\n"
  "v_cvt_pk_bf16_f32 v248, v240, v241\n"
  "v_cvt_pk_bf16_f32 v249, v242, v243\n"
  "v_cvt_pk_bf16_f32 v250, v244, v245\n"
  "buffer_load_dwordx4 a[120:123], v2, s[20:23], s80 offen offset:2048 sc1\n"
  "v_cvt_pk_bf16_f32 v251, v246, v247\n"
  "v_lshlrev_b32 v240, 16, v248\n"
  "v_and_b32 v241, 0xffff0000, v248\n"
  "v_lshlrev_b32 v242, 16, v249\n"
  "v_and_b32 v243, 0xffff0000, v249\n"
  "buffer_load_dwordx4 a[124:127], v2, s[20:23], s80 offen offset:3072 sc1\n"
  "v_lshlrev_b32 v244, 16, v250\n"
  "v_and_b32 v245, 0xffff0000, v250\n"
  "v_lshlrev_b32 v246, 16, v251\n"
  "v_and_b32 v247, 0xffff0000, v251\n"
  "v_pk_mul_f32 v[44:45], v[140:141], v[240:241] op_sel_hi:[1,0]\n"
  "s_add_u32 s80, s80, 0x1000\n"
  "v_pk_mul_f32 v[46:47], v[142:143], v[240:241] op_sel_hi:[1,0]\n"
  "v_pk_mul_f32 v[48:49], v[144:145], v[240:241] op_sel_hi:[1,0]\n"
  "v_pk_mul_f32 v[50:51], v[146:147], v[240:241] op_sel_hi:[1,0]\n"
  "v_pk_mul_f32 v[52:53], v[148:149], v[240:241] op_sel_hi:[1,0]\n"
  "v_pk_mul_f32 v[54:55], v[150:151], v[240:241] op_sel_hi:[1,0]\n"
  "v_pk_mul_f32 v[56:57], v[152:153], v[240:241] op_sel_hi:[1,0]\n"
  "v_pk_mul_f32 v[58:59], v[154:155], v[240:241] op_sel_hi:[1,0]\n"
  "v_pk_mul_f32 v[60:61], v[156:157], v[240:241] op_sel_hi:[1,0]\n"
  "v_pk_mul_f32 v[62:63], v[158:159], v[240:241] op_sel_hi:[1,0]\n"
  "v_pk_fma_f32 v[44:45], v[160:161], v[240:241], v[44:45] op_sel:[0,1,0]\n"
  "v_pk_fma_f32 v[46:47], v[162:163], v[240:241], v[46:47] op_sel:[0,1,0]\n"
  "v_pk_fma_f32 v[48:49], v[164:165], v[240:241], v[48:49] op_sel:[0,1,0]\n"
  "v_pk_fma_f32 v[50:51], v[166:167], v[240:241], v[50:51] op_sel:[0,1,0]\n"
  "v_pk_fma_f32 v[52:53], v[168:169], v[240:241], v[52:53] op_sel:[0,1,0]\n"
  "v_pk_fma_f32 v[54:55], v[170:171], v[240:241], v[54:55] op_sel:[0,1,0]\n"
  "v_pk_fma_f32 v[56:57], v[172:173], v[240:241], v[56:57] op_sel:[0,1,0]\n"
  "v_pk_fma_f32 v[58:59], v[174:175], v[240:241], v[58:59] op_sel:[0,1,0]\n"
  "v_pk_fma_f32 v[60:61], v[176:177], v[240:241], v[60:61] op_sel:[0,1,0]\n"
  "v_pk_fma_f32 v[62:63], v[178:179], v[240:241], v[62:63] op_sel:[0,1,0]\n"
  "s_waitcnt lgkmcnt(15)\n"
  "v_pk_fma_f32 v[44:45], v[180:181], v[242:243], v[44:45] op_sel_hi:[1,0,1]\n"
  "v_pk_fma_f32 v[46:47], v[182:183], v[242:243], v[46:47] op_sel_hi:[1,0,1]\n"
  "v_pk_fma_f32 v[48:49], v[184:185], v[242:243], v[48:49] op_sel_hi:[1,0,1]\n"
  "v_pk_fma_f32 v[50:51], v[186:187], v[242:243], v[50:51] op_sel_hi:[1,0,1]\n"
  "v_pk_fma_f32 v[52:53], v[188:189], v[242:243], v[52:53] op_sel_hi:[1,0,1]\n"
  "v_pk_fma_f32 v[54:55], v[190:191], v[242:243], v[54:55] op_sel_hi:[1,0,1]\n"
  "v_pk_fma_f32 v[56:57], v[192:193], v[242:243], v[56:57] op_sel_hi:[1,0,1]\n"
  "v_pk_fma_f32 v[58:59], v[194:195], v[242:243], v[58:59] op_sel_hi:[1,0,1]\n"
  "v_pk_fma_f32 v[60:61], v[196:197], v[242:243], v[60:61] op_sel_hi:[1,0,1]\n"
  "v_pk_fma_f32 v[62:63], v[198:199], v[242:243], v[62:63] op_sel_hi:[1,0,1]\n"
  "v_pk_fma_f32 v[44:45], v[200:201], v[242:243], v[44:45] op_sel:[0,1,0]\n"
  "v_pk_fma_f32 v[46:47], v[202:203], v[242:243], v[46:47] op_sel:[0,1,0]\n"
  "v_pk_fma_f32 v[48:49], v[204:205], v[242:243], v[48:49] op_sel:[0,1,0]\n"
  "v_pk_fma_f32 v[50:51], v[206:207], v[242:243], v[50:51] op_sel:[0,1,0]\n"
  "v_pk_fma_f32 v[52:53], v[208:209], v[242:243], v[52:53] op_sel:[0,1,0]\n"
  "v_pk_fma_f32 v[54:55], v[210:211], v[242:243], v[54:55] op_sel:[0,1,0]\n"
  "v_pk_fma_f32 v[56:57], v[212:213], v[242:243], v[56:57] op_sel:[0,1,0]\n"
  "v_pk_fma_f32 v[58:59], v[214:215], v[242:243], v[58:59] op_sel:[0,1,0]\n"
  "v_pk_fma_f32 v[60:61], v[216:217], v[242:243], v[60:61] op_sel:[0,1,0]\n"
  "v_pk_fma_f32 v[62:63], v[218:219], v[242:243], v[62:63] op_sel:[0,1,0]\n"
  "v_pk_fma_f32 v[44:45], v[220:221], v[244:245], v[44:45] op_sel_hi:[1,0,1]\n"
  "v_pk_fma_f32 v[46:47], v[222:223], v[244:245], v[46:47] op_sel_hi:[1,0,1]\n"
  "v_pk_fma_f32 v[48:49], v[224:225], v[244:245], v[48:49] op_sel_hi:[1,0,1]\n"
  "v_pk_fma_f32 v[50:51], v[226:227], v[244:245], v[50:51] op_sel_hi:[1,0,1]\n"
  "v_pk_fma_f32 v[52:53], v[228:229], v[244:245], v[52:53] op_sel_hi:[1,0,1]\n"
  "v_pk_fma_f32 v[54:55], v[230:231], v[244:245], v[54:55] op_sel_hi:[1,0,1]\n"
  "v_pk_fma_f32 v[56:57], v[232:233], v[244:245], v[56:57] op_sel_hi:[1,0,1]\n"
  "v_pk_fma_f32 v[58:59], v[234:235], v[244:245], v[58:59] op_sel_hi:[1,0,1]\n"
  "v_pk_fma_f32 v[60:61], v[236:237], v[244:245], v[60:61] op_sel_hi:[1,0,1]\n"
  "v_pk_fma_f32 v[62:63], v[238:239], v[244:245], v[62:63] op_sel_hi:[1,0,1]\n"
  "s_waitcnt lgkmcnt(14)\n"
  "v_pk_fma_f32 v[44:45], v[64:65], v[244:245], v[44:45] op_sel:[0,1,0]\n"
  "v_pk_fma_f32 v[46:47], v[66:67], v[244:245], v[46:47] op_sel:[0,1,0]\n"
  "s_waitcnt lgkmcnt(13)\n"
  "v_pk_fma_f32 v[48:49], v[68:69], v[244:245], v[48:49] op_sel:[0,1,0]\n"
  "v_pk_fma_f32 v[50:51], v[70:71], v[244:245], v[50:51] op_sel:[0,1,0]\n"
  "s_waitcnt lgkmcnt(12)\n"
  "v_pk_fma_f32 v[52:53], v[72:73], v[244:245], v[52:53] op_sel:[0,1,0]\n"
  "v_pk_fma_f32 v[54:55], v[74:75], v[244:245], v[54:55] op_sel:[0,1,0]\n"
  "s_waitcnt lgkmcnt(11)\n"
  "v_pk_fma_f32 v[56:57], v[76:77], v[244:245], v[56:57] op_sel:[0,1,0]\n"
  "v_pk_fma_f32 v[58:59], v[78:79], v[244:245], v[58:59] op_sel:[0,1,0]\n"
  "s_waitcnt lgkmcnt(10)\n"
  "v_pk_fma_f32 v[60:61], v[80:81], v[244:245], v[60:61] op_sel:[0,1,0]\n"
  "v_pk_fma_f32 v[62:63], v[82:83], v[244:245], v[62:63] op_sel:[0,1,0]\n"
  "s_waitcnt lgkmcnt(9)\n"
  "v_pk_fma_f32 v[44:45], v[84:85], v[246:247], v[44:45] op_sel_hi:[1,0,1]\n"
  "v_pk_fma_f32 v[46:47], v[86:87], v[246:247], v[46:47] op_sel_hi:[1,0,1]\n"
  "s_waitcnt lgkmcnt(8)\n"
  "v_pk_fma_f32 v[48:49], v[88:89], v[246:247], v[48:49] op_sel_hi:[1,0,1]\n"
  "v_pk_fma_f32 v[50:51], v[90:91], v[246:247], v[50:51] op_sel_hi:[1,0,1]\n"
  "s_waitcnt lgkmcnt(7)\n"
  "v_pk_fma_f32 v[52:53], v[92:93], v[246:247], v[52:53] op_sel_hi:[1,0,1]\n"
  "v_pk_fma_f32 v[54:55], v[94:95], v[246:247], v[54:55] op_sel_hi:[1,0,1]\n"
  "s_waitcnt lgkmcnt(6)\n"
  "v_pk_fma_f32 v[56:57], v[96:97], v[246:247], v[56:57] op_sel_hi:[1,0,1]\n"
  "v_pk_fma_f32 v[58:59], v[98:99], v[246:247], v[58:59] op_sel_hi:[1,0,1]\n"
  "s_waitcnt lgkmcnt(5)\n"
  "v_pk_fma_f32 v[60:61], v[100:101], v[246:247], v[60:61] op_sel_hi:[1,0,1]\n"
  "v_pk_fma_f32 v[62:63], v[102:103], v[246:247], v[62:63] op_sel_hi:[1,0,1]\n"
  "s_waitcnt lgkmcnt(4)\n"
  "v_pk_fma_f32 v[44:45], v[104:105], v[246:247], v[44:45] op_sel:[0,1,0]\n"
  "v_pk_fma_f32 v[46:47], v[106:107], v[246:247], v[46:47] op_sel:[0,1,0]\n"
  "s_waitcnt lgkmcnt(3)\n"
  "v_pk_fma_f32 v[48:49], v[108:109], v[246:247], v[48:49] op_sel:[0,1,0]\n"
  "v_pk_fma_f32 v[50:51], v[110:111], v[246:247], v[50:51] op_sel:[0,1,0]\n"
  "s_waitcnt lgkmcnt(2)\n"
  "v_pk_fma_f32 v[52:53], v[112:113], v[246:247], v[52:53] op_sel:[0,1,0]\n"
  "v_pk_fma_f32 v[54:55], v[114:115], v[246:247], v[54:55] op_sel:[0,1,0]\n"
  "s_waitcnt lgkmcnt(1)\n"
  "v_pk_fma_f32 v[56:57], v[116:117], v[246:247], v[56:57] op_sel:[0,1,0]\n"
  "v_pk_fma_f32 v[58:59], v[118:119], v[246:247], v[58:59] op_sel:[0,1,0]\n"
  "s_waitcnt lgkmcnt(0)\n"
  "v_pk_fma_f32 v[60:61], v[120:121], v[246:247], v[60:61] op_sel:[0,1,0]\n"
  "v_pk_fma_f32 v[62:63], v[122:123], v[246:247], v[62:63] op_sel:[0,1,0]\n"
  "v_cvt_pk_bf16_f32 v44, v44, v45\n"
  "v_cvt_pk_bf16_f32 v45, v46, v47\n"
  "v_cvt_pk_bf16_f32 v46, v48, v49\n"
  "v_cvt_pk_bf16_f32 v47, v50, v51\n"
  "v_cvt_pk_bf16_f32 v48, v52, v53\n"
  "v_cvt_pk_bf16_f32 v49, v54, v55\n"
  "v_cvt_pk_bf16_f32 v50, v56, v57\n"
  "v_cvt_pk_bf16_f32 v51, v58, v59\n"
  "v_cvt_pk_bf16_f32 v52, v60, v61\n"
  "v_cvt_pk_bf16_f32 v53, v62, v63\n"
  "v_accvgpr_read_b32 v32, a228\n"
  "v_accvgpr_read_b32 v33, a229\n"
  "v_accvgpr_read_b32 v34, a230\n"
  "v_accvgpr_read_b32 v35, a231\n"
  "v_accvgpr_read_b32 v36, a232\n"
  "buffer_store_dwordx2 v[44:45], v32, s[48:51], 0 offen sc1\n"
  "buffer_store_dwordx2 v[46:47], v33, s[48:51], 0 offen sc1\n"
  "buffer_store_dwordx2 v[48:49], v34, s[48:51], 0 offen sc1\n"
  "buffer_store_dwordx2 v[50:51], v35, s[48:51], 0 offen sc1\n"
  "buffer_store_dwordx2 v[52:53], v36, s[48:51], 0 offen sc1\n"
  "v_mov_b32 v14, s60\n"
  "v_mov_b32 v15, s61\n"
  "s_waitcnt vmcnt(21)\n"
  "s_waitcnt vmcnt(5)\n"
  "s_barrier\n"
  "LPH_%=:\n"
  "s_sub_u32 s84, s65, 2\n"
  "ds_read_b64_tr_b16 a[0:1], v1 offset:0\n"
  "ds_read_b64_tr_b16 a[2:3], v1 offset:2560\n"
  "ds_read_b64_tr_b16 a[4:5], v1 offset:64\n"
  "ds_read_b64_tr_b16 a[6:7], v1 offset:2624\n"
  "ds_read_b64_tr_b16 a[8:9], v1 offset:128\n"
  "ds_read_b64_tr_b16 a[10:11], v1 offset:2688\n"
  "ds_read_b64_tr_b16 a[12:13], v1 offset:192\n"
  "ds_read_b64_tr_b16 a[14:15], v1 offset:2752\n"
  "ds_read_b64_tr_b16 a[16:17], v1 offset:256\n"
  "ds_read_b64_tr_b16 a[18:19], v1 offset:2816\n"
  "s_waitcnt lgkmcnt(8)\n"
  "v_mfma_f32_32x32x16_bf16 v[32:47], a[64:67], a[0:3], 0\n"
  "ds_read_b64_tr_b16 a[20:21], v1 offset:5120\n"
  "ds_read_b64_tr_b16 a[22:23], v1 offset:7680\n"
  "ds_read_b64_tr_b16 a[24:25], v1 offset:5184\n"
  "s_waitcnt lgkmcnt(9)\n"
  "v_mfma_f32_32x32x16_bf16 v[48:63], a[64:67], a[4:7], 0\n"
  "s_waitcnt lgkmcnt(3)\n"
  "ds_read_b64_tr_b16 a[26:27], v1 offset:7744\n"
  "ds_read_b64_tr_b16 a[28:29], v1 offset:5248\n"
  "v_mfma_f32_32x32x16_bf16 v[64:79], a[64:67], a[8:11], 0\n"
  "ds_read_b64_tr_b16 a[30:31], v1 offset:7808\n"
  "ds_read_b64_tr_b16 a[32:33], v1 offset:5312\n"
  "ds_read_b64_tr_b16 a[34:35], v1 offset:7872\n"
  "v_mfma_f32_32x32x16_bf16 v[80:95], a[64:67], a[12:15], 0\n"
  "ds_read_b64_tr_b16 a[36:37], v1 offset:5376\n"
  "ds_read_b64_tr_b16 a[38:39], v1 offset:7936\n"
  "v_mfma_f32_32x32x16_bf16 v[96:111], a[64:67], a[16:19], 0\n"
  "s_waitcnt lgkmcnt(6)\n"
  "v_mfma_f32_32x32x16_bf16 v[32:47], a[68:71], a[20:23], v[32:47]\n"
  "ds_read_b64_tr_b16 a[0:1], v1 offset:10240\n"
  "ds_read_b64_tr_b16 a[2:3], v1 offset:12800\n"
  "ds_read_b64_tr_b16 a[4:5], v1 offset:10304\n"
  "v_mfma_f32_32x32x16_bf16 v[48:63], a[68:71], a[24:27], v[48:63]\n"
  "s_waitcnt lgkmcnt(3)\n"
  "ds_read_b64_tr_b16 a[6:7], v1 offset:12864\n"
  "ds_read_b64_tr_b16 a[8:9], v1 offset:10368\n"
  "v_mfma_f32_32x32x16_bf16 v[64:79], a[68:71], a[28:31], v[64:79]\n"
  "ds_read_b64_tr_b16 a[10:11], v1 offset:12928\n"
  "ds_read_b64_tr_b16 a[12:13], v1 offset:10432\n"
  "ds_read_b64_tr_b16 a[14:15], v1 offset:12992\n"
  "v_mfma_f32_32x32x16_bf16 v[80:95], a[68:71], a[32:35], v[80:95]\n"
  "ds_read_b64_tr_b16 a[16:17], v1 offset:10496\n"
  "ds_read_b64_tr_b16 a[18:19], v1 offset:13056\n"
  "v_mfma_f32_32x32x16_bf16 v[96:111], a[68:71], a[36:39], v[96:111]\n"
  "s_waitcnt lgkmcnt(6)\n"
  "v_mfma_f32_32x32x16_bf16 v[32:47], a[72:75], a[0:3], v[32:47]\n"
  "ds_read_b64_tr_b16 a[20:21], v1 offset:15360\n"
  "ds_read_b64_tr_b16 a[22:23], v1 offset:17920\n"
  "ds_read_b64_tr_b16 a[24:25], v1 offset:15424\n"
  "v_mfma_f32_32x32x16_bf16 v[48:63], a[72:75], a[4:7], v[48:63]\n"
  "s_waitcnt lgkmcnt(3)\n"
  "ds_read_b64_tr_b16 a[26:27], v1 offset:17984\n"
  "ds_read_b64_tr_b16 a[28:29], v1 offset:15488\n"
  "v_mfma_f32_32x32x16_bf16 v[64:79], a[72:75], a[8:11], v[64:79]\n"
  "ds_read_b64_tr_b16 a[30:31], v1 offset:18048\n"
  "ds_read_b64_tr_b16 a[32:33], v1 offset:15552\n"
  "ds_read_b64_tr_b16 a[34:35], v1 offset:18112\n"
  "v_mfma_f32_32x32x16_bf16 v[80:95], a[72:75], a[12:15], v[80:95]\n"
  "ds_read_b64_tr_b16 a[36:37], v1 offset:15616\n"
  "ds_read_b64_tr_b16 a[38:39], v1 offset:18176\n"
  "v_mfma_f32_32x32x16_bf16 v[96:111], a[72:75], a[16:19], v[96:111]\n"
  "s_waitcnt lgkmcnt(6)\n"
  ".p2align 6\n"
  "LK_%=:\n"
  "v_mfma_f32_32x32x16_bf16 v[32:47], a[76:79], a[20:23], v[32:47]\n"
  "s_waitcnt vmcnt(18) lgkmcnt(0)\n"
  "s_barrier\n"
  "ds_read_b64_tr_b16 a[0:1], v1 offset:20480\n"
  "ds_read_b64_tr_b16 a[2:3], v1 offset:23040\n"
  "ds_read_b64_tr_b16 a[4:5], v1 offset:20544\n"
  "v_mfma_f32_32x32x16_bf16 v[48:63], a[76:79], a[24:27], v[48:63]\n"
  "s_add_u32 m0, s64, 0x0\n"
  "ds_read_b64_tr_b16 a[6:7], v1 offset:23104\n"
  "ds_read_b64_tr_b16 a[8:9], v1 offset:20608\n"
  "v_mfma_f32_32x32x16_bf16 v[64:79], a[76:79], a[28:31], v[64:79]\n"
  "buffer_load_dwordx4 v3, s[24:27], s81 offen offset:0 lds\n"
  "ds_read_b64_tr_b16 a[10:11], v1 offset:23168\n"
  "ds_read_b64_tr_b16 a[12:13], v1 offset:20672\n"
  "ds_read_b64_tr_b16 a[14:15], v1 offset:23232\n"
  "v_mfma_f32_32x32x16_bf16 v[80:95], a[76:79], a[32:35], v[80:95]\n"
  "buffer_load_dwordx4 v4, s[24:27], s81 offen offset:1024 lds\n"
  "ds_read_b64_tr_b16 a[16:17], v1 offset:20736\n"
  "ds_read_b64_tr_b16 a[18:19], v1 offset:23296\n"
  "v_mfma_f32_32x32x16_bf16 v[96:111], a[76:79], a[36:39], v[96:111]\n"
  "s_waitcnt lgkmcnt(6)\n"
  "buffer_load_dwordx4 v5, s[24:27], s81 offen offset:2048 lds\n"
  "v_mfma_f32_32x32x16_bf16 v[32:47], a[80:83], a[0:3], v[32:47]\n"
  "ds_read_b64_tr_b16 a[20:21], v1 offset:25600\n"
  "ds_read_b64_tr_b16 a[22:23], v1 offset:28160\n"
  "ds_read_b64_tr_b16 a[24:25], v1 offset:25664\n"
  "v_mfma_f32_32x32x16_bf16 v[48:63], a[80:83], a[4:7], v[48:63]\n"
  "s_waitcnt lgkmcnt(3)\n"
  "buffer_load_dwordx4 v6, s[24:27], s81 offen offset:3072 lds\n"
  "ds_read_b64_tr_b16 a[26:27], v1 offset:28224\n"
  "ds_read_b64_tr_b16 a[28:29], v1 offset:25728\n"
  "v_mfma_f32_32x32x16_bf16 v[64:79], a[80:83], a[8:11], v[64:79]\n"
  "s_add_u32 m0, s64, 0x1000\n"
  "ds_read_b64_tr_b16 a[30:31], v1 offset:28288\n"
  "ds_read_b64_tr_b16 a[32:33], v1 offset:25792\n"
  "ds_read_b64_tr_b16 a[34:35], v1 offset:28352\n"
  "v_mfma_f32_32x32x16_bf16 v[80:95], a[80:83], a[12:15], v[80:95]\n"
  "buffer_load_dwordx4 v7, s[24:27], s81 offen offset:0 lds\n"
  "ds_read_b64_tr_b16 a[36:37], v1 offset:25856\n"
  "ds_read_b64_tr_b16 a[38:39], v1 offset:28416\n"
  "v_mfma_f32_32x32x16_bf16 v[96:111], a[80:83], a[16:19], v[96:111]\n"
  "s_waitcnt lgkmcnt(6)\n"
  "s_add_u32 s81, s81, 0x14000\n"
  "v_mfma_f32_32x32x16_bf16 v[32:47], a[84:87], a[20:23], v[32:47]\n"
  "buffer_load_dwordx4 a[64:67], v2, s[20:23], s80 offen offset:0 sc1\n"
  "ds_read_b64_tr_b16 a[0:1], v1 offset:30720\n"
  "ds_read_b64_tr_b16 a[2:3], v1 offset:33280\n"
  "ds_read_b64_tr_b16 a[4:5], v1 offset:30784\n"
  "v_mfma_f32_32x32x16_bf16 v[48:63], a[84:87], a[24:27], v[48:63]\n"
  "s_waitcnt lgkmcnt(3)\n"
  "buffer_load_dwordx4 a[68:71], v2, s[20:23], s80 offen offset:1024 sc1\n"
  "ds_read_b64_tr_b16 a[6:7], v1 offset:33344\n"
  "ds_read_b64_tr_b16 a[8:9], v1 offset:30848\n"
  "v_mfma_f32_32x32x16_bf16 v[64:79], a[84:87], a[28:31], v[64:79]\n"
  "ds_read_b64_tr_b16 a[10:11], v1 offset:33408\n"
  "ds_read_b64_tr_b16 a[12:13], v1 offset:30912\n"
  "ds_read_b64_tr_b16 a[14:15], v1 offset:33472\n"
  "v_mfma_f32_32x32x16_bf16 v[80:95], a[84:87], a[32:35], v[80:95]\n"
  "buffer_load_dwordx4 a[72:75], v2, s[20:23], s80 offen offset:2048 sc1\n"
  "ds_read_b64_tr_b16 a[16:17], v1 offset:30976\n"
  "ds_read_b64_tr_b16 a[18:19], v1 offset:33536\n"
  "v_mfma_f32_32x32x16_bf16 v[96:111], a[84:87], a[36:39], v[96:111]\n"
  "s_waitcnt lgkmcnt(6)\n"
  "buffer_load_dwordx4 a[76:79], v2, s[20:23], s80 offen offset:3072 sc1\n"
  "v_mfma_f32_32x32x16_bf16 v[32:47], a[88:91], a[0:3], v[32:47]\n"
  "ds_read_b64_tr_b16 a[20:21], v1 offset:35840\n"
  "ds_read_b64_tr_b16 a[22:23], v1 offset:38400\n"
  "ds_read_b64_tr_b16 a[24:25], v1 offset:35904\n"
  "v_mfma_f32_32x32x16_bf16 v[48:63], a[88:91], a[4:7], v[48:63]\n"
  "s_waitcnt lgkmcnt(3)\n"
  "s_add_u32 s80, s80, 0x1000\n"
  "ds_read_b64_tr_b16 a[26:27], v1 offset:38464\n"
  "ds_read_b64_tr_b16 a[28:29], v1 offset:35968\n"
  "v_mfma_f32_32x32x16_bf16 v[64:79], a[88:91], a[8:11], v[64:79]\n"
  "ds_read_b64_tr_b16 a[30:31], v1 offset:38528\n"
  "ds_read_b64_tr_b16 a[32:33], v1 offset:36032\n"
  "ds_read_b64_tr_b16 a[34:35], v1 offset:38592\n"
  "v_mfma_f32_32x32x16_bf16 v[80:95], a[88:91], a[12:15], v[80:95]\n"
  "s_sub_u32 s84, s84, 1\n"
  "ds_read_b64_tr_b16 a[36:37], v1 offset:36096\n"
  "ds_read_b64_tr_b16 a[38:39], v1 offset:38656\n"
  "v_mfma_f32_32x32x16_bf16 v[96:111], a[88:91], a[16:19], v[96:111]\n"
  "s_waitcnt lgkmcnt(6)\n"
  "s_cbranch_scc1 LT0_%=\n"
  "v_mfma_f32_32x32x16_bf16 v[32:47], a[92:95], a[20:23], v[32:47]\n"
  "s_waitcnt vmcnt(18) lgkmcnt(0)\n"
  "s_barrier\n"
  "ds_read_b64_tr_b16 a[0:1], v1 offset:40960\n"
  "ds_read_b64_tr_b16 a[2:3], v1 offset:43520\n"
  "ds_read_b64_tr_b16 a[4:5], v1 offset:41024\n"
  "v_mfma_f32_32x32x16_bf16 v[48:63], a[92:95], a[24:27], v[48:63]\n"
  "s_add_u32 m0, s64, 0x5000\n"
  "ds_read_b64_tr_b16 a[6:7], v1 offset:43584\n"
  "ds_read_b64_tr_b16 a[8:9], v1 offset:41088\n"
  "v_mfma_f32_32x32x16_bf16 v[64:79], a[92:95], a[28:31], v[64:79]\n"
  "buffer_load_dwordx4 v3, s[24:27], s81 offen offset:0 lds\n"
  "ds_read_b64_tr_b16 a[10:11], v1 offset:43648\n"
  "ds_read_b64_tr_b16 a[12:13], v1 offset:41152\n"
  "ds_read_b64_tr_b16 a[14:15], v1 offset:43712\n"
  "v_mfma_f32_32x32x16_bf16 v[80:95], a[92:95], a[32:35], v[80:95]\n"
  "buffer_load_dwordx4 v4, s[24:27], s81 offen offset:1024 lds\n"
  "ds_read_b64_tr_b16 a[16:17], v1 offset:41216\n"
  "ds_read_b64_tr_b16 a[18:19], v1 offset:43776\n"
  "v_mfma_f32_32x32x16_bf16 v[96:111], a[92:95], a[36:39], v[96:111]\n"
  "s_waitcnt lgkmcnt(6)\n"
  "buffer_load_dwordx4 v5, s[24:27], s81 offen offset:2048 lds\n"
  "v_mfma_f32_32x32x16_bf16 v[32:47], a[96:99], a[0:3], v[32:47]\n"
  "ds_read_b64_tr_b16 a[20:21], v1 offset:46080\n"
  "ds_read_b64_tr_b16 a[22:23], v1 offset:48640\n"
  "ds_read_b64_tr_b16 a[24:25], v1 offset:46144\n"
  "v_mfma_f32_32x32x16_bf16 v[48:63], a[96:99], a[4:7], v[48:63]\n"
  "s_waitcnt lgkmcnt(3)\n"
  "buffer_load_dwordx4 v6, s[24:27], s81 offen offset:3072 lds\n"
  "ds_read_b64_tr_b16 a[26:27], v1 offset:48704\n"
  "ds_read_b64_tr_b16 a[28:29], v1 offset:46208\n"
  "v_mfma_f32_32x32x16_bf16 v[64:79], a[96:99], a[8:11], v[64:79]\n"
  "s_add_u32 m0, s64, 0x6000\n"
  "ds_read_b64_tr_b16 a[30:31], v1 offset:48768\n"
  "ds_read_b64_tr_b16 a[32:33], v1 offset:46272\n"
  "ds_read_b64_tr_b16 a[34:35], v1 offset:48832\n"
  "v_mfma_f32_32x32x16_bf16 v[80:95], a[96:99], a[12:15], v[80:95]\n"
  "buffer_load_dwordx4 v7, s[24:27], s81 offen offset:0 lds\n"
  "ds_read_b64_tr_b16 a[36:37], v1 offset:46336\n"
  "ds_read_b64_tr_b16 a[38:39], v1 offset:48896\n"
  "v_mfma_f32_32x32x16_bf16 v[96:111], a[96:99], a[16:19], v[96:111]\n"
  "s_waitcnt lgkmcnt(6)\n"
  "s_add_u32 s81, s81, 0x14000\n"
  "v_mfma_f32_32x32x16_bf16 v[32:47], a[100:103], a[20:23], v[32:47]\n"
  "buffer_load_dwordx4 a[80:83], v2, s[20:23], s80 offen offset:0 sc1\n"
  "ds_read_b64_tr_b16 a[0:1], v1 offset:51200\n"
  "ds_read_b64_tr_b16 a[2:3], v1 offset:53760\n"
  "ds_read_b64_tr_b16 a[4:5], v1 offset:51264\n"
  "v_mfma_f32_32x32x16_bf16 v[48:63], a[100:103], a[24:27], v[48:63]\n"
  "s_waitcnt lgkmcnt(3)\n"
  "buffer_load_dwordx4 a[84:87], v2, s[20:23], s80 offen offset:1024 sc1\n"
  "ds_read_b64_tr_b16 a[6:7], v1 offset:53824\n"
  "ds_read_b64_tr_b16 a[8:9], v1 offset:51328\n"
  "v_mfma_f32_32x32x16_bf16 v[64:79], a[100:103], a[28:31], v[64:79]\n"
  "ds_read_b64_tr_b16 a[10:11], v1 offset:53888\n"
  "ds_read_b64_tr_b16 a[12:13], v1 offset:51392\n"
  "ds_read_b64_tr_b16 a[14:15], v1 offset:53952\n"
  "v_mfma_f32_32x32x16_bf16 v[80:95], a[100:103], a[32:35], v[80:95]\n"
  "buffer_load_dwordx4 a[88:91], v2, s[20:23], s80 offen offset:2048 sc1\n"
  "ds_read_b64_tr_b16 a[16:17], v1 offset:51456\n"
  "ds_read_b64_tr_b16 a[18:19], v1 offset:54016\n"
  "v_mfma_f32_32x32x16_bf16 v[96:111], a[100:103], a[36:39], v[96:111]\n"
  "s_waitcnt lgkmcnt(6)\n"
  "buffer_load_dwordx4 a[92:95], v2, s[20:23], s80 offen offset:3072 sc1\n"
  "v_mfma_f32_32x32x16_bf16 v[32:47], a[104:107], a[0:3], v[32:47]\n"
  "ds_read_b64_tr_b16 a[20:21], v1 offset:56320\n"
  "ds_read_b64_tr_b16 a[22:23], v1 offset:58880\n"
  "ds_read_b64_tr_b16 a[24:25], v1 offset:56384\n"
  "v_mfma_f32_32x32x16_bf16 v[48:63], a[104:107], a[4:7], v[48:63]\n"
  "s_waitcnt lgkmcnt(3)\n"
  "s_add_u32 s80, s80, 0x1000\n"
  "ds_read_b64_tr_b16 a[26:27], v1 offset:58944\n"
  "ds_read_b64_tr_b16 a[28:29], v1 offset:56448\n"
  "v_mfma_f32_32x32x16_bf16 v[64:79], a[104:107], a[8:11], v[64:79]\n"
  "ds_read_b64_tr_b16 a[30:31], v1 offset:59008\n"
  "ds_read_b64_tr_b16 a[32:33], v1 offset:56512\n"
  "ds_read_b64_tr_b16 a[34:35], v1 offset:59072\n"
  "v_mfma_f32_32x32x16_bf16 v[80:95], a[104:107], a[12:15], v[80:95]\n"
  "s_sub_u32 s84, s84, 1\n"
  "ds_read_b64_tr_b16 a[36:37], v1 offset:56576\n"
  "ds_read_b64_tr_b16 a[38:39], v1 offset:59136\n"
  "v_mfma_f32_32x32x16_bf16 v[96:111], a[104:107], a[16:19], v[96:111]\n"
  "s_waitcnt lgkmcnt(6)\n"
  "s_cbranch_scc1 LT1_%=\n"
  "v_mfma_f32_32x32x16_bf16 v[32:47], a[108:111], a[20:23], v[32:47]\n"
  "s_waitcnt vmcnt(18) lgkmcnt(0)\n"
  "s_barrier\n"
  "ds_read_b64_tr_b16 a[0:1], v0 offset:0\n"
  "ds_read_b64_tr_b16 a[2:3], v0 offset:2560\n"
  "ds_read_b64_tr_b16 a[4:5], v0 offset:64\n"
  "v_mfma_f32_32x32x16_bf16 v[48:63], a[108:111], a[24:27], v[48:63]\n"
  "s_add_u32 m0, s64, 0xa000\n"
  "ds_read_b64_tr_b16 a[6:7], v0 offset:2624\n"
  "ds_read_b64_tr_b16 a[8:9], v0 offset:128\n"
  "v_mfma_f32_32x32x16_bf16 v[64:79], a[108:111], a[28:31], v[64:79]\n"
  "buffer_load_dwordx4 v3, s[24:27], s81 offen offset:0 lds\n"
  "ds_read_b64_tr_b16 a[10:11], v0 offset:2688\n"
  "ds_read_b64_tr_b16 a[12:13], v0 offset:192\n"
  "ds_read_b64_tr_b16 a[14:15], v0 offset:2752\n"
  "v_mfma_f32_32x32x16_bf16 v[80:95], a[108:111], a[32:35], v[80:95]\n"
  "buffer_load_dwordx4 v4, s[24:27], s81 offen offset:1024 lds\n"
  "ds_read_b64_tr_b16 a[16:17], v0 offset:256\n"
  "ds_read_b64_tr_b16 a[18:19], v0 offset:2816\n"
  "v_mfma_f32_32x32x16_bf16 v[96:111], a[108:111], a[36:39], v[96:111]\n"
  "s_waitcnt lgkmcnt(6)\n"
  "buffer_load_dwordx4 v5, s[24:27], s81 offen offset:2048 lds\n"
  "v_mfma_f32_32x32x16_bf16 v[32:47], a[112:115], a[0:3], v[32:47]\n"
  "ds_read_b64_tr_b16 a[20:21], v0 offset:5120\n"
  "ds_read_b64_tr_b16 a[22:23], v0 offset:7680\n"
  "ds_read_b64_tr_b16 a[24:25], v0 offset:5184\n"
  "v_mfma_f32_32x32x16_bf16 v[48:63], a[112:115], a[4:7], v[48:63]\n"
  "s_waitcnt lgkmcnt(3)\n"
  "buffer_load_dwordx4 v6, s[24:27], s81 offen offset:3072 lds\n"
  "ds_read_b64_tr_b16 a[26:27], v0 offset:7744\n"
  "ds_read_b64_tr_b16 a[28:29], v0 offset:5248\n"
  "v_mfma_f32_32x32x16_bf16 v[64:79], a[112:115], a[8:11], v[64:79]\n"
  "s_add_u32 m0, s64, 0xb000\n"
  "ds_read_b64_tr_b16 a[30:31], v0 offset:7808\n"
  "ds_read_b64_tr_b16 a[32:33], v0 offset:5312\n"
  "ds_read_b64_tr_b16 a[34:35], v0 offset:7872\n"
  "v_mfma_f32_32x32x16_bf16 v[80:95], a[112:115], a[12:15], v[80:95]\n"
  "buffer_load_dwordx4 v7, s[24:27], s81 offen offset:0 lds\n"
  "ds_read_b64_tr_b16 a[36:37], v0 offset:5376\n"
  "ds_read_b64_tr_b16 a[38:39], v0 offset:7936\n"
  "v_mfma_f32_32x32x16_bf16 v[96:111], a[112:115], a[16:19], v[96:111]\n"
  "s_waitcnt lgkmcnt(6)\n"
  "s_add_u32 s81, s81, 0x14000\n"
  "v_mfma_f32_32x32x16_bf16 v[32:47], a[116:119], a[20:23], v[32:47]\n"
  "buffer_load_dwordx4 a[96:99], v2, s[20:23], s80 offen offset:0 sc1\n"
  "ds_read_b64_tr_b16 a[0:1], v0 offset:10240\n"
  "ds_read_b64_tr_b16 a[2:3], v0 offset:12800\n"
  "ds_read_b64_tr_b16 a[4:5], v0 offset:10304\n"
  "v_mfma_f32_32x32x16_bf16 v[48:63], a[116:119], a[24:27], v[48:63]\n"
  "s_waitcnt lgkmcnt(3)\n"
  "buffer_load_dwordx4 a[100:103], v2, s[20:23], s80 offen offset:1024 sc1\n"
  "ds_read_b64_tr_b16 a[6:7], v0 offset:12864\n"
  "ds_read_b64_tr_b16 a[8:9], v0 offset:10368\n"
  "v_mfma_f32_32x32x16_bf16 v[64:79], a[116:119], a[28:31], v[64:79]\n"
  "ds_read_b64_tr_b16 a[10:11], v0 offset:12928\n"
  "ds_read_b64_tr_b16 a[12:13], v0 offset:10432\n"
  "ds_read_b64_tr_b16 a[14:15], v0 offset:12992\n"
  "v_mfma_f32_32x32x16_bf16 v[80:95], a[116:119], a[32:35], v[80:95]\n"
  "buffer_load_dwordx4 a[104:107], v2, s[20:23], s80 offen offset:2048 sc1\n"
  "ds_read_b64_tr_b16 a[16:17], v0 offset:10496\n"
  "ds_read_b64_tr_b16 a[18:19], v0 offset:13056\n"
  "v_mfma_f32_32x32x16_bf16 v[96:111], a[116:119], a[36:39], v[96:111]\n"
  "s_waitcnt lgkmcnt(6)\n"
  "buffer_load_dwordx4 a[108:111], v2, s[20:23], s80 offen offset:3072 sc1\n"
  "v_mfma_f32_32x32x16_bf16 v[32:47], a[120:123], a[0:3], v[32:47]\n"
  "ds_read_b64_tr_b16 a[20:21], v0 offset:15360\n"
  "ds_read_b64_tr_b16 a[22:23], v0 offset:17920\n"
  "ds_read_b64_tr_b16 a[24:25], v0 offset:15424\n"
  "v_mfma_f32_32x32x16_bf16 v[48:63], a[120:123], a[4:7], v[48:63]\n"
  "s_waitcnt lgkmcnt(3)\n"
  "ds_read_b64_tr_b16 a[26:27], v0 offset:17984\n"
  "ds_read_b64_tr_b16 a[28:29], v0 offset:15488\n"
  "s_add_u32 s80, s80, 0x1000\n"
  "s_mov_b64 exec, 1\n"
  "buffer_store_dwordx2 v[14:15], off, s[44:47], s74 sc1 \n"
  "s_mov_b64 exec, -1\n"
  "v_mfma_f32_32x32x16_bf16 v[64:79], a[120:123], a[8:11], v[64:79]\n"
  "s_mov_b32 s74, 0x40000000\n"
  "ds_read_b64_tr_b16 a[30:31], v0 offset:18048\n"
  "ds_read_b64_tr_b16 a[32:33], v0 offset:15552\n"
  "ds_read_b64_tr_b16 a[34:35], v0 offset:18112\n"
  "v_mfma_f32_32x32x16_bf16 v[80:95], a[120:123], a[12:15], v[80:95]\n"
  "s_sub_u32 s84, s84, 1\n"
  "ds_read_b64_tr_b16 a[36:37], v0 offset:15616\n"
  "ds_read_b64_tr_b16 a[38:39], v0 offset:18176\n"
  "v_mfma_f32_32x32x16_bf16 v[96:111], a[120:123], a[16:19], v[96:111]\n"
  "s_waitcnt lgkmcnt(6)\n"
  "s_cbranch_scc1 LT2_%=\n"
  "v_mfma_f32_32x32x16_bf16 v[32:47], a[124:127], a[20:23], v[32:47]\n"
  "s_waitcnt vmcnt(18) lgkmcnt(0)\n"
  "s_barrier\n"
  "ds_read_b64_tr_b16 a[0:1], v1 offset:0\n"
  "ds_read_b64_tr_b16 a[2:3], v1 offset:2560\n"
  "ds_read_b64_tr_b16 a[4:5], v1 offset:64\n"
  "v_mfma_f32_32x32x16_bf16 v[48:63], a[124:127], a[24:27], v[48:63]\n"
  "s_add_u32 m0, s64, 0xf000\n"
  "ds_read_b64_tr_b16 a[6:7], v1 offset:2624\n"
  "ds_read_b64_tr_b16 a[8:9], v1 offset:128\n"
  "v_mfma_f32_32x32x16_bf16 v[64:79], a[124:127], a[28:31], v[64:79]\n"
  "buffer_load_dwordx4 v3, s[24:27], s81 offen offset:0 lds\n"
  "ds_read_b64_tr_b16 a[10:11], v1 offset:2688\n"
  "ds_read_b64_tr_b16 a[12:13], v1 offset:192\n"
  "ds_read_b64_tr_b16 a[14:15], v1 offset:2752\n"
  "v_mfma_f32_32x32x16_bf16 v[80:95], a[124:127], a[32:35], v[80:95]\n"
  "buffer_load_dwordx4 v4, s[24:27], s81 offen offset:1024 lds\n"
  "ds_read_b64_tr_b16 a[16:17], v1 offset:256\n"
  "ds_read_b64_tr_b16 a[18:19], v1 offset:2816\n"
  "v_mfma_f32_32x32x16_bf16 v[96:111], a[124:127], a[36:39], v[96:111]\n"
  "s_waitcnt lgkmcnt(6)\n"
  "buffer_load_dwordx4 v5, s[24:27], s81 offen offset:2048 lds\n"
  "v_mfma_f32_32x32x16_bf16 v[32:47], a[64:67], a[0:3], v[32:47]\n"
  "ds_read_b64_tr_b16 a[20:21], v1 offset:5120\n"
  "ds_read_b64_tr_b16 a[22:23], v1 offset:7680\n"
  "ds_read_b64_tr_b16 a[24:25], v1 offset:5184\n"
  "v_mfma_f32_32x32x16_bf16 v[48:63], a[64:67], a[4:7], v[48:63]\n"
  "s_waitcnt lgkmcnt(3)\n"
  "buffer_load_dwordx4 v6, s[24:27], s81 offen offset:3072 lds\n"
  "ds_read_b64_tr_b16 a[26:27], v1 offset:7744\n"
  "ds_read_b64_tr_b16 a[28:29], v1 offset:5248\n"
  "v_mfma_f32_32x32x16_bf16 v[64:79], a[64:67], a[8:11], v[64:79]\n"
  "s_add_u32 m0, s64, 0x10000\n"
  "ds_read_b64_tr_b16 a[30:31], v1 offset:7808\n"
  "ds_read_b64_tr_b16 a[32:33], v1 offset:5312\n"
  "ds_read_b64_tr_b16 a[34:35], v1 offset:7872\n"
  "v_mfma_f32_32x32x16_bf16 v[80:95], a[64:67], a[12:15], v[80:95]\n"
  "buffer_load_dwordx4 v7, s[24:27], s81 offen offset:0 lds\n"
  "ds_read_b64_tr_b16 a[36:37], v1 offset:5376\n"
  "ds_read_b64_tr_b16 a[38:39], v1 offset:7936\n"
  "v_mfma_f32_32x32x16_bf16 v[96:111], a[64:67], a[16:19], v[96:111]\n"
  "s_waitcnt lgkmcnt(6)\n"
  "s_add_u32 s81, s81, 0x14000\n"
  "v_mfma_f32_32x32x16_bf16 v[32:47], a[68:71], a[20:23], v[32:47]\n"
  "buffer_load_dwordx4 a[112:115], v2, s[20:23], s80 offen offset:0 sc1\n"
  "ds_read_b64_tr_b16 a[0:1], v1 offset:10240\n"
  "ds_read_b64_tr_b16 a[2:3], v1 offset:12800\n"
  "ds_read_b64_tr_b16 a[4:5], v1 offset:10304\n"
  "v_mfma_f32_32x32x16_bf16 v[48:63], a[68:71], a[24:27], v[48:63]\n"
  "s_waitcnt lgkmcnt(3)\n"
  "buffer_load_dwordx4 a[116:119], v2, s[20:23], s80 offen offset:1024 sc1\n"
  "ds_read_b64_tr_b16 a[6:7], v1 offset:12864\n"
  "ds_read_b64_tr_b16 a[8:9], v1 offset:10368\n"
  "v_mfma_f32_32x32x16_bf16 v[64:79], a[68:71], a[28:31], v[64:79]\n"
  "ds_read_b64_tr_b16 a[10:11], v1 offset:12928\n"
  "ds_read_b64_tr_b16 a[12:13], v1 offset:10432\n"
  "ds_read_b64_tr_b16 a[14:15], v1 offset:12992\n"
  "v_mfma_f32_32x32x16_bf16 v[80:95], a[68:71], a[32:35], v[80:95]\n"
  "buffer_load_dwordx4 a[120:123], v2, s[20:23], s80 offen offset:2048 sc1\n"
  "ds_read_b64_tr_b16 a[16:17], v1 offset:10496\n"
  "ds_read_b64_tr_b16 a[18:19], v1 offset:13056\n"
  "v_mfma_f32_32x32x16_bf16 v[96:111], a[68:71], a[36:39], v[96:111]\n"
  "s_waitcnt lgkmcnt(6)\n"
  "buffer_load_dwordx4 a[124:127], v2, s[20:23], s80 offen offset:3072 sc1\n"
  "v_mfma_f32_32x32x16_bf16 v[32:47], a[72:75], a[0:3], v[32:47]\n"
  "ds_read_b64_tr_b16 a[20:21], v1 offset:15360\n"
  "ds_read_b64_tr_b16 a[22:23], v1 offset:17920\n"
  "ds_read_b64_tr_b16 a[24:25], v1 offset:15424\n"
  "v_mfma_f32_32x32x16_bf16 v[48:63], a[72:75], a[4:7], v[48:63]\n"
  "s_waitcnt lgkmcnt(3)\n"
  "s_add_u32 s80, s80, 0x1000\n"
  "ds_read_b64_tr_b16 a[26:27], v1 offset:17984\n"
  "ds_read_b64_tr_b16 a[28:29], v1 offset:15488\n"
  "v_mfma_f32_32x32x16_bf16 v[64:79], a[72:75], a[8:11], v[64:79]\n"
  "ds_read_b64_tr_b16 a[30:31], v1 offset:18048\n"
  "ds_read_b64_tr_b16 a[32:33], v1 offset:15552\n"
  "ds_read_b64_tr_b16 a[34:35], v1 offset:18112\n"
  "v_mfma_f32_32x32x16_bf16 v[80:95], a[72:75], a[12:15], v[80:95]\n"
  "s_sub_u32 s84, s84, 1\n"
  "ds_read_b64_tr_b16 a[36:37], v1 offset:15616\n"
  "ds_read_b64_tr_b16 a[38:39], v1 offset:18176\n"
  "v_mfma_f32_32x32x16_bf16 v[96:111], a[72:75], a[16:19], v[96:111]\n"
  "s_waitcnt lgkmcnt(6)\n"
  "s_cbranch_scc0 LK_%=\n"
  "v_mfma_f32_32x32x16_bf16 v[32:47], a[76:79], a[20:23], v[32:47]\n"
  "v_mfma_f32_32x32x16_bf16 v[48:63], a[76:79], a[24:27], v[48:63]\n"
  "s_waitcnt lgkmcnt(0)\n"
  "v_mfma_f32_32x32x16_bf16 v[64:79], a[76:79], a[28:31], v[64:79]\n"
  "v_mfma_f32_32x32x16_bf16 v[80:95], a[76:79], a[32:35], v[80:95]\n"
  "v_mfma_f32_32x32x16_bf16 v[96:111], a[76:79], a[36:39], v[96:111]\n"
  "LTE_%=:\n"
  "s_cmp_eq_u32 s87, 1\n"
  "s_cbranch_scc1 LBOUT_%=\n"
  "s_add_u32 s85, s68, 3072\n"
  "s_add_u32 s86, s68, 6144\n"
  "s_cmp_lg_u32 s66, 3\n"
  "s_cbranch_scc1 LPL_%=\n"
  "v_and_b32 v12, 0x78, v12\n"
  "buffer_load_dwordx2 v[14:15], v12, s[44:47], s77 offen sc1\n"
  "s_mov_b32 s87, 1\n"
  "s_mov_b32 s65, 10\n"
  "s_mov_b32 s80, 0\n"
  "s_mov_b32 s81, 0\n"
  "s_mov_b64 s[20:21], s[28:29]\n"
  "s_mov_b64 s[22:23], s[30:31]\n"
  "s_mov_b64 s[24:25], s[32:33]\n"
  "s_mov_b64 s[26:27], s[34:35]\n"
  "s_waitcnt vmcnt(1)\n"
  "s_barrier\n"
  "s_add_u32 m0, s64, 0x0\n"
  "s_nop 0\n"
  "buffer_load_dwordx4 v3, s[24:27], s81 offen offset:0 lds\n"
  "buffer_load_dwordx4 v4, s[24:27], s81 offen offset:1024 lds\n"
  "buffer_load_dwordx4 v5, s[24:27], s81 offen offset:2048 lds\n"
  "buffer_load_dwordx4 v6, s[24:27], s81 offen offset:3072 lds\n"
  "s_add_u32 m0, s64, 0x1000\n"
  "s_nop 0\n"
  "buffer_load_dwordx4 v7, s[24:27], s81 offen offset:0 lds\n"
  "s_add_u32 s81, s81, 0x14000\n"
  "s_add_u32 m0, s64, 0x5000\n"
  "s_nop 0\n"
  "buffer_load_dwordx4 v3, s[24:27], s81 offen offset:0 lds\n"
  "buffer_load_dwordx4 v4, s[24:27], s81 offen offset:1024 lds\n"
  "buffer_load_dwordx4 v5, s[24:27], s81 offen offset:2048 lds\n"
  "buffer_load_dwordx4 v6, s[24:27], s81 offen offset:3072 lds\n"
  "s_add_u32 m0, s64, 0x6000\n"
  "s_nop 0\n"
  "buffer_load_dwordx4 v7, s[24:27], s81 offen offset:0 lds\n"
  "s_add_u32 s81, s81, 0x14000\n"
  "s_add_u32 m0, s64, 0xa000\n"
  "s_nop 0\n"
  "buffer_load_dwordx4 v3, s[24:27], s81 offen offset:0 lds\n"
  "buffer_load_dwordx4 v4, s[24:27], s81 offen offset:1024 lds\n"
  "buffer_load_dwordx4 v5, s[24:27], s81 offen offset:2048 lds\n"
  "buffer_load_dwordx4 v6, s[24:27], s81 offen offset:3072 lds\n"
  "s_add_u32 m0, s64, 0xb000\n"
  "s_nop 0\n"
  "buffer_load_dwordx4 v7, s[24:27], s81 offen offset:0 lds\n"
  "s_add_u32 s81, s81, 0x14000\n"
  "s_add_u32 m0, s64, 0xf000\n"
  "s_nop 0\n"
  "buffer_load_dwordx4 v3, s[24:27], s81 offen offset:0 lds\n"
  "buffer_load_dwordx4 v4, s[24:27], s81 offen offset:1024 lds\n"
  "buffer_load_dwordx4 v5, s[24:27], s81 offen offset:2048 lds\n"
  "buffer_load_dwordx4 v6, s[24:27], s81 offen offset:3072 lds\n"
  "s_add_u32 m0, s64, 0x10000\n"
  "s_nop 0\n"
  "buffer_load_dwordx4 v7, s[24:27], s81 offen offset:0 lds\n"
  "s_add_u32 s81, s81, 0x14000\n"
  "s_waitcnt vmcnt(20)\n"
  "v_cmp_ne_u64 vcc, s[60:61], v[14:15]\n"
  "s_nop 0\n"
  "s_cbranch_vccnz PO_%=\n"
  "PO_%=_r:\n"
  "buffer_load_dwordx4 a[64:67], v2, s[20:23], s80 offen offset:0 sc1\n"
  "buffer_load_dwordx4 a[68:71], v2, s[20:23], s80 offen offset:1024 sc1\n"
  "buffer_load_dwordx4 a[72:75], v2, s[20:23], s80 offen offset:2048 sc1\n"
  "buffer_load_dwordx4 a[76:79], v2, s[20:23], s80 offen offset:3072 sc1\n"
  "s_add_u32 s80, s80, 0x1000\n"
  "buffer_load_dwordx4 a[80:83], v2, s[20:23], s80 offen offset:0 sc1\n"
  "buffer_load_dwordx4 a[84:87], v2, s[20:23], s80 offen offset:1024 sc1\n"
  "buffer_load_dwordx4 a[88:91], v2, s[20:23], s80 offen offset:2048 sc1\n"
  "buffer_load_dwordx4 a[92:95], v2, s[20:23], s80 offen offset:3072 sc1\n"
  "s_add_u32 s80, s80, 0x1000\n"
  "buffer_load_dwordx4 a[96:99], v2, s[20:23], s80 offen offset:0 sc1\n"
  "buffer_load_dwordx4 a[100:103], v2, s[20:23], s80 offen offset:1024 sc1\n"
  "buffer_load_dwordx4 a[104:107], v2, s[20:23], s80 offen offset:2048 sc1\n"
  "buffer_load_dwordx4 a[108:111], v2, s[20:23], s80 offen offset:3072 sc1\n"
  "s_add_u32 s80, s80, 0x1000\n"
  "buffer_load_dwordx4 a[112:115], v2, s[20:23], s80 offen offset:0 sc1\n"
  "buffer_load_dwordx4 a[116:119], v2, s[20:23], s80 offen offset:1024 sc1\n"
  "buffer_load_dwordx4 a[120:123], v2, s[20:23], s80 offen offset:2048 sc1\n"
  "buffer_load_dwordx4 a[124:127], v2, s[20:23], s80 offen offset:3072 sc1\n"
  "s_add_u32 s80, s80, 0x1000\n"
  "LPUB_%=:\n"
  "s_nop 7\n"
  "s_sub_u32 s83, s66, 0\n"
  "s_and_b32 s83, s83, 3\n"
  "s_cbranch_scc0 LOWN0_%=\n"
  "v_cvt_pk_bf16_f32 v160, v32, v33\n"
  "v_cvt_pk_bf16_f32 v161, v34, v35\n"
  "v_cvt_pk_bf16_f32 v162, v48, v49\n"
  "v_cvt_pk_bf16_f32 v163, v50, v51\n"
  "v_cvt_pk_bf16_f32 v164, v64, v65\n"
  "v_cvt_pk_bf16_f32 v165, v66, v67\n"
  "v_cvt_pk_bf16_f32 v166, v80, v81\n"
  "v_cvt_pk_bf16_f32 v167, v82, v83\n"
  "v_cvt_pk_bf16_f32 v168, v96, v97\n"
  "v_cvt_pk_bf16_f32 v169, v98, v99\n"
  "s_mul_i32 s83, s83, 3072\n"
  "s_add_u32 s83, s83, s67\n"
  "s_add_u32 s83, s83, 0xfffff400\n"
  "buffer_store_dwordx4 v[160:163], v2, s[36:39], s83 offen offset:0 sc1\n"
  "buffer_store_dwordx4 v[164:167], v2, s[36:39], s83 offen offset:1024 sc1\n"
  "buffer_store_dwordx2 v[168:169], v2, s[36:39], s83 offen offset:2048 sc1\n"
  "LOWN0_%=_r:\n"
  "s_sub_u32 s83, s66, 1\n"
  "s_and_b32 s83, s83, 3\n"
  "s_cbranch_scc0 LOWN1_%=\n"
  "v_cvt_pk_bf16_f32 v172, v36, v37\n"
  "v_cvt_pk_bf16_f32 v173, v38, v39\n"
  "v_cvt_pk_bf16_f32 v174, v52, v53\n"
  "v_cvt_pk_bf16_f32 v175, v54, v55\n"
  "v_cvt_pk_bf16_f32 v176, v68, v69\n"
  "v_cvt_pk_bf16_f32 v177, v70, v71\n"
  "v_cvt_pk_bf16_f32 v178, v84, v85\n"
  "v_cvt_pk_bf16_f32 v179, v86, v87\n"
  "v_cvt_pk_bf16_f32 v180, v100, v101\n"
  "v_cvt_pk_bf16_f32 v181, v102, v103\n"
  "s_mul_i32 s83, s83, 3072\n"
  "s_add_u32 s83, s83, s67\n"
  "s_add_u32 s83, s83, 0x8400\n"
  "buffer_store_dwordx4 v[172:175], v2, s[36:39], s83 offen offset:0 sc1\n"
  "buffer_store_dwordx4 v[176:179], v2, s[36:39], s83 offen offset:1024 sc1\n"
  "buffer_store_dwordx2 v[180:181], v2, s[36:39], s83 offen offset:2048 sc1\n"
  "LOWN1_%=_r:\n"
  "s_sub_u32 s83, s66, 2\n"
  "s_and_b32 s83, s83, 3\n"
  "s_cbranch_scc0 LOWN2_%=\n"
  "v_cvt_pk_bf16_f32 v184, v40, v41\n"
  "v_cvt_pk_bf16_f32 v185, v42, v43\n"
  "v_cvt_pk_bf16_f32 v186, v56, v57\n"
  "v_cvt_pk_bf16_f32 v187, v58, v59\n"
  "v_cvt_pk_bf16_f32 v188, v72, v73\n"
  "v_cvt_pk_bf16_f32 v189, v74, v75\n"
  "v_cvt_pk_bf16_f32 v190, v88, v89\n"
  "v_cvt_pk_bf16_f32 v191, v90, v91\n"
  "v_cvt_pk_bf16_f32 v192, v104, v105\n"
  "v_cvt_pk_bf16_f32 v193, v106, v107\n"
  "s_mul_i32 s83, s83, 3072\n"
  "s_add_u32 s83, s83, s67\n"
  "s_add_u32 s83, s83, 0x11400\n"
  "buffer_store_dwordx4 v[184:187], v2, s[36:39], s83 offen offset:0 sc1\n"
  "buffer_store_dwordx4 v[188:191], v2, s[36:39], s83 offen offset:1024 sc1\n"
  "buffer_store_dwordx2 v[192:193], v2, s[36:39], s83 offen offset:2048 sc1\n"
  "LOWN2_%=_r:\n"
  "s_sub_u32 s83, s66, 3\n"
  "s_and_b32 s83, s83, 3\n"
  "s_cbranch_scc0 LOWN3_%=\n"
  "v_cvt_pk_bf16_f32 v196, v44, v45\n"
  "v_cvt_pk_bf16_f32 v197, v46, v47\n"
  "v_cvt_pk_bf16_f32 v198, v60, v61\n"
  "v_cvt_pk_bf16_f32 v199, v62, v63\n"
  "v_cvt_pk_bf16_f32 v200, v76, v77\n"
  "v_cvt_pk_bf16_f32 v201, v78, v79\n"
  "v_cvt_pk_bf16_f32 v202, v92, v93\n"
  "v_cvt_pk_bf16_f32 v203, v94, v95\n"
  "v_cvt_pk_bf16_f32 v204, v108, v109\n"
  "v_cvt_pk_bf16_f32 v205, v110, v111\n"
  "s_mul_i32 s83, s83, 3072\n"
  "s_add_u32 s83, s83, s67\n"
  "s_add_u32 s83, s83, 0x1a400\n"
  "buffer_store_dwordx4 v[196:199], v2, s[36:39], s83 offen offset:0 sc1\n"
  "buffer_store_dwordx4 v[200:203], v2, s[36:39], s83 offen offset:1024 sc1\n"
  "buffer_store_dwordx2 v[204:205], v2, s[36:39], s83 offen offset:2048 sc1\n"
  "LOWN3_%=_r:\n"
  "s_cmp_lg_u32 s66, 3\n"
  "s_cbranch_scc1 LP2_%=\n"
  "s_waitcnt vmcnt(17)\n"
  "s_barrier\n"
  "s_branch LPH_%=\n"
  "LBOUT_%=:\n"
  "s_nop 11\n"
  "v_cvt_pk_bf16_f32 v32, v32, v33\n"
  "v_cvt_pk_bf16_f32 v34, v34, v35\n"
  "v_cvt_pk_bf16_f32 v48, v48, v49\n"
  "v_cvt_pk_bf16_f32 v50, v50, v51\n"
  "v_cvt_pk_bf16_f32 v64, v64, v65\n"
  "v_cvt_pk_bf16_f32 v66, v66, v67\n"
  "v_cvt_pk_bf16_f32 v80, v80, v81\n"
  "v_cvt_pk_bf16_f32 v82, v82, v83\n"
  "v_cvt_pk_bf16_f32 v96, v96, v97\n"
  "v_cvt_pk_bf16_f32 v98, v98, v99\n"
  "v_mov_b32_dpp v33, v32 quad_perm:[1,0,3,2] row_mask:0xf bank_mask:0xf\n"
  "v_mov_b32_dpp v35, v34 quad_perm:[1,0,3,2] row_mask:0xf bank_mask:0xf\n"
  "v_mov_b32_dpp v49, v48 quad_perm:[1,0,3,2] row_mask:0xf bank_mask:0xf\n"
  "v_mov_b32_dpp v51, v50 quad_perm:[1,0,3,2] row_mask:0xf bank_mask:0xf\n"
  "v_mov_b32_dpp v65, v64 quad_perm:[1,0,3,2] row_mask:0xf bank_mask:0xf\n"
  "v_mov_b32_dpp v67, v66 quad_perm:[1,0,3,2] row_mask:0xf bank_mask:0xf\n"
  "v_mov_b32_dpp v81, v80 quad_perm:[1,0,3,2] row_mask:0xf bank_mask:0xf\n"
  "v_mov_b32_dpp v83, v82 quad_perm:[1,0,3,2] row_mask:0xf bank_mask:0xf\n"
  "v_mov_b32_dpp v97, v96 quad_perm:[1,0,3,2] row_mask:0xf bank_mask:0xf\n"
  "v_mov_b32_dpp v99, v98 quad_perm:[1,0,3,2] row_mask:0xf bank_mask:0xf\n"
  "v_perm_b32 v32, v33, v32, v29\n"
  "v_perm_b32 v34, v35, v34, v29\n"
  "v_perm_b32 v48, v49, v48, v29\n"
  "v_perm_b32 v50, v51, v50, v29\n"
  "v_perm_b32 v64, v65, v64, v29\n"
  "v_perm_b32 v66, v67, v66, v29\n"
  "v_perm_b32 v80, v81, v80, v29\n"
  "v_perm_b32 v82, v83, v82, v29\n"
  "v_perm_b32 v96, v97, v96, v29\n"
  "v_perm_b32 v98, v99, v98, v29\n"
  "buffer_store_dword v32, v8, s[40:43], s70 offen offset:0 sc1\n"
  "buffer_store_dword v48, v8, s[40:43], s70 offen offset:64 sc1\n"
  "buffer_store_dword v64, v8, s[40:43], s70 offen offset:128 sc1\n"
  "buffer_store_dword v80, v8, s[40:43], s70 offen offset:192 sc1\n"
  "buffer_store_dword v96, v8, s[40:43], s70 offen offset:256 sc1\n"
  "buffer_store_dword v34, v9, s[40:43], s70 offen offset:0 sc1\n"
  "buffer_store_dword v50, v9, s[40:43], s70 offen offset:64 sc1\n"
  "buffer_store_dword v66, v9, s[40:43], s70 offen offset:128 sc1\n"
  "buffer_store_dword v82, v9, s[40:43], s70 offen offset:192 sc1\n"
  "buffer_store_dword v98, v9, s[40:43], s70 offen offset:256 sc1\n"
  "s_add_u32 s70, s70, 0x5000\n"
  "v_cvt_pk_bf16_f32 v36, v36, v37\n"
  "v_cvt_pk_bf16_f32 v38, v38, v39\n"
  "v_cvt_pk_bf16_f32 v52, v52, v53\n"
  "v_cvt_pk_bf16_f32 v54, v54, v55\n"
  "v_cvt_pk_bf16_f32 v68, v68, v69\n"
  "v_cvt_pk_bf16_f32 v70, v70, v71\n"
  "v_cvt_pk_bf16_f32 v84, v84, v85\n"
  "v_cvt_pk_bf16_f32 v86, v86, v87\n"
  "v_cvt_pk_bf16_f32 v100, v100, v101\n"
  "v_cvt_pk_bf16_f32 v102, v102, v103\n"
  "v_mov_b32_dpp v37, v36 quad_perm:[1,0,3,2] row_mask:0xf bank_mask:0xf\n"
  "v_mov_b32_dpp v39, v38 quad_perm:[1,0,3,2] row_mask:0xf bank_mask:0xf\n"
  "v_mov_b32_dpp v53, v52 quad_perm:[1,0,3,2] row_mask:0xf bank_mask:0xf\n"
  "v_mov_b32_dpp v55, v54 quad_perm:[1,0,3,2] row_mask:0xf bank_mask:0xf\n"
  "v_mov_b32_dpp v69, v68 quad_perm:[1,0,3,2] row_mask:0xf bank_mask:0xf\n"
  "v_mov_b32_dpp v71, v70 quad_perm:[1,0,3,2] row_mask:0xf bank_mask:0xf\n"
  "v_mov_b32_dpp v85, v84 quad_perm:[1,0,3,2] row_mask:0xf bank_mask:0xf\n"
  "v_mov_b32_dpp v87, v86 quad_perm:[1,0,3,2] row_mask:0xf bank_mask:0xf\n"
  "v_mov_b32_dpp v101, v100 quad_perm:[1,0,3,2] row_mask:0xf bank_mask:0xf\n"
  "v_mov_b32_dpp v103, v102 quad_perm:[1,0,3,2] row_mask:0xf bank_mask:0xf\n"
  "v_perm_b32 v36, v37, v36, v29\n"
  "v_perm_b32 v38, v39, v38, v29\n"
  "v_perm_b32 v52, v53, v52, v29\n"
  "v_perm_b32 v54, v55, v54, v29\n"
  "v_perm_b32 v68, v69, v68, v29\n"
  "v_perm_b32 v70, v71, v70, v29\n"
  "v_perm_b32 v84, v85, v84, v29\n"
  "v_perm_b32 v86, v87, v86, v29\n"
  "v_perm_b32 v100, v101, v100, v29\n"
  "v_perm_b32 v102, v103, v102, v29\n"
  "buffer_store_dword v36, v8, s[40:43], s70 offen offset:0 sc1\n"
  "buffer_store_dword v52, v8, s[40:43], s70 offen offset:64 sc1\n"
  "buffer_store_dword v68, v8, s[40:43], s70 offen offset:128 sc1\n"
  "buffer_store_dword v84, v8, s[40:43], s70 offen offset:192 sc1\n"
  "buffer_store_dword v100, v8, s[40:43], s70 offen offset:256 sc1\n"
  "buffer_store_dword v38, v9, s[40:43], s70 offen offset:0 sc1\n"
  "buffer_store_dword v54, v9, s[40:43], s70 offen offset:64 sc1\n"
  "buffer_store_dword v70, v9, s[40:43], s70 offen offset:128 sc1\n"
  "buffer_store_dword v86, v9, s[40:43], s70 offen offset:192 sc1\n"
  "buffer_store_dword v102, v9, s[40:43], s70 offen offset:256 sc1\n"
  "s_add_u32 s70, s70, 0x5000\n"
  "v_cvt_pk_bf16_f32 v40, v40, v41\n"
  "v_cvt_pk_bf16_f32 v42, v42, v43\n"
  "v_cvt_pk_bf16_f32 v56, v56, v57\n"
  "v_cvt_pk_bf16_f32 v58, v58, v59\n"
  "v_cvt_pk_bf16_f32 v72, v72, v73\n"
  "v_cvt_pk_bf16_f32 v74, v74, v75\n"
  "v_cvt_pk_bf16_f32 v88, v88, v89\n"
  "v_cvt_pk_bf16_f32 v90, v90, v91\n"
  "v_cvt_pk_bf16_f32 v104, v104, v105\n"
  "v_cvt_pk_bf16_f32 v106, v106, v107\n"
  "v_mov_b32_dpp v41, v40 quad_perm:[1,0,3,2] row_mask:0xf bank_mask:0xf\n"
  "v_mov_b32_dpp v43, v42 quad_perm:[1,0,3,2] row_mask:0xf bank_mask:0xf\n"
  "v_mov_b32_dpp v57, v56 quad_perm:[1,0,3,2] row_mask:0xf bank_mask:0xf\n"
  "v_mov_b32_dpp v59, v58 quad_perm:[1,0,3,2] row_mask:0xf bank_mask:0xf\n"
  "v_mov_b32_dpp v73, v72 quad_perm:[1,0,3,2] row_mask:0xf bank_mask:0xf\n"
  "v_mov_b32_dpp v75, v74 quad_perm:[1,0,3,2] row_mask:0xf bank_mask:0xf\n"
  "v_mov_b32_dpp v89, v88 quad_perm:[1,0,3,2] row_mask:0xf bank_mask:0xf\n"
  "v_mov_b32_dpp v91, v90 quad_perm:[1,0,3,2] row_mask:0xf bank_mask:0xf\n"
  "v_mov_b32_dpp v105, v104 quad_perm:[1,0,3,2] row_mask:0xf bank_mask:0xf\n"
  "v_mov_b32_dpp v107, v106 quad_perm:[1,0,3,2] row_mask:0xf bank_mask:0xf\n"
  "v_perm_b32 v40, v41, v40, v29\n"
  "v_perm_b32 v42, v43, v42, v29\n"
  "v_perm_b32 v56, v57, v56, v29\n"
  "v_perm_b32 v58, v59, v58, v29\n"
  "v_perm_b32 v72, v73, v72, v29\n"
  "v_perm_b32 v74, v75, v74, v29\n"
  "v_perm_b32 v88, v89, v88, v29\n"
  "v_perm_b32 v90, v91, v90, v29\n"
  "v_perm_b32 v104, v105, v104, v29\n"
  "v_perm_b32 v106, v107, v106, v29\n"
  "buffer_store_dword v40, v8, s[40:43], s70 offen offset:0 sc1\n"
  "buffer_store_dword v56, v8, s[40:43], s70 offen offset:64 sc1\n"
  "buffer_store_dword v72, v8, s[40:43], s70 offen offset:128 sc1\n"
  "buffer_store_dword v88, v8, s[40:43], s70 offen offset:192 sc1\n"
  "buffer_store_dword v104, v8, s[40:43], s70 offen offset:256 sc1\n"
  "buffer_store_dword v42, v9, s[40:43], s70 offen offset:0 sc1\n"
  "buffer_store_dword v58, v9, s[40:43], s70 offen offset:64 sc1\n"
  "buffer_store_dword v74, v9, s[40:43], s70 offen offset:128 sc1\n"
  "buffer_store_dword v90, v9, s[40:43], s70 offen offset:192 sc1\n"
  "buffer_store_dword v106, v9, s[40:43], s70 offen offset:256 sc1\n"
  "s_add_u32 s70, s70, 0x5000\n"
  "v_cvt_pk_bf16_f32 v44, v44, v45\n"
  "v_cvt_pk_bf16_f32 v46, v46, v47\n"
  "v_cvt_pk_bf16_f32 v60, v60, v61\n"
  "v_cvt_pk_bf16_f32 v62, v62, v63\n"
  "v_cvt_pk_bf16_f32 v76, v76, v77\n"
  "v_cvt_pk_bf16_f32 v78, v78, v79\n"
  "v_cvt_pk_bf16_f32 v92, v92, v93\n"
  "v_cvt_pk_bf16_f32 v94, v94, v95\n"
  "v_cvt_pk_bf16_f32 v108, v108, v109\n"
  "v_cvt_pk_bf16_f32 v110, v110, v111\n"
  "v_mov_b32_dpp v45, v44 quad_perm:[1,0,3,2] row_mask:0xf bank_mask:0xf\n"
  "v_mov_b32_dpp v47, v46 quad_perm:[1,0,3,2] row_mask:0xf bank_mask:0xf\n"
  "v_mov_b32_dpp v61, v60 quad_perm:[1,0,3,2] row_mask:0xf bank_mask:0xf\n"
  "v_mov_b32_dpp v63, v62 quad_perm:[1,0,3,2] row_mask:0xf bank_mask:0xf\n"
  "v_mov_b32_dpp v77, v76 quad_perm:[1,0,3,2] row_mask:0xf bank_mask:0xf\n"
  "v_mov_b32_dpp v79, v78 quad_perm:[1,0,3,2] row_mask:0xf bank_mask:0xf\n"
  "v_mov_b32_dpp v93, v92 quad_perm:[1,0,3,2] row_mask:0xf bank_mask:0xf\n"
  "v_mov_b32_dpp v95, v94 quad_perm:[1,0,3,2] row_mask:0xf bank_mask:0xf\n"
  "v_mov_b32_dpp v109, v108 quad_perm:[1,0,3,2] row_mask:0xf bank_mask:0xf\n"
  "v_mov_b32_dpp v111, v110 quad_perm:[1,0,3,2] row_mask:0xf bank_mask:0xf\n"
  "v_perm_b32 v44, v45, v44, v29\n"
  "v_perm_b32 v46, v47, v46, v29\n"
  "v_perm_b32 v60, v61, v60, v29\n"
  "v_perm_b32 v62, v63, v62, v29\n"
  "v_perm_b32 v76, v77, v76, v29\n"
  "v_perm_b32 v78, v79, v78, v29\n"
  "v_perm_b32 v92, v93, v92, v29\n"
  "v_perm_b32 v94, v95, v94, v29\n"
  "v_perm_b32 v108, v109, v108, v29\n"
  "v_perm_b32 v110, v111, v110, v29\n"
  "buffer_store_dword v44, v8, s[40:43], s70 offen offset:0 sc1\n"
  "buffer_store_dword v60, v8, s[40:43], s70 offen offset:64 sc1\n"
  "buffer_store_dword v76, v8, s[40:43], s70 offen offset:128 sc1\n"
  "buffer_store_dword v92, v8, s[40:43], s70 offen offset:192 sc1\n"
  "buffer_store_dword v108, v8, s[40:43], s70 offen offset:256 sc1\n"
  "buffer_store_dword v46, v9, s[40:43], s70 offen offset:0 sc1\n"
  "buffer_store_dword v62, v9, s[40:43], s70 offen offset:64 sc1\n"
  "buffer_store_dword v78, v9, s[40:43], s70 offen offset:128 sc1\n"
  "buffer_store_dword v94, v9, s[40:43], s70 offen offset:192 sc1\n"
  "buffer_store_dword v110, v9, s[40:43], s70 offen offset:256 sc1\n"
  "s_add_u32 s70, s70, 0x5000\n"
  "s_mov_b32 s84, 0x20000\n"
  "LRP_%=:\n"
  "buffer_load_dwordx4 v[208:211], v2, s[36:39], s68 offen offset:0 sc1\n"
  "buffer_load_dwordx4 v[212:215], v2, s[36:39], s68 offen offset:1024 sc1\n"
  "buffer_load_dwordx2 v[216:217], v2, s[36:39], s68 offen offset:2048 sc1\n"
  "buffer_load_dwordx4 v[218:221], v2, s[36:39], s85 offen offset:0 sc1\n"
  "buffer_load_dwordx4 v[222:225], v2, s[36:39], s85 offen offset:1024 sc1\n"
  "buffer_load_dwordx2 v[226:227], v2, s[36:39], s85 offen offset:2048 sc1\n"
  "buffer_load_dwordx4 v[228:231], v2, s[36:39], s86 offen offset:0 sc1\n"
  "buffer_load_dwordx4 v[232:235], v2, s[36:39], s86 offen offset:1024 sc1\n"
  "buffer_load_dwordx2 v[236:237], v2, s[36:39], s86 offen offset:2048 sc1\n"
  "s_waitcnt vmcnt(0)\n"
  "LCK_%=:\n"
  "v_max3_u32 v10, v208, v208, v209\n"
  "v_max3_u32 v11, v218, v218, v219\n"
  "v_max3_u32 v12, v228, v228, v229\n"
  "v_max3_u32 v10, v10, v210, v211\n"
  "v_max3_u32 v11, v11, v220, v221\n"
  "v_max3_u32 v12, v12, v230, v231\n"
  "v_max3_u32 v10, v10, v212, v213\n"
  "v_max3_u32 v11, v11, v222, v223\n"
  "v_max3_u32 v12, v12, v232, v233\n"
  "v_max3_u32 v10, v10, v214, v215\n"
  "v_max3_u32 v11, v11, v224, v225\n"
  "v_max3_u32 v12, v12, v234, v235\n"
  "v_max3_u32 v10, v10, v216, v217\n"
  "v_max3_u32 v11, v11, v226, v227\n"
  "v_max3_u32 v12, v12, v236, v237\n"
  "v_max3_u32 v12, v10, v11, v12\n"
  "v_cmp_eq_u32 vcc, -1, v12\n"
  "s_nop 0\n"
  "s_cbranch_vccnz LRW_%=\n"
  "LRD_%=:\n"
  "v_mov_b32 v14, s60\n"
  "v_mov_b32 v15, s61\n"
  "s_mov_b64 exec, 1\n"
  "buffer_store_dwordx2 v[14:15], off, s[44:47], s88 sc1\n"
  "s_mov_b64 exec, -1\n"
  "v_lshlrev_b32 v32, 16, v208\n"
  "v_and_b32 v33, 0xffff0000, v208\n"
  "v_lshlrev_b32 v34, 16, v209\n"
  "v_and_b32 v35, 0xffff0000, v209\n"
  "v_lshlrev_b32 v36, 16, v210\n"
  "v_and_b32 v37, 0xffff0000, v210\n"
  "v_lshlrev_b32 v38, 16, v211\n"
  "v_and_b32 v39, 0xffff0000, v211\n"
  "v_lshlrev_b32 v40, 16, v212\n"
  "v_and_b32 v41, 0xffff0000, v212\n"
  "v_lshlrev_b32 v42, 16, v213\n"
  "v_and_b32 v43, 0xffff0000, v213\n"
  "v_lshlrev_b32 v44, 16, v214\n"
  "v_and_b32 v45, 0xffff0000, v214\n"
  "v_lshlrev_b32 v46, 16, v215\n"
  "v_and_b32 v47, 0xffff0000, v215\n"
  "v_lshlrev_b32 v48, 16, v216\n"
  "v_and_b32 v49, 0xffff0000, v216\n"
  "v_lshlrev_b32 v50, 16, v217\n"
  "v_and_b32 v51, 0xffff0000, v217\n"
  "v_add_f32 v128, v128, v32\n"
  "v_add_f32 v129, v129, v33\n"
  "v_add_f32 v130, v130, v34\n"
  "v_add_f32 v131, v131, v35\n"
  "v_add_f32 v132, v132, v36\n"
  "v_add_f32 v133, v133, v37\n"
  "v_add_f32 v134, v134, v38\n"
  "v_add_f32 v135, v135, v39\n"
  "v_add_f32 v136, v136, v40\n"
  "v_add_f32 v137, v137, v41\n"
  "v_add_f32 v138, v138, v42\n"
  "v_add_f32 v139, v139, v43\n"
  "v_add_f32 v140, v140, v44\n"
  "v_add_f32 v141, v141, v45\n"
  "v_add_f32 v142, v142, v46\n"
  "v_add_f32 v143, v143, v47\n"
  "v_add_f32 v144, v144, v48\n"
  "v_add_f32 v145, v145, v49\n"
  "v_add_f32 v146, v146, v50\n"
  "v_add_f32 v147, v147, v51\n"
  "v_lshlrev_b32 v32, 16, v218\n"
  "v_and_b32 v33, 0xffff0000, v218\n"
  "v_lshlrev_b32 v34, 16, v219\n"
  "v_and_b32 v35, 0xffff0000, v219\n"
  "v_lshlrev_b32 v36, 16, v220\n"
  "v_and_b32 v37, 0xffff0000, v220\n"
  "v_lshlrev_b32 v38, 16, v221\n"
  "v_and_b32 v39, 0xffff0000, v221\n"
  "v_lshlrev_b32 v40, 16, v222\n"
  "v_and_b32 v41, 0xffff0000, v222\n"
  "v_lshlrev_b32 v42, 16, v223\n"
  "v_and_b32 v43, 0xffff0000, v223\n"
  "v_lshlrev_b32 v44, 16, v224\n"
  "v_and_b32 v45, 0xffff0000, v224\n"
  "v_lshlrev_b32 v46, 16, v225\n"
  "v_and_b32 v47, 0xffff0000, v225\n"
  "v_lshlrev_b32 v48, 16, v226\n"
  "v_and_b32 v49, 0xffff0000, v226\n"
  "v_lshlrev_b32 v50, 16, v227\n"
  "v_and_b32 v51, 0xffff0000, v227\n"
  "v_add_f32 v128, v128, v32\n"
  "v_add_f32 v129, v129, v33\n"
  "v_add_f32 v130, v130, v34\n"
  "v_add_f32 v131, v131, v35\n"
  "v_add_f32 v132, v132, v36\n"
  "v_add_f32 v133, v133, v37\n"
  "v_add_f32 v134, v134, v38\n"
  "v_add_f32 v135, v135, v39\n"
  "v_add_f32 v136, v136, v40\n"
  "v_add_f32 v137, v137, v41\n"
  "v_add_f32 v138, v138, v42\n"
  "v_add_f32 v139, v139, v43\n"
  "v_add_f32 v140, v140, v44\n"
  "v_add_f32 v141, v141, v45\n"
  "v_add_f32 v142, v142, v46\n"
  "v_add_f32 v143, v143, v47\n"
  "v_add_f32 v144, v144, v48\n"
  "v_add_f32 v145, v145, v49\n"
  "v_add_f32 v146, v146, v50\n"
  "v_add_f32 v147, v147, v51\n"
  "v_lshlrev_b32 v32, 16, v228\n"
  "v_and_b32 v33, 0xffff0000, v228\n"
  "v_lshlrev_b32 v34, 16, v229\n"
  "v_and_b32 v35, 0xffff0000, v229\n"
  "v_lshlrev_b32 v36, 16, v230\n"
  "v_and_b32 v37, 0xffff0000, v230\n"
  "v_lshlrev_b32 v38, 16, v231\n"
  "v_and_b32 v39, 0xffff0000, v231\n"
  "v_lshlrev_b32 v40, 16, v232\n"
  "v_and_b32 v41, 0xffff0000, v232\n"
  "v_lshlrev_b32 v42, 16, v233\n"
  "v_and_b32 v43, 0xffff0000, v233\n"
  "v_lshlrev_b32 v44, 16, v234\n"
  "v_and_b32 v45, 0xffff0000, v234\n"
  "v_lshlrev_b32 v46, 16, v235\n"
  "v_and_b32 v47, 0xffff0000, v235\n"
  "v_lshlrev_b32 v48, 16, v236\n"
  "v_and_b32 v49, 0xffff0000, v236\n"
  "v_lshlrev_b32 v50, 16, v237\n"
  "v_and_b32 v51, 0xffff0000, v237\n"
  "v_add_f32 v128, v128, v32\n"
  "v_add_f32 v129, v129, v33\n"
  "v_add_f32 v130, v130, v34\n"
  "v_add_f32 v131, v131, v35\n"
  "v_add_f32 v132, v132, v36\n"
  "v_add_f32 v133, v133, v37\n"
  "v_add_f32 v134, v134, v38\n"
  "v_add_f32 v135, v135, v39\n"
  "v_add_f32 v136, v136, v40\n"
  "v_add_f32 v137, v137, v41\n"
  "v_add_f32 v138, v138, v42\n"
  "v_add_f32 v139, v139, v43\n"
  "v_add_f32 v140, v140, v44\n"
  "v_add_f32 v141, v141, v45\n"
  "v_add_f32 v142, v142, v46\n"
  "v_add_f32 v143, v143, v47\n"
  "v_add_f32 v144, v144, v48\n"
  "v_add_f32 v145, v145, v49\n"
  "v_add_f32 v146, v146, v50\n"
  "v_add_f32 v147, v147, v51\n"
  "v_cvt_pk_bf16_f32 v128, v128, v129\n"
  "v_cvt_pk_bf16_f32 v130, v130, v131\n"
  "v_cvt_pk_bf16_f32 v132, v132, v133\n"
  "v_cvt_pk_bf16_f32 v134, v134, v135\n"
  "v_cvt_pk_bf16_f32 v136, v136, v137\n"
  "v_cvt_pk_bf16_f32 v138, v138, v139\n"
  "v_cvt_pk_bf16_f32 v140, v140, v141\n"
  "v_cvt_pk_bf16_f32 v142, v142, v143\n"
  "v_cvt_pk_bf16_f32 v144, v144, v145\n"
  "v_cvt_pk_bf16_f32 v146, v146, v147\n"
  "v_mov_b32_dpp v129, v128 quad_perm:[1,0,3,2] row_mask:0xf bank_mask:0xf\n"
  "v_mov_b32_dpp v131, v130 quad_perm:[1,0,3,2] row_mask:0xf bank_mask:0xf\n"
  "v_mov_b32_dpp v133, v132 quad_perm:[1,0,3,2] row_mask:0xf bank_mask:0xf\n"
  "v_mov_b32_dpp v135, v134 quad_perm:[1,0,3,2] row_mask:0xf bank_mask:0xf\n"
  "v_mov_b32_dpp v137, v136 quad_perm:[1,0,3,2] row_mask:0xf bank_mask:0xf\n"
  "v_mov_b32_dpp v139, v138 quad_perm:[1,0,3,2] row_mask:0xf bank_mask:0xf\n"
  "v_mov_b32_dpp v141, v140 quad_perm:[1,0,3,2] row_mask:0xf bank_mask:0xf\n"
  "v_mov_b32_dpp v143, v142 quad_perm:[1,0,3,2] row_mask:0xf bank_mask:0xf\n"
  "v_mov_b32_dpp v145, v144 quad_perm:[1,0,3,2] row_mask:0xf bank_mask:0xf\n"
  "v_mov_b32_dpp v147, v146 quad_perm:[1,0,3,2] row_mask:0xf bank_mask:0xf\n"
  "v_perm_b32 v128, v129, v128, v29\n"
  "v_perm_b32 v130, v131, v130, v29\n"
  "v_perm_b32 v132, v133, v132, v29\n"
  "v_perm_b32 v134, v135, v134, v29\n"
  "v_perm_b32 v136, v137, v136, v29\n"
  "v_perm_b32 v138, v139, v138, v29\n"
  "v_perm_b32 v140, v141, v140, v29\n"
  "v_perm_b32 v142, v143, v142, v29\n"
  "v_perm_b32 v144, v145, v144, v29\n"
  "v_perm_b32 v146, v147, v146, v29\n"
  "buffer_store_dword v128, v8, s[40:43], s69 offen offset:0 sc1\n"
  "buffer_store_dword v132, v8, s[40:43], s69 offen offset:64 sc1\n"
  "buffer_store_dword v136, v8, s[40:43], s69 offen offset:128 sc1\n"
  "buffer_store_dword v140, v8, s[40:43], s69 offen offset:192 sc1\n"
  "buffer_store_dword v144, v8, s[40:43], s69 offen offset:256 sc1\n"
  "buffer_store_dword v130, v9, s[40:43], s69 offen offset:0 sc1\n"
  "buffer_store_dword v134, v9, s[40:43], s69 offen offset:64 sc1\n"
  "buffer_store_dword v138, v9, s[40:43], s69 offen offset:128 sc1\n"
  "buffer_store_dword v142, v9, s[40:43], s69 offen offset:192 sc1\n"
  "buffer_store_dword v146, v9, s[40:43], s69 offen offset:256 sc1\n"
  "s_branch LEND_%=\n"
  "LW0_%=:\n"
  "v_cvt_pk_bf16_f32 v24, v128, v129\n"
  "v_cvt_pk_bf16_f32 v25, v130, v131\n"
  "v_cvt_pk_bf16_f32 v26, v132, v133\n"
  "v_cvt_pk_bf16_f32 v27, v134, v135\n"
  "v_lshlrev_b32 v128, 16, v24\n"
  "v_and_b32 v129, 0xffff0000, v24\n"
  "v_lshlrev_b32 v130, 16, v25\n"
  "v_and_b32 v131, 0xffff0000, v25\n"
  "v_lshlrev_b32 v132, 16, v26\n"
  "v_and_b32 v133, 0xffff0000, v26\n"
  "v_lshlrev_b32 v134, 16, v27\n"
  "v_and_b32 v135, 0xffff0000, v27\n"
  "ds_write_b128 v18, v[128:131] offset:0\n"
  "ds_write_b128 v19, v[132:135] offset:0\n"
  "v_cvt_pk_bf16_f32 v24, v144, v145\n"
  "v_cvt_pk_bf16_f32 v25, v146, v147\n"
  "v_cvt_pk_bf16_f32 v26, v148, v149\n"
  "v_cvt_pk_bf16_f32 v27, v150, v151\n"
  "v_lshlrev_b32 v144, 16, v24\n"
  "v_and_b32 v145, 0xffff0000, v24\n"
  "v_lshlrev_b32 v146, 16, v25\n"
  "v_and_b32 v147, 0xffff0000, v25\n"
  "v_lshlrev_b32 v148, 16, v26\n"
  "v_and_b32 v149, 0xffff0000, v26\n"
  "v_lshlrev_b32 v150, 16, v27\n"
  "v_and_b32 v151, 0xffff0000, v27\n"
  "ds_write_b128 v18, v[144:147] offset:11776\n"
  "ds_write_b128 v19, v[148:151] offset:11776\n"
  "v_cvt_pk_bf16_f32 v24, v160, v161\n"
  "v_cvt_pk_bf16_f32 v25, v162, v163\n"
  "v_cvt_pk_bf16_f32 v26, v164, v165\n"
  "v_cvt_pk_bf16_f32 v27, v166, v167\n"
  "v_lshlrev_b32 v160, 16, v24\n"
  "v_and_b32 v161, 0xffff0000, v24\n"
  "v_lshlrev_b32 v162, 16, v25\n"
  "v_and_b32 v163, 0xffff0000, v25\n"
  "v_lshlrev_b32 v164, 16, v26\n"
  "v_and_b32 v165, 0xffff0000, v26\n"
  "v_lshlrev_b32 v166, 16, v27\n"
  "v_and_b32 v167, 0xffff0000, v27\n"
  "ds_write_b128 v18, v[160:163] offset:23552\n"
  "ds_write_b128 v19, v[164:167] offset:23552\n"
  "v_cvt_pk_bf16_f32 v24, v176, v177\n"
  "v_cvt_pk_bf16_f32 v25, v178, v179\n"
  "v_cvt_pk_bf16_f32 v26, v180, v181\n"
  "v_cvt_pk_bf16_f32 v27, v182, v183\n"
  "v_lshlrev_b32 v176, 16, v24\n"
  "v_and_b32 v177, 0xffff0000, v24\n"
  "v_lshlrev_b32 v178, 16, v25\n"
  "v_and_b32 v179, 0xffff0000, v25\n"
  "v_lshlrev_b32 v180, 16, v26\n"
  "v_and_b32 v181, 0xffff0000, v26\n"
  "v_lshlrev_b32 v182, 16, v27\n"
  "v_and_b32 v183, 0xffff0000, v27\n"
  "ds_write_b128 v18, v[176:179] offset:35328\n"
  "ds_write_b128 v19, v[180:183] offset:35328\n"
  "v_cvt_pk_bf16_f32 v24, v192, v193\n"
  "v_cvt_pk_bf16_f32 v25, v194, v195\n"
  "v_cvt_pk_bf16_f32 v26, v196, v197\n"
  "v_cvt_pk_bf16_f32 v27, v198, v199\n"
  "v_lshlrev_b32 v192, 16, v24\n"
  "v_and_b32 v193, 0xffff0000, v24\n"
  "v_lshlrev_b32 v194, 16, v25\n"
  "v_and_b32 v195, 0xffff0000, v25\n"
  "v_lshlrev_b32 v196, 16, v26\n"
  "v_and_b32 v197, 0xffff0000, v26\n"
  "v_lshlrev_b32 v198, 16, v27\n"
  "v_and_b32 v199, 0xffff0000, v27\n"
  "ds_write_b128 v18, v[192:195] offset:47104\n"
  "ds_write_b128 v19, v[196:199] offset:47104\n"
  "s_branch LW0_%=_r\n"
  "LW2_%=:\n"
  "v_cvt_pk_bf16_f32 v24, v136, v137\n"
  "v_cvt_pk_bf16_f32 v25, v138, v139\n"
  "v_cvt_pk_bf16_f32 v26, v140, v141\n"
  "v_cvt_pk_bf16_f32 v27, v142, v143\n"
  "v_lshlrev_b32 v136, 16, v24\n"
  "v_and_b32 v137, 0xffff0000, v24\n"
  "v_lshlrev_b32 v138, 16, v25\n"
  "v_and_b32 v139, 0xffff0000, v25\n"
  "v_lshlrev_b32 v140, 16, v26\n"
  "v_and_b32 v141, 0xffff0000, v26\n"
  "v_lshlrev_b32 v142, 16, v27\n"
  "v_and_b32 v143, 0xffff0000, v27\n"
  "ds_write_b128 v20, v[136:139] offset:0\n"
  "ds_write_b128 v21, v[140:143] offset:0\n"
  "v_cvt_pk_bf16_f32 v24, v152, v153\n"
  "v_cvt_pk_bf16_f32 v25, v154, v155\n"
  "v_cvt_pk_bf16_f32 v26, v156, v157\n"
  "v_cvt_pk_bf16_f32 v27, v158, v159\n"
  "v_lshlrev_b32 v152, 16, v24\n"
  "v_and_b32 v153, 0xffff0000, v24\n"
  "v_lshlrev_b32 v154, 16, v25\n"
  "v_and_b32 v155, 0xffff0000, v25\n"
  "v_lshlrev_b32 v156, 16, v26\n"
  "v_and_b32 v157, 0xffff0000, v26\n"
  "v_lshlrev_b32 v158, 16, v27\n"
  "v_and_b32 v159, 0xffff0000, v27\n"
  "ds_write_b128 v20, v[152:155] offset:11776\n"
  "ds_write_b128 v21, v[156:159] offset:11776\n"
  "v_cvt_pk_bf16_f32 v24, v168, v169\n"
  "v_cvt_pk_bf16_f32 v25, v170, v171\n"
  "v_cvt_pk_bf16_f32 v26, v172, v173\n"
  "v_cvt_pk_bf16_f32 v27, v174, v175\n"
  "v_lshlrev_b32 v168, 16, v24\n"
  "v_and_b32 v169, 0xffff0000, v24\n"
  "v_lshlrev_b32 v170, 16, v25\n"
  "v_and_b32 v171, 0xffff0000, v25\n"
  "v_lshlrev_b32 v172, 16, v26\n"
  "v_and_b32 v173, 0xffff0000, v26\n"
  "v_lshlrev_b32 v174, 16, v27\n"
  "v_and_b32 v175, 0xffff0000, v27\n"
  "ds_write_b128 v20, v[168:171] offset:23552\n"
  "ds_write_b128 v21, v[172:175] offset:23552\n"
  "v_cvt_pk_bf16_f32 v24, v184, v185\n"
  "v_cvt_pk_bf16_f32 v25, v186, v187\n"
  "v_cvt_pk_bf16_f32 v26, v188, v189\n"
  "v_cvt_pk_bf16_f32 v27, v190, v191\n"
  "v_lshlrev_b32 v184, 16, v24\n"
  "v_and_b32 v185, 0xffff0000, v24\n"
  "v_lshlrev_b32 v186, 16, v25\n"
  "v_and_b32 v187, 0xffff0000, v25\n"
  "v_lshlrev_b32 v188, 16, v26\n"
  "v_and_b32 v189, 0xffff0000, v26\n"
  "v_lshlrev_b32 v190, 16, v27\n"
  "v_and_b32 v191, 0xffff0000, v27\n"
  "ds_write_b128 v20, v[184:187] offset:35328\n"
  "ds_write_b128 v21, v[188:191] offset:35328\n"
  "v_cvt_pk_bf16_f32 v24, v200, v201\n"
  "v_cvt_pk_bf16_f32 v25, v202, v203\n"
  "v_cvt_pk_bf16_f32 v26, v204, v205\n"
  "v_cvt_pk_bf16_f32 v27, v206, v207\n"
  "v_lshlrev_b32 v200, 16, v24\n"
  "v_and_b32 v201, 0xffff0000, v24\n"
  "v_lshlrev_b32 v202, 16, v25\n"
  "v_and_b32 v203, 0xffff0000, v25\n"
  "v_lshlrev_b32 v204, 16, v26\n"
  "v_and_b32 v205, 0xffff0000, v26\n"
  "v_lshlrev_b32 v206, 16, v27\n"
  "v_and_b32 v207, 0xffff0000, v27\n"
  "ds_write_b128 v20, v[200:203] offset:47104\n"
  "ds_write_b128 v21, v[204:207] offset:47104\n"
  "s_branch LW2_%=_r\n"
  "PC_%=:\n"
  "s_mov_b32 s84, 0x20000\n"
  "PC_%=_l:\n"
  "s_sleep 2\n"
  "buffer_load_dwordx2 v[14:15], v12, s[44:47], s76 offen sc1 \n"
  "s_waitcnt vmcnt(0)\n"
  "v_cmp_ne_u64 vcc, s[60:61], v[14:15]\n"
  "s_sub_u32 s84, s84, 1\n"
  "s_cbranch_scc1 PC_%=_r\n"
  "s_cbranch_vccnz PC_%=_l\n"
  "s_branch PC_%=_r\n"
  "LT0_%=:\n"
  "s_waitcnt lgkmcnt(0)\n"
  "v_mfma_f32_32x32x16_bf16 v[32:47], a[92:95], a[20:23], v[32:47]\n"
  "v_mfma_f32_32x32x16_bf16 v[48:63], a[92:95], a[24:27], v[48:63]\n"
  "v_mfma_f32_32x32x16_bf16 v[64:79], a[92:95], a[28:31], v[64:79]\n"
  "v_mfma_f32_32x32x16_bf16 v[80:95], a[92:95], a[32:35], v[80:95]\n"
  "v_mfma_f32_32x32x16_bf16 v[96:111], a[92:95], a[36:39], v[96:111]\n"
  "s_branch LTE_%=\n"
  "LT1_%=:\n"
  "s_waitcnt lgkmcnt(0)\n"
  "v_mfma_f32_32x32x16_bf16 v[32:47], a[108:111], a[20:23], v[32:47]\n"
  "v_mfma_f32_32x32x16_bf16 v[48:63], a[108:111], a[24:27], v[48:63]\n"
  "v_mfma_f32_32x32x16_bf16 v[64:79], a[108:111], a[28:31], v[64:79]\n"
  "v_mfma_f32_32x32x16_bf16 v[80:95], a[108:111], a[32:35], v[80:95]\n"
  "v_mfma_f32_32x32x16_bf16 v[96:111], a[108:111], a[36:39], v[96:111]\n"
  "s_branch LTE_%=\n"
  "LT2_%=:\n"
  "s_waitcnt lgkmcnt(0)\n"
  "v_mfma_f32_32x32x16_bf16 v[32:47], a[124:127], a[20:23], v[32:47]\n"
  "v_mfma_f32_32x32x16_bf16 v[48:63], a[124:127], a[24:27], v[48:63]\n"
  "v_mfma_f32_32x32x16_bf16 v[64:79], a[124:127], a[28:31], v[64:79]\n"
  "v_mfma_f32_32x32x16_bf16 v[80:95], a[124:127], a[32:35], v[80:95]\n"
  "v_mfma_f32_32x32x16_bf16 v[96:111], a[124:127], a[36:39], v[96:111]\n"
  "s_branch LTE_%=\n"
  "LPL_%=:\n"
  "s_branch LPUB_%=\n"
  "LP2_%=:\n"
  "s_mov_b32 s84, 0x20000\n"
  "s_branch LRP_%=\n"
  "PO_%=:\n"
  "s_mov_b32 s84, 0x20000\n"
  "PO_%=_l:\n"
  "s_sleep 2\n"
  "buffer_load_dwordx2 v[14:15], v12, s[44:47], s77 offen sc1 \n"
  "s_waitcnt vmcnt(0)\n"
  "v_cmp_ne_u64 vcc, s[60:61], v[14:15]\n"
  "s_sub_u32 s84, s84, 1\n"
  "s_cbranch_scc1 PO_%=_r\n"
  "s_cbranch_vccnz PO_%=_l\n"
  "s_branch PO_%=_r\n"
  "LOWN0_%=:\n"
  "v_mov_b32 v128, v32\n"
  "v_mov_b32 v129, v33\n"
  "v_mov_b32 v130, v34\n"
  "v_mov_b32 v131, v35\n"
  "v_mov_b32 v132, v48\n"
  "v_mov_b32 v133, v49\n"
  "v_mov_b32 v134, v50\n"
  "v_mov_b32 v135, v51\n"
  "v_mov_b32 v136, v64\n"
  "v_mov_b32 v137, v65\n"
  "v_mov_b32 v138, v66\n"
  "v_mov_b32 v139, v67\n"
  "v_mov_b32 v140, v80\n"
  "v_mov_b32 v141, v81\n"
  "v_mov_b32 v142, v82\n"
  "v_mov_b32 v143, v83\n"
  "v_mov_b32 v144, v96\n"
  "v_mov_b32 v145, v97\n"
  "v_mov_b32 v146, v98\n"
  "v_mov_b32 v147, v99\n"
  "s_branch LOWN0_%=_r\n"
  "LOWN1_%=:\n"
  "v_mov_b32 v128, v36\n"
  "v_mov_b32 v129, v37\n"
  "v_mov_b32 v130, v38\n"
  "v_mov_b32 v131, v39\n"
  "v_mov_b32 v132, v52\n"
  "v_mov_b32 v133, v53\n"
  "v_mov_b32 v134, v54\n"
  "v_mov_b32 v135, v55\n"
  "v_mov_b32 v136, v68\n"
  "v_mov_b32 v137, v69\n"
  "v_mov_b32 v138, v70\n"
  "v_mov_b32 v139, v71\n"
  "v_mov_b32 v140, v84\n"
  "v_mov_b32 v141, v85\n"
  "v_mov_b32 v142, v86\n"
  "v_mov_b32 v143, v87\n"
  "v_mov_b32 v144, v100\n"
  "v_mov_b32 v145, v101\n"
  "v_mov_b32 v146, v102\n"
  "v_mov_b32 v147, v103\n"
  "s_branch LOWN1_%=_r\n"
  "LOWN2_%=:\n"
  "v_mov_b32 v128, v40\n"
  "v_mov_b32 v129, v41\n"
  "v_mov_b32 v130, v42\n"
  "v_mov_b32 v131, v43\n"
  "v_mov_b32 v132, v56\n"
  "v_mov_b32 v133, v57\n"
  "v_mov_b32 v134, v58\n"
  "v_mov_b32 v135, v59\n"
  "v_mov_b32 v136, v72\n"
  "v_mov_b32 v137, v73\n"
  "v_mov_b32 v138, v74\n"
  "v_mov_b32 v139, v75\n"
  "v_mov_b32 v140, v88\n"
  "v_mov_b32 v141, v89\n"
  "v_mov_b32 v142, v90\n"
  "v_mov_b32 v143, v91\n"
  "v_mov_b32 v144, v104\n"
  "v_mov_b32 v145, v105\n"
  "v_mov_b32 v146, v106\n"
  "v_mov_b32 v147, v107\n"
  "s_branch LOWN2_%=_r\n"
  "LOWN3_%=:\n"
  "v_mov_b32 v128, v44\n"
  "v_mov_b32 v129, v45\n"
  "v_mov_b32 v130, v46\n"
  "v_mov_b32 v131, v47\n"
  "v_mov_b32 v132, v60\n"
  "v_mov_b32 v133, v61\n"
  "v_mov_b32 v134, v62\n"
  "v_mov_b32 v135, v63\n"
  "v_mov_b32 v136, v76\n"
  "v_mov_b32 v137, v77\n"
  "v_mov_b32 v138, v78\n"
  "v_mov_b32 v139, v79\n"
  "v_mov_b32 v140, v92\n"
  "v_mov_b32 v141, v93\n"
  "v_mov_b32 v142, v94\n"
  "v_mov_b32 v143, v95\n"
  "v_mov_b32 v144, v108\n"
  "v_mov_b32 v145, v109\n"
  "v_mov_b32 v146, v110\n"
  "v_mov_b32 v147, v111\n"
  "s_branch LOWN3_%=_r\n"
  "LRW_%=:\n"
  "s_sub_u32 s84, s84, 1\n"
  "s_cbranch_scc1 LRD_%=\n"
  "s_sleep 1\n"
  "s_branch LRP_%=\n"
  "LEND_%=:\n"
  ".Lkc1:\n"
  : "+{v0}"(v[0]), "+{v1}"(v[1]), "+{v2}"(v[2]), "+{v3}"(v[3]), "+{v4}"(v[4]), "+{v5}"(v[5]), "+{v6}"(v[6]), "+{v7}"(v[7]), "+{v8}"(v[8]), "+{v9}"(v[9]), "+{v13}"(v[13]), "+{v14}"(v[14]), "+{v16}"(v[16]), "+{v17}"(v[17]), "+{v18}"(v[18]), "+{v19}"(v[19]), "+{v20}"(v[20]), "+{v21}"(v[21]), "+{v22}"(v[22]), "+{v23}"(v[23]), "+{v24}"(v[24]), "+{v25}"(v[25]), "+{v26}"(v[26]), "+{v27}"(v[27]), "+{v28}"(v[28]), "+{v29}"(v[29]), "+{a224}"(a224), "+{a225}"(a225), "+{a226}"(a226), "+{a227}"(a227), "+{a228}"(a228), "+{a229}"(a229), "+{a230}"(a230), "+{a231}"(a231), "+{a232}"(a232), "+{s62}"(s62), "+{s63}"(s63), "+{s64}"(s64), "+{s65}"(s65), "+{s66}"(s66), "+{s67}"(s67), "+{s68}"(s68), "+{s69}"(s69), "+{s70}"(s70), "+{s71}"(s71), "+{s73}"(s73), "+{s74}"(s74), "+{s76}"(s76), "+{s77}"(s77), "+{s88}"(s88), "+{s[20:23]}"(d20), "+{s[24:27]}"(d24), "+{s[28:31]}"(d28), "+{s[32:35]}"(d32), "+{s[36:39]}"(d36), "+{s[40:43]}"(d40), "+{s[44:47]}"(d44), "+{s[48:51]}"(d48), "+{s[52:55]}"(d52), "+{s[56:59]}"(d56), "+{s[60:61]}"(p60), "+{a[0:31]}"(O0), "+{a[32:63]}"(O1)
  :
  : "v10","v11","v12","v15","v30","v31","v32","v33","v34","v35","v36","v37","v38","v39","v40","v41","v42","v43","v44","v45","v46","v47","v48","v49","v50","v51","v52","v53","v54","v55","v56","v57","v58","v59","v60","v61","v62","v63","v64","v65","v66","v67","v68","v69","v70","v71","v72","v73","v74","v75","v76","v77","v78","v79","v80","v81","v82","v83","v84","v85","v86","v87","v88","v89","v90","v91","v92","v93","v94","v95","v96","v97","v98","v99","v100","v101","v102","v103","v104","v105","v106","v107","v108","v109","v110","v111","v112","v113","v114","v115","v116","v117","v118","v119","v120","v121","v122","v123","v124","v125","v126","v127","v128","v129","v130","v131","v132","v133","v134","v135","v136","v137","v138","v139","v140","v141","v142","v143","v144","v145","v146","v147","v148","v149","v150","v151","v152","v153","v154","v155","v156","v157","v158","v159","v160","v161","v162","v163","v164","v165","v166","v167","v168","v169","v170","v171","v172","v173","v174","v175","v176","v177","v178","v179","v180","v181","v182","v183","v184","v185","v186","v187","v188","v189","v190","v191","v192","v193","v194","v195","v196","v197","v198","v199","v200","v201","v202","v203","v204","v205","v206","v207","v208","v209","v210","v211","v212","v213","v214","v215","v216","v217","v218","v219","v220","v221","v222","v223","v224","v225","v226","v227","v228","v229","v230","v231","v232","v233","v234","v235","v236","v237","v238","v239","v240","v241","v242","v243","v244","v245","v246","v247","v248","v249","v250","v251","v252","v253","v254","v255","a64","a65","a66","a67","a68","a69","a70","a71","a72","a73","a74","a75","a76","a77","a78","a79","a80","a81","a82","a83","a84","a85","a86","a87","a88","a89","a90","a91","a92","a93","a94","a95","a96","a97","a98","a99","a100","a101","a102","a103","a104","a105","a106","a107","a108","a109","a110","a111","a112","a113","a114","a115","a116","a117","a118","a119","a120","a121","a122","a123","a124","a125","a126","a127","a128","a129","a130","a131","a132","a133","a134","a135","a136","a137","a138","a139","a140","a141","a142","a143","a144","a145","a146","a147","a148","a149","a150","a151","a152","a153","a154","a155","a156","a157","a158","a159","a160","a161","a162","a163","a164","a165","a166","a167","a168","a169","a170","a171","a172","a173","a174","a175","a176","a177","a178","a179","a180","a181","a182","a183","a184","a185","a186","a187","a188","a189","a190","a191","a192","a193","a194","a195","a196","a197","a198","a199","a200","a201","a202","a203","a204","a205","a206","a207","a208","a209","a210","a211","a212","a213","a214","a215","a216","a217","a218","a219","a220","a221","a222","a223","a233","a234","a235","a236","a237","a238","a239","a240","a241","a242","a243","a244","a245","a246","a247","a248","a249","a250","a251","a252","a253","a254","a255","s80","s81","s83","s84","s85","s86","s87","vcc","scc","memory");
  }
}

extern "C" void kernel_launch(void* const* d_in, const int* in_sizes, int n_in, void* d_out, int out_size, void* d_ws, size_t ws_size, hipStream_t stream) {
  (void)hipFuncSetAttribute((const void*)fused, hipFuncAttributeMaxDynamicSharedMemorySize, 163840);
  fused<<<256, 256, 163840, stream>>>((const char*)d_in[0], (const char*)d_in[1], (const char*)d_in[2], (const char*)d_in[3], (const char*)d_in[4], (const char*)d_in[5],
                                      (const char*)d_in[6], (const char*)d_in[7], (const char*)d_in[8], (char*)d_ws, (char*)d_out);
}
```
